```python
import torch

def _softmax(x, axis=-1):
    return torch.softmax(x, dim=axis)

def _gelu(x):
    return torch.nn.functional.gelu(x, approximate='tanh')

def _silu(x):
    return torch.nn.functional.silu(x)

def _sigmoid(x):
    return torch.sigmoid(x)

def _layernorm(x, axis=-1, eps=1e-05):
    m = x.mean(dim=axis, keepdim=True)
    v = x.var(dim=axis, keepdim=True, unbiased=False)
    return (x - m) / torch.sqrt(v + eps)

def _scatter_add(tgt, idx, src, axis=0):
    return tgt.clone().index_add_(axis, idx, src)

def _segment_sum(src, seg, n):
    out = torch.zeros((n,) + src.shape[1:], dtype=src.dtype)
    return out.index_add_(0, seg, src)

def _pad(x, pw):
    flat = tuple((v for pair in reversed(pw) for v in pair))
    return torch.nn.functional.pad(x, flat)
kernel_name = 'adaLN_cond__compressed_kv_cla__dense_activation_mish__prompt'

def setup_inputs():

    _w = lambda a: (lambda t: t.to(torch.bfloat16) if t.is_floating_point() else t)(torch.as_tensor(a))

    def _A_setup():
        B, H, Sq, Sk, D, M = (1, 32, 2048, 2048, 128, 512)
        Dc = 4 * D
        return {'x': _w((lambda *s: torch.randn(*s, dtype=torch.bfloat16) * 0.05)(B, Sq, M)), 'c_kv': _w((lambda *s: torch.randn(*s, dtype=torch.bfloat16) * 0.4)(B, Sk, Dc)), 'w_q': _w((lambda *s: torch.randn(*s, dtype=torch.bfloat16) * 3.2)(M, H, D) * M ** (-0.5)), 'w_uk': _w((lambda *s: torch.randn(*s, dtype=torch.bfloat16) * 3.2)(Dc, H, D) * Dc ** (-0.5)), 'w_uv': _w((lambda *s: torch.randn(*s, dtype=torch.bfloat16) * 0.4)(Dc, H, D) * Dc ** (-0.5)), 'w_o': _w((lambda *s: torch.randn(*s, dtype=torch.bfloat16) * 0.4)(H, D, M) * (H * D) ** (-0.5))}
    _A_in = _A_setup()

    def _B_setup():
        B, S, D, F = (1, 2048, 512, 1792)
        return {'x': _w((lambda *s: torch.randn(*s, dtype=torch.bfloat16) * 0.05)(B, S, D)), 'w1': _w((lambda *s: torch.randn(*s, dtype=torch.bfloat16) * 0.8)(D, F) * D ** (-0.5)), 'b1': _w((lambda *s: torch.randn(*s, dtype=torch.bfloat16) * 0.2)(F)), 'w2': _w((lambda *s: torch.randn(*s, dtype=torch.bfloat16) * 0.1)(F, D) * F ** (-0.5))}
    _B_in = _B_setup()
    return {'A_x': _A_in['x'], 'A_c_kv': _A_in['c_kv'], 'A_w_q': _A_in['w_q'], 'A_w_uk': _A_in['w_uk'], 'A_w_uv': _A_in['w_uv'], 'A_w_o': _A_in['w_o'], 'B_x': _B_in['x'], 'B_w1': _B_in['w1'], 'B_b1': _B_in['b1'], 'B_w2': _B_in['w2'], 'c': _w((lambda *s: torch.randn(*s, dtype=torch.bfloat16) * 0.1)(1, 512) * 8.0), 'w_mod': _w((lambda *s: torch.randn(*s, dtype=torch.bfloat16) * 0.1)(512, 6 * 512) * 512 ** (-0.5) * 8.0)}

def reference(A_x, A_c_kv, A_w_q, A_w_uk, A_w_uv, A_w_o, B_x, B_w1, B_b1, B_w2, c, w_mod):

    def _A_ref(x, c_kv, w_q, w_uk, w_uv, w_o):
        B, Sq, M = (x.shape[0], x.shape[1], x.shape[2])
        Sk = c_kv.shape[1]
        H, D = (w_q.shape[1], w_q.shape[2])
        q = torch.einsum('bqm,mhd->bhqd', x, w_q)
        k = torch.einsum('bkc,chd->bhkd', c_kv, w_uk)
        v = torch.einsum('bkc,chd->bhkd', c_kv, w_uv)
        scores = torch.einsum('bhqd,bhkd->bhqk', q, k) * D ** (-0.5)
        off = Sk - Sq
        qi = torch.arange(Sq)[:, None]
        kj = torch.arange(Sk)[None, :]
        cmask = kj <= qi + off
        scores = torch.where(cmask[None, None], scores, -1000000000.0)
        p = _softmax(scores, axis=-1)
        ctx = torch.einsum('bhqk,bhkd->bhqd', p, v)
        return torch.einsum('bhqd,hdm->bqm', ctx, w_o)

    def _B_ref(x, w1, b1, w2):
        h = torch.einsum('bsd,df->bsf', x, w1) + b1
        sp = torch.log1p(torch.exp(-torch.abs(h))) + torch.where(h > 0, h, torch.zeros_like(h))
        act = h * torch.tanh(sp)
        return torch.einsum('bsf,fd->bsd', act, w2)
    x = A_x
    mod = (_silu(c) @ w_mod).reshape(c.shape[0], 6, 1, -1)
    sa, ca, ga, sb, cb, gb = (mod[:, 0].clone(), mod[:, 1].clone(), mod[:, 2].clone(), mod[:, 3].clone(), mod[:, 4].clone(), mod[:, 5].clone())
    h = _layernorm(x, axis=-1) * (1 + ca) + sa
    x = x + ga * _A_ref(h, A_c_kv, A_w_q, A_w_uk, A_w_uv, A_w_o)
    h = _layernorm(x, axis=-1) * (1 + cb) + sb
    x = x + gb * _B_ref(h, B_w1, B_b1, B_w2)
    return x
```

```cpp
#include <hip/hip_runtime.h>
#include <hip/hip_bf16.h>
#include <stdint.h>
typedef unsigned short u16;
typedef short bf16x8 __attribute__((ext_vector_type(8)));
typedef float f32x4 __attribute__((ext_vector_type(4)));
typedef float f32x16 __attribute__((ext_vector_type(16)));
__device__ __forceinline__ float bf2f(u16 x){ return __uint_as_float(((unsigned)x)<<16); }
__device__ __forceinline__ u16 f2bf(float f){ __hip_bfloat16 b = __float2bfloat16(f); return *(u16*)&b; }
__device__ __forceinline__ float rbf(float f){ return bf2f(f2bf(f)); }

__global__ void k_mod(const u16* c, const u16* w, float* mod){
  __shared__ float sc[512];
  for(int i=threadIdx.x;i<512;i+=blockDim.x){ float v=bf2f(c[i]); sc[i]=rbf(v/(1.f+__expf(-v))); }
  __syncthreads();
  int n = blockIdx.x*blockDim.x+threadIdx.x;
  float a=0; for(int k=0;k<512;++k) a+=sc[k]*bf2f(w[k*3072+n]);
  mod[n]=rbf(a);
}
__global__ void k_ln(const u16* x, const float* sh, const float* scl, u16* h){
  int row=blockIdx.x, t=threadIdx.x;
  float v[8]; float s=0;
  for(int j=0;j<8;++j){ v[j]=bf2f(x[row*512+t*8+j]); s+=v[j]; }
  for(int o=32;o;o>>=1) s+=__shfl_xor(s,o);
  float m=s/512.f; float q=0;
  for(int j=0;j<8;++j){ float d=v[j]-m; q+=d*d; }
  for(int o=32;o;o>>=1) q+=__shfl_xor(q,o);
  float r=rsqrtf(q/512.f+1e-5f);
  for(int j=0;j<8;++j){ int cidx=t*8+j; h[row*512+cidx]=f2bf((v[j]-m)*r*(1.f+scl[cidx])+sh[cidx]); }
}
template<int MODE> __global__ __launch_bounds__(256) void k_gemm(const u16* A,const u16* B,u16* C,int M,int N,int K,const u16* bias,const u16* res,const float* gate){
  __shared__ u16 sA[64][40]; __shared__ u16 sB[64][40];
  int bm=blockIdx.y*64,bn=blockIdx.x*64; int t=threadIdx.x,w=t>>6,l=t&63;
  int wm=(w>>1)*32, wn=(w&1)*32;
  f32x16 acc={};
  for(int k0=0;k0<K;k0+=32){
    { int r=t>>2, cc=(t&3)*8; *(bf16x8*)&sA[r][cc]=*(const bf16x8*)&A[(size_t)(bm+r)*K+k0+cc]; }
    { int kk=t>>3, nn=(t&7)*8; bf16x8 v=*(const bf16x8*)&B[(size_t)(k0+kk)*N+bn+nn]; for(int j=0;j<8;++j) sB[nn+j][kk]=v[j]; }
    __syncthreads();
    for(int ks=0;ks<2;++ks){
      bf16x8 a=*(bf16x8*)&sA[wm+(l&31)][ks*16+(l>>5)*8];
      bf16x8 b=*(bf16x8*)&sB[wn+(l&31)][ks*16+(l>>5)*8];
      acc=__builtin_amdgcn_mfma_f32_32x32x16_bf16(a,b,acc,0,0,0);
    }
    __syncthreads();
  }
  for(int r=0;r<16;++r){ int row=bm+wm+(r&3)+8*(r>>2)+4*(l>>5), col=bn+wn+(l&31); float v=acc[r];
    if(MODE==1){ v=v+bf2f(bias[col]); float e=__expf(-fabsf(v)); float sp=log1pf(e)+fmaxf(v,0.f); v=v*tanhf(sp);}
    if(MODE==2){ v=bf2f(res[(size_t)row*N+col])+gate[col]*v; }
    C[(size_t)row*N+col]=f2bf(v);
  }
}
__global__ __launch_bounds__(64) void k_attn(const u16* Q,const u16* Kp,const u16* V,u16* O){
  int h=blockIdx.y, q0=blockIdx.x*32, l=threadIdx.x, r=l&31, hi=l>>5;
  __shared__ u16 sV[32][136];
  bf16x8 qf[8]; for(int d=0;d<8;++d) qf[d]=*(const bf16x8*)&Q[(size_t)(q0+r)*4096+h*128+d*16+hi*8];
  f32x16 o[4]={}; float m=-1e30f, lsum=0; const float sc=0.08838834764831845f*1.4426950408889634f;
  for(int k0=0;k0<=q0;k0+=32){
    f32x16 s={};
    for(int d=0;d<8;++d){ bf16x8 kf=*(const bf16x8*)&Kp[(size_t)(k0+r)*4096+h*128+d*16+hi*8]; s=__builtin_amdgcn_mfma_f32_32x32x16_bf16(kf,qf[d],s,0,0,0);}
    for(int i=0;i<8;++i){ int idx=l+i*64; int kk=idx>>4, c=(idx&15)*8; *(bf16x8*)&sV[kk][c]=*(const bf16x8*)&V[(size_t)(k0+kk)*4096+h*128+c]; }
    __syncthreads();
    float mx=-1e30f;
    for(int j=0;j<16;++j){ int key=k0+(j&3)+8*(j>>2)+4*hi; float v=s[j]*sc; if(key>q0+r) v=-__builtin_inff(); s[j]=v; mx=fmaxf(mx,v);}
    mx=fmaxf(mx,__shfl_xor(mx,32)); float mn=fmaxf(m,mx); float al=exp2f(m-mn); m=mn; float ps=0;
    for(int j=0;j<16;++j){ s[j]=exp2f(s[j]-mn); ps+=s[j]; }
    ps+=__shfl_xor(ps,32); lsum=lsum*al+ps;
    for(int b=0;b<4;++b) for(int j=0;j<16;++j) o[b][j]*=al;
    for(int st=0;st<2;++st){ bf16x8 p; for(int j=0;j<8;++j) p[j]=f2bf(s[8*st+j]);
      for(int b=0;b<4;++b){ bf16x8 vf; for(int j=0;j<8;++j) vf[j]=sV[16*st+8*(j>>2)+4*hi+(j&3)][b*32+r]; o[b]=__builtin_amdgcn_mfma_f32_32x32x16_bf16(vf,p,o[b],0,0,0);} }
    __syncthreads();
  }
  float il=1.f/lsum;
  for(int b=0;b<4;++b) for(int j=0;j<16;++j){ int d=b*32+(j&3)+8*(j>>2)+4*hi; O[(size_t)(q0+r)*4096+h*128+d]=f2bf(o[b][j]*il);}
}
extern "C" void kernel_launch(void* const* d_in, const int* in_sizes, int n_in, void* d_out, int out_size, void* d_ws, size_t ws_size, hipStream_t stream){
  const u16* x=(const u16*)d_in[0]; const u16* ckv=(const u16*)d_in[1]; const u16* wq=(const u16*)d_in[2]; const u16* wk=(const u16*)d_in[3]; const u16* wv=(const u16*)d_in[4]; const u16* wo=(const u16*)d_in[5];
  const u16* w1=(const u16*)d_in[7]; const u16* b1=(const u16*)d_in[8]; const u16* w2=(const u16*)d_in[9]; const u16* c=(const u16*)d_in[10]; const u16* wm=(const u16*)d_in[11];
  char* ws=(char*)d_ws; float* mod=(float*)ws; u16* h=(u16*)(ws+(1<<20)); u16* q=(u16*)(ws+(4<<20)); u16* k=(u16*)(ws+(20<<20)); u16* v=(u16*)(ws+(36<<20)); u16* ctx=(u16*)(ws+(52<<20)); u16* x1=(u16*)(ws+(68<<20)); u16* h2=(u16*)(ws+(72<<20)); u16* act=(u16*)(ws+(76<<20));
  k_mod<<<48,64,0,stream>>>(c,wm,mod);
  k_ln<<<2048,64,0,stream>>>(x,mod,mod+512,h);
  k_gemm<0><<<dim3(64,32),256,0,stream>>>(h,wq,q,2048,4096,512,0,0,0);
  k_gemm<0><<<dim3(64,32),256,0,stream>>>(ckv,wk,k,2048,4096,512,0,0,0);
  k_gemm<0><<<dim3(64,32),256,0,stream>>>(ckv,wv,v,2048,4096,512,0,0,0);
  k_attn<<<dim3(64,32),64,0,stream>>>(q,k,v,ctx);
  k_gemm<2><<<dim3(8,32),256,0,stream>>>(ctx,wo,x1,2048,512,4096,0,x,mod+1024);
  k_ln<<<2048,64,0,stream>>>(x1,mod+1536,mod+2048,h2);
  k_gemm<1><<<dim3(28,32),256,0,stream>>>(h2,w1,act,2048,1792,512,b1,0,0);
  k_gemm<2><<<dim3(8,32),256,0,stream>>>(act,w2,(u16*)d_out,2048,512,1792,0,x1,mod+2560);
}
```

```cpp
#include <hip/hip_runtime.h>
__global__ __launch_bounds__(256, 1) void k_block(const void* x, const void* ckv, const void* wq, const void* wk, const void* wv, const void* wo,
    const void* w1, const void* b1, const void* w2, const void* c, const void* wmod, void* out, void* ws) {
  __shared__ __attribute__((aligned(16))) char lds[163840];
  unsigned wid = __builtin_amdgcn_readfirstlane(threadIdx.x >> 6);
  asm volatile(
"s_load_dwordx8 s[84:91], %0, 8\n"
"s_load_dwordx4 s[92:95], %0, 72\n"
"s_load_dwordx2 s[56:57], %0, 96\n"
"s_load_dwordx2 s[96:97], %0, 0\n"
"s_load_dwordx4 s[68:71], %0, 40\n"
"s_load_dwordx2 s[8:9], %0, 56\n"
"s_load_dwordx2 s[78:79], %0, 64\n"
"s_load_dwordx2 s[80:81], %0, 88\n"
"s_xor_b32 s10, %1, 0\n"
"s_mov_b32 s11, %2\n"
"v_mbcnt_lo_u32_b32 v244, -1, 0\n"
"v_mbcnt_hi_u32_b32 v244, -1, v244\n"
"s_getpc_b64 s[98:99]\n"
"Lpc0_%=:\n"
"s_lshr_b32 s16, s10, 3\n"
"s_mul_i32 s16, s16, 2560\n"
"s_mul_i32 s15, s11, 640\n"
"s_add_u32 s16, s16, s15\n"
"v_lshl_add_u32 v241, v244, 7, s16\n"
"v_min_u32 v241, Lslow_%=-Lpc0_%=, v241\n"
"s_mov_b64 exec, 31\n"
"global_load_dword v243, v241, s[98:99]\n"
"s_mov_b64 exec, -1\n"
"s_and_b32 s12, s10, 7\n"
"s_lshr_b32 s15, s10, 6\n"
"s_lshl_b32 s13, s12, 2\n"
"s_add_u32 s13, s13, s15\n"
"s_xor_b32 s13, s13, 2\n"
"s_bfe_u32 s14, s10, 0x30003\n"
"s_lshl_b32 s58, s11, 11\n"
"s_mul_i32 s59, s11, 24576\n"
"s_add_u32 s59, s59, 65536\n"
"v_lshrrev_b32 v245, 3, v244\n"
"v_and_b32 v246, 7, v244\n"
"v_lshrrev_b32 v247, 4, v244\n"
"v_xor_b32 v247, v246, v247\n"
"v_lshlrev_b32 v247, 4, v247\n"
"v_lshl_add_u32 v247, v245, 10, v247\n"
"s_lshl_b32 s16, s14, 18\n"
"s_lshl_b32 s15, s11, 16\n"
"s_add_u32 s16, s16, s15\n"
"v_add_u32 v128, s16, v247\n"
"v_xor_b32 v129, 64, v128\n"
"v_add_u32 v129, 7168, v129\n"
"v_add_u32 v130, 14336, v128\n"
"v_add_u32 v131, 14336, v129\n"
"v_and_b32 v247, 2, v245\n"
"v_xor_b32 v247, v246, v247\n"
"v_lshlrev_b32 v247, 4, v247\n"
"v_lshl_add_u32 v248, v245, 13, v247\n"
"s_lshl_b32 s16, s11, 17\n"
"s_lshl_b32 s15, s13, 8\n"
"s_add_u32 s16, s16, s15\n"
"v_add_u32 v234, s16, v248\n"
"v_xor_b32 v235, 64, v234\n"
"v_add_u32 v235, 64512, v235\n"
"s_and_b32 s17, s10, 3\n"
"s_lshr_b32 s18, s10, 2\n"
"s_mul_i32 s16, s17, 786432\n"
"s_mul_i32 s15, s11, 98304\n"
"s_add_u32 s16, s16, s15\n"
"s_lshl_b32 s15, s18, 7\n"
"s_add_u32 s16, s16, s15\n"
"s_cmp_lt_u32 s10, 192\n"
"s_cselect_b32 s16, s16, 0x10000000\n"
"v_mul_u32_u24 v248, 6144, v245\n"
"v_add3_u32 v134, v248, v247, s16\n"
"v_xor_b32 v135, 64, v134\n"
"v_add_u32 v135, 48128, v135\n"
"s_lshl_b32 s17, s17, 8\n"
"v_lshl_add_u32 v248, v244, 2, s17\n"
"s_lshl_b32 s16, s10, 14\n"
"s_lshl_b32 s15, s11, 12\n"
"s_add_u32 s16, s16, s15\n"
"v_and_b32 v241, 31, v244\n"
"v_lshl_add_u32 v241, v241, 7, s16\n"
"v_lshrrev_b32 v242, 1, v241\n"
"s_waitcnt lgkmcnt(0)\n"
"s_mov_b32 s20, s84\n"
"s_and_b32 s21, s85, 0xffff\n"
"s_mov_b32 s22, 2097152\n"
"s_mov_b32 s23, 0x20000\n"
"s_mov_b32 s24, s88\n"
"s_and_b32 s25, s89, 0xffff\n"
"s_mov_b32 s26, 4194304\n"
"s_mov_b32 s27, 0x20000\n"
"s_load_dwordx2 s[6:7], s[56:57], 0\n"
"global_load_dword v140, v248, s[92:93]\n"
"s_mov_b64 exec, 0x55555555\n"
"buffer_load_dword v243, v242, s[20:23], 0 offen\n"
"s_mov_b64 exec, -1\n"
"s_lshr_b32 s16, s10, 3\n"
"s_lshl_b32 s16, s16, 17\n"
"s_lshl_b32 s15, s11, 15\n"
"s_add_u32 s16, s16, s15\n"
"s_lshl_b32 s15, s12, 10\n"
"s_add_u32 s16, s16, s15\n"
"v_bfe_u32 v241, v244, 3, 2\n"
"v_lshlrev_b32 v241, 13, v241\n"
"v_lshl_add_u32 v241, v246, 7, v241\n"
"v_add_u32 v241, s16, v241\n"
"s_add_u32 m0, s59, 0\n"
"s_mov_b32 s61, 0\n"
"buffer_load_dwordx4 v128, s[20:23], s61 offen offset:0 lds\n"
"buffer_load_dwordx4 v129, s[20:23], s61 offen offset:1024 lds\n"
"buffer_load_dwordx4 v130, s[20:23], s61 offen offset:2048 lds\n"
"buffer_load_dwordx4 v131, s[20:23], s61 offen offset:3072 lds\n"
"s_add_u32 m0, s59, 4096\n"
"s_mov_b32 s61, 32768\n"
"buffer_load_dwordx4 v128, s[20:23], s61 offen offset:0 lds\n"
"buffer_load_dwordx4 v129, s[20:23], s61 offen offset:1024 lds\n"
"buffer_load_dwordx4 v130, s[20:23], s61 offen offset:2048 lds\n"
"buffer_load_dwordx4 v131, s[20:23], s61 offen offset:3072 lds\n"
"s_add_u32 m0, s58, 0\n"
"s_mov_b32 s60, 0\n"
"buffer_load_dwordx4 v234, s[24:27], s60 offen offset:0 lds\n"
"buffer_load_dwordx4 v235, s[24:27], s60 offen offset:1024 lds\n"
"s_add_u32 m0, s58, 8192\n"
"s_mov_b32 s60, 128\n"
"buffer_load_dwordx4 v234, s[24:27], s60 offen offset:0 lds\n"
"buffer_load_dwordx4 v235, s[24:27], s60 offen offset:1024 lds\n"
"s_mov_b32 s48, s94\n"
"s_and_b32 s49, s95, 0xffff\n"
"s_mov_b32 s50, 3145728\n"
"s_mov_b32 s51, 0x20000\n"
"s_mov_b32 s52, s96\n"
"s_and_b32 s53, s97, 0xffff\n"
"s_mov_b32 s54, 2097152\n"
"s_mov_b32 s55, 0x20000\n"
"s_add_u32 m0, s58, 40960\n"
"s_mov_b32 s60, 0\n"
"buffer_load_dwordx4 v134, s[48:51], s60 offen offset:0 lds\n"
"buffer_load_dwordx4 v135, s[48:51], s60 offen offset:1024 lds\n"
"s_add_u32 m0, s58, 49152\n"
"s_mov_b32 s60, 393216\n"
"buffer_load_dwordx4 v134, s[48:51], s60 offen offset:0 lds\n"
"buffer_load_dwordx4 v135, s[48:51], s60 offen offset:1024 lds\n"
"v_lshlrev_b32 v232, 4, v244\n"
"v_and_b32 v245, 15, v244\n"
"v_lshrrev_b32 v246, 4, v244\n"
"v_and_b32 v248, 3, v244\n"
"v_lshrrev_b32 v247, 1, v245\n"
"v_xor_b32 v247, v247, v246\n"
"v_lshlrev_b32 v247, 4, v247\n"
"v_lshl_add_u32 v247, v245, 7, v247\n"
"v_add_u32 v132, s59, v247\n"
"v_xor_b32 v133, 64, v132\n"
"v_lshrrev_b32 v245, 2, v245\n"
"v_lshrrev_b32 v236, 2, v244\n"
"v_lshlrev_b32 v236, 8, v236\n"
"v_lshl_add_u32 v236, v248, 4, v236\n"
"v_lshlrev_b32 v236, 12, v245\n"
"v_lshl_add_u32 v236, v246, 6, v236\n"
"v_lshl_add_u32 v236, v248, 4, v236\n"
"v_lshl_add_u32 v247, v246, 3, v245\n"
"v_lshlrev_b32 v247, 7, v247\n"
"v_lshl_add_u32 v247, v248, 3, v247\n"
"v_bfe_u32 v245, v244, 3, 2\n"
"v_lshl_add_u32 v224, v245, 5, v247\n"
"v_xor_b32 v225, 32, v224\n"
"v_xor_b32 v226, 64, v224\n"
"v_xor_b32 v227, 96, v224\n"
"s_lshl_b32 s16, s11, 8\n"
"s_add_u32 s16, s16, 57344\n"
"v_lshl_add_u32 v240, v244, 2, s16\n"
"v_lshl_add_u32 v139, v246, 4, s16\n"
"s_lshl_b32 s17, s11, 5\n"
"v_xor_b32 v134, s17, v224\n"
"s_and_b32 s17, s11, 1\n"
"s_lshr_b32 s18, s11, 1\n"
"s_lshl_b32 s16, s10, 13\n"
"s_lshl_b32 s15, s18, 12\n"
"s_add_u32 s16, s16, s15\n"
"s_lshl_b32 s15, s17, 9\n"
"s_add_u32 s16, s16, s15\n"
"v_and_b32 v245, 0xf0, v232\n"
"v_lshl_add_u32 v245, v246, 10, v245\n"
"v_add_u32 v245, s16, v245\n"
"v_xor_b32 v246, 512, v245\n"
"s_mov_b32 s28, s90\n"
"s_and_b32 s29, s91, 0xffff\n"
"s_mov_b32 s30, 4194304\n"
"s_mov_b32 s31, 0x20000\n"
"s_mov_b32 s36, s86\n"
"s_and_b32 s37, s87, 0xffff\n"
"s_mov_b32 s38, 4194304\n"
"s_mov_b32 s39, 0x20000\n"
"s_mov_b32 s40, s56\n"
"s_and_b32 s41, s57, 0xffff\n"
"s_mov_b32 s42, 67108864\n"
"s_mov_b32 s43, 0x20000\n"
"v_mov_b32 v254, v245\n"
"v_mov_b32 v255, v246\n"
"s_add_u32 m0, s58, 16384\n"
"s_mov_b32 s60, 524288\n"
"buffer_load_dwordx4 v234, s[24:27], s60 offen offset:0 lds\n"
"buffer_load_dwordx4 v235, s[24:27], s60 offen offset:1024 lds\n"
"s_add_u32 m0, s58, 24576\n"
"s_mov_b32 s60, 524416\n"
"buffer_load_dwordx4 v234, s[24:27], s60 offen offset:0 lds\n"
"buffer_load_dwordx4 v235, s[24:27], s60 offen offset:1024 lds\n"
"s_add_u32 m0, s59, 8192\n"
"s_mov_b32 s61, 128\n"
"buffer_load_dwordx4 v128, s[20:23], s61 offen offset:0 lds\n"
"buffer_load_dwordx4 v129, s[20:23], s61 offen offset:1024 lds\n"
"buffer_load_dwordx4 v130, s[20:23], s61 offen offset:2048 lds\n"
"buffer_load_dwordx4 v131, s[20:23], s61 offen offset:3072 lds\n"
"s_waitcnt vmcnt(25)\n"
"v_lshlrev_b32 v141, 16, v140\n"
"v_and_b32 v140, 0xffff0000, v140\n"
"v_mul_f32 v142, 0xbfb8aa3b, v140\n"
"v_mul_f32 v143, 0xbfb8aa3b, v141\n"
"v_exp_f32 v142, v142\n"
"v_exp_f32 v143, v143\n"
"v_add_f32 v142, 1.0, v142\n"
"v_add_f32 v143, 1.0, v143\n"
"v_rcp_f32 v142, v142\n"
"v_rcp_f32 v143, v143\n"
"v_mul_f32 v140, v140, v142\n"
"v_mul_f32 v141, v141, v143\n"
"v_cvt_pk_bf16_f32 v140, v141, v140\n"
"ds_write_b32 v240, v140 offset:1024\n"
"ds_read_b128 a[208:211], v139 offset:1024\n"
"ds_read_b128 a[212:215], v139 offset:1088\n"
"ds_read_b128 a[216:219], v139 offset:1152\n"
"ds_read_b128 a[220:223], v139 offset:1216\n"
"s_waitcnt lgkmcnt(0)\n"
"s_add_u32 s6, s6, 1\n"
"s_mov_b32 s19, 0\n"
"s_waitcnt vmcnt(8)\n"
"s_barrier\n"
"ds_read_b64_tr_b16 a[192:193], v134 offset:40960\n"
"ds_read_b64_tr_b16 a[194:195], v134 offset:41472\n"
"ds_read_b64_tr_b16 a[196:197], v134 offset:45056\n"
"ds_read_b64_tr_b16 a[198:199], v134 offset:45568\n"
"ds_read_b64_tr_b16 a[200:201], v134 offset:49152\n"
"ds_read_b64_tr_b16 a[202:203], v134 offset:49664\n"
"ds_read_b64_tr_b16 a[204:205], v134 offset:53248\n"
"ds_read_b64_tr_b16 a[206:207], v134 offset:53760\n"
"ds_read_b128 a[0:3], v132 offset:0\n"
"ds_read_b128 a[16:19], v133 offset:0\n"
"ds_read_b128 a[4:7], v132 offset:2048\n"
"ds_read_b128 a[20:23], v133 offset:2048\n"
"ds_read_b128 a[8:11], v132 offset:4096\n"
"ds_read_b128 a[24:27], v133 offset:4096\n"
"ds_read_b128 a[12:15], v132 offset:6144\n"
"ds_read_b128 a[28:31], v133 offset:6144\n"
"ds_read_b64_tr_b16 v[192:193], v224 offset:0\n"
"ds_read_b64_tr_b16 v[194:195], v224 offset:512\n"
"ds_read_b64_tr_b16 v[196:197], v224 offset:4096\n"
"ds_read_b64_tr_b16 v[198:199], v224 offset:4608\n"
"s_waitcnt lgkmcnt(12)\n"
"v_mfma_f32_16x16x32_bf16 v[140:143], a[192:195], a[208:211], 0\n"
"v_mfma_f32_16x16x32_bf16 v[140:143], a[196:199], a[212:215], v[140:143]\n"
"v_mfma_f32_16x16x32_bf16 v[140:143], a[200:203], a[216:219], v[140:143]\n"
"v_mfma_f32_16x16x32_bf16 v[140:143], a[204:207], a[220:223], v[140:143]\n"
"s_barrier\n"
"ds_read_b64_tr_b16 v[200:201], v225 offset:0\n"
"ds_read_b64_tr_b16 v[202:203], v225 offset:512\n"
"ds_read_b64_tr_b16 v[204:205], v225 offset:4096\n"
"ds_read_b64_tr_b16 v[206:207], v225 offset:4608\n"
"s_waitcnt lgkmcnt(4)\n"
"v_mfma_f32_16x16x32_bf16 v[0:3], v[192:195], a[0:3], 0\n"
"s_add_u32 m0, s59, 12288\n"
"s_mov_b32 s61, 32896\n"
"v_mfma_f32_16x16x32_bf16 v[0:3], v[196:199], a[16:19], v[0:3]\n"
"buffer_load_dwordx4 v128, s[20:23], s61 offen offset:0 lds\n"
"v_mfma_f32_16x16x32_bf16 v[4:7], v[192:195], a[4:7], 0\n"
"ds_read_b64_tr_b16 v[208:209], v226 offset:0\n"
"s_and_b32 s17, s10, 3\n"
"v_mfma_f32_16x16x32_bf16 v[4:7], v[196:199], a[20:23], v[4:7]\n"
"ds_read_b64_tr_b16 v[210:211], v226 offset:512\n"
"s_lshr_b32 s18, s10, 2\n"
"v_mfma_f32_16x16x32_bf16 v[8:11], v[192:195], a[8:11], 0\n"
"ds_read_b64_tr_b16 v[212:213], v226 offset:4096\n"
"s_mul_i32 s48, s17, 12288\n"
"v_mfma_f32_16x16x32_bf16 v[8:11], v[196:199], a[24:27], v[8:11]\n"
"ds_read_b64_tr_b16 v[214:215], v226 offset:4608\n"
"s_lshl_b32 s15, s18, 8\n"
"v_mfma_f32_16x16x32_bf16 v[12:15], v[192:195], a[12:15], 0\n"
"buffer_load_dwordx4 v129, s[20:23], s61 offen offset:1024 lds\n"
"v_mfma_f32_16x16x32_bf16 v[12:15], v[196:199], a[28:31], v[12:15]\n"
"s_add_u32 s48, s48, s15\n"
"s_waitcnt lgkmcnt(4)\n"
"v_mfma_f32_16x16x32_bf16 v[16:19], v[200:203], a[0:3], 0\n"
"s_lshl_b32 s15, s11, 6\n"
"s_add_u32 s48, s48, s15\n"
"s_add_u32 s48, s48, 4096\n"
"v_mfma_f32_16x16x32_bf16 v[16:19], v[204:207], a[16:19], v[16:19]\n"
"s_cmp_lt_u32 s10, 192\n"
"s_cselect_b32 s48, s48, 0x10000000\n"
"v_and_b32 v245, 15, v244\n"
"v_mfma_f32_16x16x32_bf16 v[20:23], v[200:203], a[4:7], 0\n"
"v_lshlrev_b32 v245, 27, v245\n"
"v_and_or_b32 v245, v244, 48, v245\n"
"buffer_store_dwordx4 v[140:143], v245, s[40:43], s48 offen sc1\n"
"v_mfma_f32_16x16x32_bf16 v[20:23], v[204:207], a[20:23], v[20:23]\n"
"buffer_load_dwordx4 v130, s[20:23], s61 offen offset:2048 lds\n"
"v_mfma_f32_16x16x32_bf16 v[24:27], v[200:203], a[8:11], 0\n"
"ds_read_b64_tr_b16 v[216:217], v227 offset:0\n"
"s_lshl_b32 s16, s11, 28\n"
"v_mfma_f32_16x16x32_bf16 v[24:27], v[204:207], a[24:27], v[24:27]\n"
"ds_read_b64_tr_b16 v[218:219], v227 offset:512\n"
"v_mul_u32_u24 v237, 4194312, v244\n"
"v_mfma_f32_16x16x32_bf16 v[28:31], v[200:203], a[12:15], 0\n"
"ds_read_b64_tr_b16 v[220:221], v227 offset:4096\n"
"v_and_b32 v237, 0x0c000078, v237\n"
"ds_read_b64_tr_b16 v[222:223], v227 offset:4608\n"
"v_mfma_f32_16x16x32_bf16 v[28:31], v[204:207], a[28:31], v[28:31]\n"
"ds_read_b64_tr_b16 v[192:193], v224 offset:8192\n"
"buffer_load_dwordx4 v131, s[20:23], s61 offen offset:3072 lds\n"
"s_waitcnt lgkmcnt(5)\n"
"v_mfma_f32_16x16x32_bf16 v[32:35], v[208:211], a[0:3], 0\n"
"buffer_load_dwordx4 v[144:147], v254, s[52:55], 0 offen offset:0\n"
"v_mfma_f32_16x16x32_bf16 v[32:35], v[212:215], a[16:19], v[32:35]\n"
"ds_read_b64_tr_b16 v[194:195], v224 offset:8704\n"
"v_add_u32 v237, s16, v237\n"
"v_mfma_f32_16x16x32_bf16 v[36:39], v[208:211], a[4:7], 0\n"
"ds_read_b64_tr_b16 v[196:197], v224 offset:12288\n"
"s_lshl_b32 s67, s10, 8\n"
"v_mfma_f32_16x16x32_bf16 v[36:39], v[212:215], a[20:23], v[36:39]\n"
"ds_read_b64_tr_b16 v[198:199], v224 offset:12800\n"
"s_add_u32 s67, s67, 65536\n"
"v_mfma_f32_16x16x32_bf16 v[40:43], v[208:211], a[8:11], 0\n"
"buffer_load_dwordx4 v[148:151], v254, s[52:55], 0 offen offset:256\n"
"v_mfma_f32_16x16x32_bf16 v[40:43], v[212:215], a[24:27], v[40:43]\n"
"ds_read_b64_tr_b16 v[200:201], v225 offset:8192\n"
"s_lshl_b32 s16, s13, 20\n"
"v_mfma_f32_16x16x32_bf16 v[44:47], v[208:211], a[12:15], 0\n"
"ds_read_b64_tr_b16 v[202:203], v225 offset:8704\n"
"s_add_u32 s16, s16, 67108864\n"
"v_mfma_f32_16x16x32_bf16 v[44:47], v[212:215], a[28:31], v[44:47]\n"
"ds_read_b64_tr_b16 v[204:205], v225 offset:12288\n"
"s_waitcnt lgkmcnt(7)\n"
"v_mfma_f32_16x16x32_bf16 v[48:51], v[216:219], a[0:3], 0\n"
"buffer_load_dwordx4 v[152:155], v255, s[52:55], 0 offen offset:0\n"
"v_mfma_f32_16x16x32_bf16 v[48:51], v[220:223], a[16:19], v[48:51]\n"
"ds_read_b64_tr_b16 v[206:207], v225 offset:12800\n"
"s_lshl_b32 s66, s14, 17\n"
"v_mfma_f32_16x16x32_bf16 v[52:55], v[216:219], a[4:7], 0\n"
"ds_read_b64_tr_b16 v[208:209], v226 offset:8192\n"
"s_lshl_b32 s15, s11, 15\n"
"v_mfma_f32_16x16x32_bf16 v[52:55], v[220:223], a[20:23], v[52:55]\n"
"ds_read_b64_tr_b16 v[210:211], v226 offset:8704\n"
"s_add_u32 s66, s66, s15\n"
"v_mfma_f32_16x16x32_bf16 v[56:59], v[216:219], a[8:11], 0\n"
"buffer_load_dwordx4 v[156:159], v255, s[52:55], 0 offen offset:256\n"
"v_mfma_f32_16x16x32_bf16 v[56:59], v[220:223], a[24:27], v[56:59]\n"
"ds_read_b64_tr_b16 v[212:213], v226 offset:12288\n"
"s_lshl_b32 s17, s14, 12\n"
"v_mfma_f32_16x16x32_bf16 v[60:63], v[216:219], a[12:15], 0\n"
"s_add_u32 m0, s58, 32768\n"
"s_mov_b32 s60, 1048576\n"
"v_mfma_f32_16x16x32_bf16 v[60:63], v[220:223], a[28:31], v[60:63]\n"
"ds_read_b64_tr_b16 v[214:215], v226 offset:12800\n"
"s_waitcnt lgkmcnt(8)\n"
"v_mfma_f32_16x16x32_bf16 v[64:67], v[192:195], a[0:3], 0\n"
"buffer_load_dwordx4 v234, s[24:27], s60 offen offset:0 lds\n"
"v_mfma_f32_16x16x32_bf16 v[64:67], v[196:199], a[16:19], v[64:67]\n"
"ds_read_b64_tr_b16 v[216:217], v227 offset:8192\n"
"s_lshl_b32 s15, s11, 10\n"
"v_mfma_f32_16x16x32_bf16 v[68:71], v[192:195], a[4:7], 0\n"
"ds_read_b64_tr_b16 v[218:219], v227 offset:8704\n"
"s_add_u32 s17, s17, s15\n"
"v_mfma_f32_16x16x32_bf16 v[68:71], v[196:199], a[20:23], v[68:71]\n"
"ds_read_b64_tr_b16 v[220:221], v227 offset:12288\n"
"s_add_u32 s17, s17, 16384\n"
"v_mfma_f32_16x16x32_bf16 v[72:75], v[192:195], a[8:11], 0\n"
"buffer_load_dwordx4 v235, s[24:27], s60 offen offset:1024 lds\n"
"v_mfma_f32_16x16x32_bf16 v[72:75], v[196:199], a[24:27], v[72:75]\n"
"ds_read_b64_tr_b16 v[222:223], v227 offset:12800\n"
"v_and_b32 v135, 3, v244\n"
"v_mfma_f32_16x16x32_bf16 v[76:79], v[192:195], a[12:15], 0\n"
"s_waitcnt vmcnt(15)\n"
"s_barrier\n"
"v_mfma_f32_16x16x32_bf16 v[76:79], v[196:199], a[28:31], v[76:79]\n"
"s_add_u32 m0, s58, 40960\n"
"s_waitcnt lgkmcnt(8)\n"
"v_mfma_f32_16x16x32_bf16 v[80:83], v[200:203], a[0:3], 0\n"
"s_mov_b32 s60, 1048704\n"
"ds_read_b64_tr_b16 v[192:193], v224 offset:16384\n"
"v_mfma_f32_16x16x32_bf16 v[80:83], v[204:207], a[16:19], v[80:83]\n"
"buffer_load_dwordx4 v234, s[24:27], s60 offen offset:0 lds\n"
"v_mfma_f32_16x16x32_bf16 v[84:87], v[200:203], a[4:7], 0\n"
"ds_read_b64_tr_b16 v[194:195], v224 offset:16896\n"
"v_lshl_add_u32 v135, v135, 8, s17\n"
"v_mfma_f32_16x16x32_bf16 v[84:87], v[204:207], a[20:23], v[84:87]\n"
"ds_read_b64_tr_b16 v[196:197], v224 offset:20480\n"
"s_mov_b32 s49, 65536\n"
"v_mfma_f32_16x16x32_bf16 v[88:91], v[200:203], a[8:11], 0\n"
"buffer_load_dwordx4 v235, s[24:27], s60 offen offset:1024 lds\n"
"v_mfma_f32_16x16x32_bf16 v[88:91], v[204:207], a[24:27], v[88:91]\n"
"s_waitcnt vmcnt(8)\n"
"ds_read_b128 a[32:35], v132 offset:8192\n"
"s_lshl_b32 s48, s11, 10\n"
"v_mfma_f32_16x16x32_bf16 v[92:95], v[200:203], a[12:15], 0\n"
"s_add_u32 m0, s59, 16384\n"
"s_mov_b32 s61, 256\n"
"v_mfma_f32_16x16x32_bf16 v[92:95], v[204:207], a[28:31], v[92:95]\n"
"ds_read_b128 a[48:51], v133 offset:8192\n"
"s_waitcnt lgkmcnt(9)\n"
"v_mfma_f32_16x16x32_bf16 v[96:99], v[208:211], a[0:3], 0\n"
"buffer_load_dwordx4 v128, s[20:23], s61 offen offset:0 lds\n"
"v_mfma_f32_16x16x32_bf16 v[96:99], v[212:215], a[16:19], v[96:99]\n"
"ds_read_b128 a[36:39], v132 offset:10240\n"
"s_add_u32 s48, s48, 4096\n"
"v_mfma_f32_16x16x32_bf16 v[100:103], v[208:211], a[4:7], 0\n"
"ds_read_b128 a[52:55], v133 offset:10240\n"
"s_lshl_b32 s15, s11, 8\n"
"v_mfma_f32_16x16x32_bf16 v[100:103], v[212:215], a[20:23], v[100:103]\n"
"ds_read_b128 a[40:43], v132 offset:12288\n"
"s_and_b32 s17, s11, 1\n"
"v_mfma_f32_16x16x32_bf16 v[104:107], v[208:211], a[8:11], 0\n"
"ds_read_b128 a[56:59], v133 offset:12288\n"
"v_add_u32 v228, 0x10000, v224\n"
"v_mfma_f32_16x16x32_bf16 v[104:107], v[212:215], a[24:27], v[104:107]\n"
"ds_read_b128 a[44:47], v132 offset:14336\n"
"v_add_u32 v229, 0x10000, v225\n"
"v_mfma_f32_16x16x32_bf16 v[108:111], v[208:211], a[12:15], 0\n"
"ds_read_b128 a[60:63], v133 offset:14336\n"
"v_add_u32 v230, 0x10000, v226\n"
"ds_read_b64_tr_b16 v[198:199], v224 offset:20992\n"
"v_mfma_f32_16x16x32_bf16 v[108:111], v[212:215], a[28:31], v[108:111]\n"
"ds_read_b64_tr_b16 v[200:201], v225 offset:16384\n"
"buffer_load_dwordx4 v129, s[20:23], s61 offen offset:1024 lds\n"
"s_waitcnt lgkmcnt(13)\n"
"v_mfma_f32_16x16x32_bf16 v[112:115], v[216:219], a[0:3], 0\n"
"buffer_load_dwordx4 v130, s[20:23], s61 offen offset:2048 lds\n"
"v_mfma_f32_16x16x32_bf16 v[112:115], v[220:223], a[16:19], v[112:115]\n"
"ds_read_b64_tr_b16 v[202:203], v225 offset:16896\n"
"v_add_u32 v231, 0x10000, v227\n"
"v_mfma_f32_16x16x32_bf16 v[116:119], v[216:219], a[4:7], 0\n"
"ds_read_b64_tr_b16 v[204:205], v225 offset:20480\n"
"v_and_b32 v238, 7, v244\n"
"v_mfma_f32_16x16x32_bf16 v[116:119], v[220:223], a[20:23], v[116:119]\n"
"ds_read_b64_tr_b16 v[206:207], v225 offset:20992\n"
"v_lshlrev_b32 v238, 8, v238\n"
"v_mfma_f32_16x16x32_bf16 v[120:123], v[216:219], a[8:11], 0\n"
"buffer_load_dwordx4 v131, s[20:23], s61 offen offset:3072 lds\n"
"v_mfma_f32_16x16x32_bf16 v[120:123], v[220:223], a[24:27], v[120:123]\n"
"ds_read_b64_tr_b16 v[208:209], v226 offset:16384\n"
"v_and_b32 v239, 1792, v238\n"
"v_mfma_f32_16x16x32_bf16 v[124:127], v[216:219], a[12:15], 0\n"
"s_add_u32 m0, s59, 20480\n"
"s_mov_b32 s61, 33024\n"
"v_mfma_f32_16x16x32_bf16 v[124:127], v[220:223], a[28:31], v[124:127]\n"
"ds_read_b64_tr_b16 v[210:211], v226 offset:16896\n"
"s_waitcnt lgkmcnt(6)\n"
"v_mfma_f32_16x16x32_bf16 v[0:3], v[192:195], a[32:35], v[0:3]\n"
"buffer_load_dwordx4 v128, s[20:23], s61 offen offset:0 lds\n"
"v_mfma_f32_16x16x32_bf16 v[0:3], v[196:199], a[48:51], v[0:3]\n"
"ds_read_b64_tr_b16 v[212:213], v226 offset:20480\n"
"s_mov_b32 s46, 1048576\n"
"v_mfma_f32_16x16x32_bf16 v[4:7], v[192:195], a[36:39], v[4:7]\n"
"ds_read_b64_tr_b16 v[214:215], v226 offset:20992\n"
"s_mov_b32 s47, 0x20000\n"
"v_mfma_f32_16x16x32_bf16 v[4:7], v[196:199], a[52:55], v[4:7]\n"
"ds_read_b64_tr_b16 v[216:217], v227 offset:16384\n"
"v_lshrrev_b32 v252, 5, v244\n"
"v_mfma_f32_16x16x32_bf16 v[8:11], v[192:195], a[40:43], v[8:11]\n"
"buffer_load_dwordx4 v129, s[20:23], s61 offen offset:1024 lds\n"
"v_mfma_f32_16x16x32_bf16 v[8:11], v[196:199], a[56:59], v[8:11]\n"
"ds_read_b64_tr_b16 v[218:219], v227 offset:16896\n"
"v_lshlrev_b32 v252, 26, v252\n"
"v_mfma_f32_16x16x32_bf16 v[12:15], v[192:195], a[44:47], v[12:15]\n"
"ds_read_b64_tr_b16 v[220:221], v227 offset:20480\n"
"v_lshl_add_u32 v252, v244, 7, v252\n"
"v_mfma_f32_16x16x32_bf16 v[12:15], v[196:199], a[60:63], v[12:15]\n"
"ds_read_b64_tr_b16 v[222:223], v227 offset:20992\n"
"s_waitcnt lgkmcnt(8)\n"
"v_mfma_f32_16x16x32_bf16 v[16:19], v[200:203], a[32:35], v[16:19]\n"
"buffer_load_dwordx4 v130, s[20:23], s61 offen offset:2048 lds\n"
"v_mfma_f32_16x16x32_bf16 v[16:19], v[204:207], a[48:51], v[16:19]\n"
"ds_read_b64_tr_b16 v[192:193], v224 offset:24576\n"
"v_add_u32 v253, 50, v244\n"
"v_mfma_f32_16x16x32_bf16 v[20:23], v[200:203], a[36:39], v[20:23]\n"
"ds_read_b64_tr_b16 v[194:195], v224 offset:25088\n"
"v_lshrrev_b32 v253, 6, v253\n"
"v_mfma_f32_16x16x32_bf16 v[20:23], v[204:207], a[52:55], v[20:23]\n"
"ds_read_b64_tr_b16 v[196:197], v224 offset:28672\n"
"v_lshlrev_b32 v253, 26, v253\n"
"v_mfma_f32_16x16x32_bf16 v[24:27], v[200:203], a[40:43], v[24:27]\n"
"buffer_load_dwordx4 v131, s[20:23], s61 offen offset:3072 lds\n"
"v_mfma_f32_16x16x32_bf16 v[24:27], v[204:207], a[56:59], v[24:27]\n"
"ds_read_b64_tr_b16 v[198:199], v224 offset:29184\n"
"v_lshl_add_u32 v253, v244, 7, v253\n"
"v_mfma_f32_16x16x32_bf16 v[28:31], v[200:203], a[44:47], v[28:31]\n"
"s_add_u32 m0, s58, 49152\n"
"s_mov_b32 s60, 1572864\n"
"v_mfma_f32_16x16x32_bf16 v[28:31], v[204:207], a[60:63], v[28:31]\n"
"ds_read_b64_tr_b16 v[200:201], v225 offset:24576\n"
"s_waitcnt lgkmcnt(9)\n"
"v_mfma_f32_16x16x32_bf16 v[32:35], v[208:211], a[32:35], v[32:35]\n"
"buffer_load_dwordx4 v234, s[24:27], s60 offen offset:0 lds\n"
"v_mfma_f32_16x16x32_bf16 v[32:35], v[212:215], a[48:51], v[32:35]\n"
"ds_read_b64_tr_b16 v[202:203], v225 offset:25088\n"
"s_waitcnt vmcnt(16)\n"
"v_lshlrev_b32 v160, 16, v144\n"
"v_mfma_f32_16x16x32_bf16 v[36:39], v[208:211], a[36:39], v[36:39]\n"
"ds_read_b64_tr_b16 v[204:205], v225 offset:28672\n"
"v_and_b32 v161, 0xffff0000, v144\n"
"v_mfma_f32_16x16x32_bf16 v[36:39], v[212:215], a[52:55], v[36:39]\n"
"ds_read_b64_tr_b16 v[206:207], v225 offset:29184\n"
"v_lshlrev_b32 v162, 16, v145\n"
"v_mfma_f32_16x16x32_bf16 v[40:43], v[208:211], a[40:43], v[40:43]\n"
"buffer_load_dwordx4 v235, s[24:27], s60 offen offset:1024 lds\n"
"v_mfma_f32_16x16x32_bf16 v[40:43], v[212:215], a[56:59], v[40:43]\n"
"s_add_u32 s44, s56, s16\n"
"s_addc_u32 s45, s41, 0\n"
"v_mfma_f32_16x16x32_bf16 v[44:47], v[208:211], a[44:47], v[44:47]\n"
"s_add_u32 m0, s58, 0\n"
"s_mov_b32 s60, 1572992\n"
"v_mfma_f32_16x16x32_bf16 v[44:47], v[212:215], a[60:63], v[44:47]\n"
"ds_read_b64_tr_b16 v[208:209], v226 offset:24576\n"
"s_waitcnt lgkmcnt(9)\n"
"v_mfma_f32_16x16x32_bf16 v[48:51], v[216:219], a[32:35], v[48:51]\n"
"buffer_load_dwordx4 v234, s[24:27], s60 offen offset:0 lds\n"
"v_mfma_f32_16x16x32_bf16 v[48:51], v[220:223], a[48:51], v[48:51]\n"
"ds_read_b64_tr_b16 v[210:211], v226 offset:25088\n"
"s_lshl_b32 s16, s11, 12\n"
"v_mfma_f32_16x16x32_bf16 v[52:55], v[216:219], a[36:39], v[52:55]\n"
"ds_read_b64_tr_b16 v[212:213], v226 offset:28672\n"
"v_lshl_add_u32 v134, v244, 8, s16\n"
"v_mfma_f32_16x16x32_bf16 v[52:55], v[220:223], a[52:55], v[52:55]\n"
"ds_read_b64_tr_b16 v[214:215], v226 offset:29184\n"
"v_and_b32 v163, 0xffff0000, v145\n"
"v_mfma_f32_16x16x32_bf16 v[56:59], v[216:219], a[40:43], v[56:59]\n"
"buffer_load_dwordx4 v235, s[24:27], s60 offen offset:1024 lds\n"
"v_mfma_f32_16x16x32_bf16 v[56:59], v[220:223], a[56:59], v[56:59]\n"
"v_cmp_gt_u32 vcc, 16, v244\n"
"s_nop 1\n"
"v_cndmask_b32 v134, v135, v134, vcc\n"
"v_mfma_f32_16x16x32_bf16 v[60:63], v[216:219], a[44:47], v[60:63]\n"
"s_add_u32 m0, s59, 0\n"
"s_mov_b32 s61, 384\n"
"v_mfma_f32_16x16x32_bf16 v[60:63], v[220:223], a[60:63], v[60:63]\n"
"ds_read_b64_tr_b16 v[216:217], v227 offset:24576\n"
"s_waitcnt lgkmcnt(9)\n"
"v_mfma_f32_16x16x32_bf16 v[64:67], v[192:195], a[32:35], v[64:67]\n"
"buffer_load_dwordx4 v128, s[20:23], s61 offen offset:0 lds\n"
"v_mfma_f32_16x16x32_bf16 v[64:67], v[196:199], a[48:51], v[64:67]\n"
"ds_read_b64_tr_b16 v[218:219], v227 offset:25088\n"
"v_lshlrev_b32 v164, 16, v146\n"
"v_mfma_f32_16x16x32_bf16 v[68:71], v[192:195], a[36:39], v[68:71]\n"
"ds_read_b64_tr_b16 v[220:221], v227 offset:28672\n"
"v_and_b32 v165, 0xffff0000, v146\n"
"v_mfma_f32_16x16x32_bf16 v[68:71], v[196:199], a[52:55], v[68:71]\n"
"ds_read_b64_tr_b16 v[222:223], v227 offset:29184\n"
"v_lshlrev_b32 v166, 16, v147\n"
"v_mfma_f32_16x16x32_bf16 v[72:75], v[192:195], a[40:43], v[72:75]\n"
"buffer_load_dwordx4 v129, s[20:23], s61 offen offset:1024 lds\n"
"v_mfma_f32_16x16x32_bf16 v[72:75], v[196:199], a[56:59], v[72:75]\n"
"v_and_b32 v167, 0xffff0000, v147\n"
"s_waitcnt vmcnt(20)\n"
"v_lshlrev_b32 v168, 16, v148\n"
"v_mfma_f32_16x16x32_bf16 v[76:79], v[192:195], a[44:47], v[76:79]\n"
"s_waitcnt vmcnt(14)\n"
"s_barrier\n"
"v_mfma_f32_16x16x32_bf16 v[76:79], v[196:199], a[60:63], v[76:79]\n"
"ds_read_b64_tr_b16 v[192:193], v224 offset:32768\n"
"s_waitcnt lgkmcnt(9)\n"
"v_mfma_f32_16x16x32_bf16 v[80:83], v[200:203], a[32:35], v[80:83]\n"
"buffer_load_dwordx4 v130, s[20:23], s61 offen offset:2048 lds\n"
"v_mfma_f32_16x16x32_bf16 v[80:83], v[204:207], a[48:51], v[80:83]\n"
"ds_read_b64_tr_b16 v[194:195], v224 offset:33280\n"
"v_and_b32 v169, 0xffff0000, v148\n"
"v_mfma_f32_16x16x32_bf16 v[84:87], v[200:203], a[36:39], v[84:87]\n"
"ds_read_b64_tr_b16 v[196:197], v224 offset:36864\n"
"v_lshlrev_b32 v170, 16, v149\n"
"v_mfma_f32_16x16x32_bf16 v[84:87], v[204:207], a[52:55], v[84:87]\n"
"ds_read_b64_tr_b16 v[198:199], v224 offset:37376\n"
"v_and_b32 v171, 0xffff0000, v149\n"
"v_mfma_f32_16x16x32_bf16 v[88:91], v[200:203], a[40:43], v[88:91]\n"
"buffer_load_dwordx4 v131, s[20:23], s61 offen offset:3072 lds\n"
"v_mfma_f32_16x16x32_bf16 v[88:91], v[204:207], a[56:59], v[88:91]\n"
"s_waitcnt vmcnt(8)\n"
"ds_read_b128 a[64:67], v132 offset:16384\n"
"v_lshlrev_b32 v172, 16, v150\n"
"v_mfma_f32_16x16x32_bf16 v[92:95], v[200:203], a[44:47], v[92:95]\n"
"s_add_u32 m0, s59, 4096\n"
"s_mov_b32 s61, 33152\n"
"v_mfma_f32_16x16x32_bf16 v[92:95], v[204:207], a[60:63], v[92:95]\n"
"v_mov_b32 v250, s6\n"
"s_waitcnt lgkmcnt(9)\n"
"v_mfma_f32_16x16x32_bf16 v[96:99], v[208:211], a[32:35], v[96:99]\n"
"buffer_load_dwordx4 v128, s[20:23], s61 offen offset:0 lds\n"
"v_mfma_f32_16x16x32_bf16 v[96:99], v[212:215], a[48:51], v[96:99]\n"
"v_mov_b32 v251, s7\n"
"ds_read_b128 a[80:83], v133 offset:16384\n"
"v_mfma_f32_16x16x32_bf16 v[100:103], v[208:211], a[36:39], v[100:103]\n"
"ds_read_b128 a[68:71], v132 offset:18432\n"
"v_and_b32 v173, 0xffff0000, v150\n"
"v_mfma_f32_16x16x32_bf16 v[100:103], v[212:215], a[52:55], v[100:103]\n"
"ds_read_b128 a[84:87], v133 offset:18432\n"
"v_lshlrev_b32 v174, 16, v151\n"
"v_mfma_f32_16x16x32_bf16 v[104:107], v[208:211], a[40:43], v[104:107]\n"
"ds_read_b128 a[72:75], v132 offset:20480\n"
"v_and_b32 v175, 0xffff0000, v151\n"
"v_mfma_f32_16x16x32_bf16 v[104:107], v[212:215], a[56:59], v[104:107]\n"
"buffer_store_dwordx2 v[250:251], v237, s[40:43], s67 offen sc1\n"
"ds_read_b128 a[88:91], v133 offset:20480\n"
"v_mfma_f32_16x16x32_bf16 v[108:111], v[208:211], a[44:47], v[108:111]\n"
"ds_read_b128 a[76:79], v132 offset:22528\n"
"v_lshlrev_b32 v176, 16, v152\n"
"ds_read_b128 a[92:95], v133 offset:22528\n"
"v_mfma_f32_16x16x32_bf16 v[108:111], v[212:215], a[60:63], v[108:111]\n"
"ds_read_b64_tr_b16 v[200:201], v225 offset:32768\n"
"buffer_load_dwordx4 v129, s[20:23], s61 offen offset:1024 lds\n"
"s_waitcnt lgkmcnt(13)\n"
"v_mfma_f32_16x16x32_bf16 v[112:115], v[216:219], a[32:35], v[112:115]\n"
"buffer_load_dwordx4 v130, s[20:23], s61 offen offset:2048 lds\n"
"v_mfma_f32_16x16x32_bf16 v[112:115], v[220:223], a[48:51], v[112:115]\n"
"ds_read_b64_tr_b16 v[202:203], v225 offset:33280\n"
"v_and_b32 v177, 0xffff0000, v152\n"
"v_mfma_f32_16x16x32_bf16 v[116:119], v[216:219], a[36:39], v[116:119]\n"
"ds_read_b64_tr_b16 v[204:205], v225 offset:36864\n"
"v_lshlrev_b32 v178, 16, v153\n"
"v_mfma_f32_16x16x32_bf16 v[116:119], v[220:223], a[52:55], v[116:119]\n"
"ds_read_b64_tr_b16 v[206:207], v225 offset:37376\n"
"v_and_b32 v179, 0xffff0000, v153\n"
"v_mfma_f32_16x16x32_bf16 v[120:123], v[216:219], a[40:43], v[120:123]\n"
"buffer_load_dwordx4 v131, s[20:23], s61 offen offset:3072 lds\n"
"v_mfma_f32_16x16x32_bf16 v[120:123], v[220:223], a[56:59], v[120:123]\n"
"ds_read_b64_tr_b16 v[208:209], v226 offset:32768\n"
"v_lshlrev_b32 v180, 16, v154\n"
"v_mfma_f32_16x16x32_bf16 v[124:127], v[216:219], a[44:47], v[124:127]\n"
"ds_read_b64_tr_b16 v[210:211], v226 offset:33280\n"
"v_and_b32 v181, 0xffff0000, v154\n"
"v_mfma_f32_16x16x32_bf16 v[124:127], v[220:223], a[60:63], v[124:127]\n"
"ds_read_b64_tr_b16 v[212:213], v226 offset:36864\n"
"s_waitcnt lgkmcnt(7)\n"
"v_mfma_f32_16x16x32_bf16 v[0:3], v[192:195], a[64:67], v[0:3]\n"
"ds_read_b64_tr_b16 v[214:215], v226 offset:37376\n"
"v_lshlrev_b32 v182, 16, v155\n"
"v_mfma_f32_16x16x32_bf16 v[0:3], v[196:199], a[80:83], v[0:3]\n"
"s_add_u32 m0, s58, 8192\n"
"s_mov_b32 s60, 2097152\n"
"v_mfma_f32_16x16x32_bf16 v[4:7], v[192:195], a[68:71], v[4:7]\n"
"buffer_load_dwordx4 v234, s[24:27], s60 offen offset:0 lds\n"
"v_mfma_f32_16x16x32_bf16 v[4:7], v[196:199], a[84:87], v[4:7]\n"
"ds_read_b64_tr_b16 v[216:217], v227 offset:32768\n"
"v_and_b32 v183, 0xffff0000, v155\n"
"v_mfma_f32_16x16x32_bf16 v[8:11], v[192:195], a[72:75], v[8:11]\n"
"ds_read_b64_tr_b16 v[218:219], v227 offset:33280\n"
"v_lshlrev_b32 v184, 16, v156\n"
"v_mfma_f32_16x16x32_bf16 v[8:11], v[196:199], a[88:91], v[8:11]\n"
"ds_read_b64_tr_b16 v[220:221], v227 offset:36864\n"
"v_and_b32 v185, 0xffff0000, v156\n"
"v_mfma_f32_16x16x32_bf16 v[12:15], v[192:195], a[76:79], v[12:15]\n"
"buffer_load_dwordx4 v235, s[24:27], s60 offen offset:1024 lds\n"
"v_mfma_f32_16x16x32_bf16 v[12:15], v[196:199], a[92:95], v[12:15]\n"
"ds_read_b64_tr_b16 v[222:223], v227 offset:37376\n"
"s_waitcnt lgkmcnt(8)\n"
"v_mfma_f32_16x16x32_bf16 v[16:19], v[200:203], a[64:67], v[16:19]\n"
"ds_read_b64_tr_b16 v[192:193], v224 offset:40960\n"
"v_lshlrev_b32 v186, 16, v157\n"
"v_mfma_f32_16x16x32_bf16 v[16:19], v[204:207], a[80:83], v[16:19]\n"
"ds_read_b64_tr_b16 v[194:195], v224 offset:41472\n"
"v_and_b32 v187, 0xffff0000, v157\n"
"v_mfma_f32_16x16x32_bf16 v[20:23], v[200:203], a[68:71], v[20:23]\n"
"ds_read_b64_tr_b16 v[196:197], v224 offset:45056\n"
"v_lshlrev_b32 v188, 16, v158\n"
"v_mfma_f32_16x16x32_bf16 v[20:23], v[204:207], a[84:87], v[20:23]\n"
"s_add_u32 m0, s58, 16384\n"
"s_mov_b32 s60, 2097280\n"
"v_mfma_f32_16x16x32_bf16 v[24:27], v[200:203], a[72:75], v[24:27]\n"
"buffer_load_dwordx4 v234, s[24:27], s60 offen offset:0 lds\n"
"v_mfma_f32_16x16x32_bf16 v[24:27], v[204:207], a[88:91], v[24:27]\n"
"ds_read_b64_tr_b16 v[198:199], v224 offset:45568\n"
"v_and_b32 v189, 0xffff0000, v158\n"
"v_mfma_f32_16x16x32_bf16 v[28:31], v[200:203], a[76:79], v[28:31]\n"
"ds_read_b64_tr_b16 v[200:201], v225 offset:40960\n"
"v_lshlrev_b32 v190, 16, v159\n"
"v_mfma_f32_16x16x32_bf16 v[28:31], v[204:207], a[92:95], v[28:31]\n"
"ds_read_b64_tr_b16 v[202:203], v225 offset:41472\n"
"s_waitcnt lgkmcnt(10)\n"
"v_mfma_f32_16x16x32_bf16 v[32:35], v[208:211], a[64:67], v[32:35]\n"
"buffer_load_dwordx4 v235, s[24:27], s60 offen offset:1024 lds\n"
"v_mfma_f32_16x16x32_bf16 v[32:35], v[212:215], a[80:83], v[32:35]\n"
"ds_read_b64_tr_b16 v[204:205], v225 offset:45056\n"
"v_and_b32 v191, 0xffff0000, v159\n"
"v_mfma_f32_16x16x32_bf16 v[36:39], v[208:211], a[68:71], v[36:39]\n"
"ds_read_b64_tr_b16 v[206:207], v225 offset:45568\n"
"s_lshl_b32 s16, s11, 10\n"
"v_mfma_f32_16x16x32_bf16 v[36:39], v[212:215], a[84:87], v[36:39]\n"
"s_cmp_ge_u32 s11, 2\n"
"s_cselect_b32 s50, 1.0, 0\n"
"v_mfma_f32_16x16x32_bf16 v[40:43], v[208:211], a[72:75], v[40:43]\n"
"s_add_u32 s16, s16, 61440\n"
"v_add_u32 v139, s16, v232\n"
"v_mfma_f32_16x16x32_bf16 v[40:43], v[212:215], a[88:91], v[40:43]\n"
"s_add_u32 m0, s59, 8192\n"
"s_mov_b32 s61, 512\n"
"v_mfma_f32_16x16x32_bf16 v[44:47], v[208:211], a[76:79], v[44:47]\n"
"buffer_load_dwordx4 v128, s[20:23], s61 offen offset:0 lds\n"
"v_mfma_f32_16x16x32_bf16 v[44:47], v[212:215], a[92:95], v[44:47]\n"
"ds_read_b64_tr_b16 v[208:209], v226 offset:40960\n"
"s_waitcnt lgkmcnt(9)\n"
"v_mfma_f32_16x16x32_bf16 v[48:51], v[216:219], a[64:67], v[48:51]\n"
"ds_read_b64_tr_b16 v[210:211], v226 offset:41472\n"
"v_lshrrev_b32 v135, 2, v232\n"
"v_mfma_f32_16x16x32_bf16 v[48:51], v[220:223], a[80:83], v[48:51]\n"
"ds_read_b64_tr_b16 v[212:213], v226 offset:45056\n"
"v_add_f32 v246, v160, v161\n"
"v_mfma_f32_16x16x32_bf16 v[52:55], v[216:219], a[68:71], v[52:55]\n"
"buffer_load_dwordx4 v129, s[20:23], s61 offen offset:1024 lds\n"
"v_mfma_f32_16x16x32_bf16 v[52:55], v[220:223], a[84:87], v[52:55]\n"
"ds_read_b64_tr_b16 v[214:215], v226 offset:45568\n"
"v_add_f32 v246, v246, v162\n"
"v_mfma_f32_16x16x32_bf16 v[56:59], v[216:219], a[72:75], v[56:59]\n"
"v_add_f32 v246, v246, v163\n"
"v_add_f32 v246, v246, v164\n"
"v_mfma_f32_16x16x32_bf16 v[56:59], v[220:223], a[88:91], v[56:59]\n"
"v_add_f32 v246, v246, v165\n"
"v_add_f32 v246, v246, v166\n"
"v_mfma_f32_16x16x32_bf16 v[60:63], v[216:219], a[76:79], v[60:63]\n"
"ds_read_b64_tr_b16 v[216:217], v227 offset:40960\n"
"v_add_f32 v246, v246, v167\n"
"v_mfma_f32_16x16x32_bf16 v[60:63], v[220:223], a[92:95], v[60:63]\n"
"ds_read_b64_tr_b16 v[218:219], v227 offset:41472\n"
"s_waitcnt lgkmcnt(10)\n"
"v_mfma_f32_16x16x32_bf16 v[64:67], v[192:195], a[64:67], v[64:67]\n"
"buffer_load_dwordx4 v130, s[20:23], s61 offen offset:2048 lds\n"
"v_mfma_f32_16x16x32_bf16 v[64:67], v[196:199], a[80:83], v[64:67]\n"
"ds_read_b64_tr_b16 v[220:221], v227 offset:45056\n"
"v_add_f32 v246, v246, v168\n"
"v_mfma_f32_16x16x32_bf16 v[68:71], v[192:195], a[68:71], v[68:71]\n"
"ds_read_b64_tr_b16 v[222:223], v227 offset:45568\n"
"v_add_f32 v246, v246, v169\n"
"v_mfma_f32_16x16x32_bf16 v[68:71], v[196:199], a[84:87], v[68:71]\n"
"v_add_f32 v246, v246, v170\n"
"v_add_f32 v246, v246, v171\n"
"v_mfma_f32_16x16x32_bf16 v[72:75], v[192:195], a[72:75], v[72:75]\n"
"buffer_load_dwordx4 v131, s[20:23], s61 offen offset:3072 lds\n"
"v_mfma_f32_16x16x32_bf16 v[72:75], v[196:199], a[88:91], v[72:75]\n"
"s_add_u32 m0, s59, 12288\n"
"s_mov_b32 s61, 33280\n"
"v_mfma_f32_16x16x32_bf16 v[76:79], v[192:195], a[76:79], v[76:79]\n"
"s_waitcnt vmcnt(17)\n"
"s_barrier\n"
"v_mfma_f32_16x16x32_bf16 v[76:79], v[196:199], a[92:95], v[76:79]\n"
"ds_read_b64_tr_b16 v[192:193], v224 offset:49152\n"
"s_waitcnt lgkmcnt(9)\n"
"v_mfma_f32_16x16x32_bf16 v[80:83], v[200:203], a[64:67], v[80:83]\n"
"ds_read_b64_tr_b16 v[194:195], v224 offset:49664\n"
"v_add_f32 v246, v246, v172\n"
"v_mfma_f32_16x16x32_bf16 v[80:83], v[204:207], a[80:83], v[80:83]\n"
"buffer_load_dwordx4 v128, s[20:23], s61 offen offset:0 lds\n"
"v_mfma_f32_16x16x32_bf16 v[84:87], v[200:203], a[68:71], v[84:87]\n"
"ds_read_b64_tr_b16 v[196:197], v224 offset:53248\n"
"v_add_f32 v246, v246, v173\n"
"v_mfma_f32_16x16x32_bf16 v[84:87], v[204:207], a[84:87], v[84:87]\n"
"ds_read_b64_tr_b16 v[198:199], v224 offset:53760\n"
"v_add_f32 v246, v246, v174\n"
"v_mfma_f32_16x16x32_bf16 v[88:91], v[200:203], a[72:75], v[88:91]\n"
"s_waitcnt vmcnt(9)\n"
"ds_read_b128 a[96:99], v132 offset:0\n"
"v_add_f32 v246, v246, v175\n"
"v_mfma_f32_16x16x32_bf16 v[88:91], v[204:207], a[88:91], v[88:91]\n"
"ds_read_b128 a[112:115], v133 offset:0\n"
"v_add_f32 v246, v246, v176\n"
"v_mfma_f32_16x16x32_bf16 v[92:95], v[200:203], a[76:79], v[92:95]\n"
"buffer_load_dwordx4 v129, s[20:23], s61 offen offset:1024 lds\n"
"v_mfma_f32_16x16x32_bf16 v[92:95], v[204:207], a[92:95], v[92:95]\n"
"ds_read_b128 a[100:103], v132 offset:2048\n"
"s_waitcnt lgkmcnt(11)\n"
"v_mfma_f32_16x16x32_bf16 v[96:99], v[208:211], a[64:67], v[96:99]\n"
"ds_read_b128 a[116:119], v133 offset:2048\n"
"v_add_f32 v246, v246, v177\n"
"v_mfma_f32_16x16x32_bf16 v[96:99], v[212:215], a[80:83], v[96:99]\n"
"ds_read_b128 a[104:107], v132 offset:4096\n"
"v_add_f32 v246, v246, v178\n"
"v_mfma_f32_16x16x32_bf16 v[100:103], v[208:211], a[68:71], v[100:103]\n"
"ds_read_b128 a[120:123], v133 offset:4096\n"
"v_add_f32 v246, v246, v179\n"
"v_mfma_f32_16x16x32_bf16 v[100:103], v[212:215], a[84:87], v[100:103]\n"
"ds_read_b128 a[108:111], v132 offset:6144\n"
"v_add_f32 v246, v246, v180\n"
"v_mfma_f32_16x16x32_bf16 v[104:107], v[208:211], a[72:75], v[104:107]\n"
"ds_read_b128 a[124:127], v133 offset:6144\n"
"v_add_f32 v246, v246, v181\n"
"v_mfma_f32_16x16x32_bf16 v[104:107], v[212:215], a[88:91], v[104:107]\n"
"buffer_load_dwordx4 v130, s[20:23], s61 offen offset:2048 lds\n"
"v_mfma_f32_16x16x32_bf16 v[108:111], v[208:211], a[76:79], v[108:111]\n"
"ds_read_b64_tr_b16 v[200:201], v225 offset:49152\n"
"v_add_f32 v246, v246, v182\n"
"v_mfma_f32_16x16x32_bf16 v[108:111], v[212:215], a[92:95], v[108:111]\n"
"ds_read_b64_tr_b16 v[202:203], v225 offset:49664\n"
"s_waitcnt lgkmcnt(14)\n"
"v_mfma_f32_16x16x32_bf16 v[112:115], v[216:219], a[64:67], v[112:115]\n"
"buffer_load_dwordx4 v131, s[20:23], s61 offen offset:3072 lds\n"
"v_mfma_f32_16x16x32_bf16 v[112:115], v[220:223], a[80:83], v[112:115]\n"
"ds_read_b64_tr_b16 v[204:205], v225 offset:53248\n"
"v_add_f32 v246, v246, v183\n"
"v_mfma_f32_16x16x32_bf16 v[116:119], v[216:219], a[68:71], v[116:119]\n"
"ds_read_b64_tr_b16 v[206:207], v225 offset:53760\n"
"v_add_f32 v246, v246, v184\n"
"v_mfma_f32_16x16x32_bf16 v[116:119], v[220:223], a[84:87], v[116:119]\n"
"ds_read_b64_tr_b16 v[208:209], v226 offset:49152\n"
"v_add_f32 v246, v246, v185\n"
"v_mfma_f32_16x16x32_bf16 v[120:123], v[216:219], a[72:75], v[120:123]\n"
"ds_read_b64_tr_b16 v[210:211], v226 offset:49664\n"
"v_add_f32 v246, v246, v186\n"
"v_mfma_f32_16x16x32_bf16 v[120:123], v[220:223], a[88:91], v[120:123]\n"
"ds_read_b64_tr_b16 v[212:213], v226 offset:53248\n"
"v_add_f32 v246, v246, v187\n"
"v_mfma_f32_16x16x32_bf16 v[124:127], v[216:219], a[76:79], v[124:127]\n"
"ds_read_b64_tr_b16 v[214:215], v226 offset:53760\n"
"v_add_f32 v246, v246, v188\n"
"v_mfma_f32_16x16x32_bf16 v[124:127], v[220:223], a[92:95], v[124:127]\n"
"ds_read_b64_tr_b16 v[216:217], v227 offset:49152\n"
"s_waitcnt lgkmcnt(9)\n"
"v_mfma_f32_16x16x32_bf16 v[0:3], v[192:195], a[96:99], v[0:3]\n"
"ds_read_b64_tr_b16 v[218:219], v227 offset:49664\n"
"v_add_f32 v246, v246, v189\n"
"v_mfma_f32_16x16x32_bf16 v[0:3], v[196:199], a[112:115], v[0:3]\n"
"s_add_u32 m0, s58, 24576\n"
"s_mov_b32 s60, 2621440\n"
"v_mfma_f32_16x16x32_bf16 v[4:7], v[192:195], a[100:103], v[4:7]\n"
"buffer_load_dwordx4 v234, s[24:27], s60 offen offset:0 lds\n"
"v_mfma_f32_16x16x32_bf16 v[4:7], v[196:199], a[116:119], v[4:7]\n"
"ds_read_b64_tr_b16 v[220:221], v227 offset:53248\n"
"v_add_f32 v246, v246, v190\n"
"v_mfma_f32_16x16x32_bf16 v[8:11], v[192:195], a[104:107], v[8:11]\n"
"ds_read_b64_tr_b16 v[222:223], v227 offset:53760\n"
"v_add_f32 v246, v246, v191\n"
"v_mfma_f32_16x16x32_bf16 v[8:11], v[196:199], a[120:123], v[8:11]\n"
"s_nop 0\n"
"v_add_f32_dpp v246, v246, v246 row_ror:1 row_mask:0xf bank_mask:0xf\n"
"s_lshr_b32 s16, s14, 1\n"
"v_mfma_f32_16x16x32_bf16 v[12:15], v[192:195], a[108:111], v[12:15]\n"
"buffer_load_dwordx4 v235, s[24:27], s60 offen offset:1024 lds\n"
"v_mfma_f32_16x16x32_bf16 v[12:15], v[196:199], a[124:127], v[12:15]\n"
"ds_read_b64_tr_b16 v[192:193], v224 offset:0\n"
"s_waitcnt lgkmcnt(9)\n"
"v_mfma_f32_16x16x32_bf16 v[16:19], v[200:203], a[96:99], v[16:19]\n"
"ds_read_b64_tr_b16 v[194:195], v224 offset:512\n"
"v_add_f32_dpp v246, v246, v246 row_ror:2 row_mask:0xf bank_mask:0xf\n"
"v_mfma_f32_16x16x32_bf16 v[16:19], v[204:207], a[112:115], v[16:19]\n"
"ds_read_b64_tr_b16 v[196:197], v224 offset:4096\n"
"v_add_f32_dpp v246, v246, v246 row_ror:4 row_mask:0xf bank_mask:0xf\n"
"v_mfma_f32_16x16x32_bf16 v[20:23], v[200:203], a[100:103], v[20:23]\n"
"ds_read_b64_tr_b16 v[198:199], v224 offset:4608\n"
"v_add_f32_dpp v246, v246, v246 row_ror:8 row_mask:0xf bank_mask:0xf\n"
"v_mfma_f32_16x16x32_bf16 v[20:23], v[204:207], a[116:119], v[20:23]\n"
"s_add_u32 m0, s58, 32768\n"
"s_mov_b32 s60, 2621568\n"
"v_mfma_f32_16x16x32_bf16 v[24:27], v[200:203], a[104:107], v[24:27]\n"
"buffer_load_dwordx4 v234, s[24:27], s60 offen offset:0 lds\n"
"v_mfma_f32_16x16x32_bf16 v[24:27], v[204:207], a[120:123], v[24:27]\n"
"v_mul_f32 v246, 0x3b000000, v246\n"
"v_sub_f32 v160, v160, v246\n"
"v_mfma_f32_16x16x32_bf16 v[28:31], v[200:203], a[108:111], v[28:31]\n"
"ds_read_b64_tr_b16 v[200:201], v225 offset:0\n"
"v_sub_f32 v161, v161, v246\n"
"v_mfma_f32_16x16x32_bf16 v[28:31], v[204:207], a[124:127], v[28:31]\n"
"ds_read_b64_tr_b16 v[202:203], v225 offset:512\n"
"s_waitcnt lgkmcnt(10)\n"
"v_mfma_f32_16x16x32_bf16 v[32:35], v[208:211], a[96:99], v[32:35]\n"
"buffer_load_dwordx4 v235, s[24:27], s60 offen offset:1024 lds\n"
"v_mfma_f32_16x16x32_bf16 v[32:35], v[212:215], a[112:115], v[32:35]\n"
"ds_read_b64_tr_b16 v[204:205], v225 offset:4096\n"
"v_sub_f32 v162, v162, v246\n"
"v_mfma_f32_16x16x32_bf16 v[36:39], v[208:211], a[100:103], v[36:39]\n"
"ds_read_b64_tr_b16 v[206:207], v225 offset:4608\n"
"v_sub_f32 v163, v163, v246\n"
"v_mfma_f32_16x16x32_bf16 v[36:39], v[212:215], a[116:119], v[36:39]\n"
"v_sub_f32 v164, v164, v246\n"
"v_sub_f32 v165, v165, v246\n"
"v_mfma_f32_16x16x32_bf16 v[40:43], v[208:211], a[104:107], v[40:43]\n"
"v_sub_f32 v166, v166, v246\n"
"v_sub_f32 v167, v167, v246\n"
"v_mfma_f32_16x16x32_bf16 v[40:43], v[212:215], a[120:123], v[40:43]\n"
"s_add_u32 m0, s59, 16384\n"
"s_mov_b32 s61, 640\n"
"v_mfma_f32_16x16x32_bf16 v[44:47], v[208:211], a[108:111], v[44:47]\n"
"buffer_load_dwordx4 v128, s[20:23], s61 offen offset:0 lds\n"
"v_mfma_f32_16x16x32_bf16 v[44:47], v[212:215], a[124:127], v[44:47]\n"
"ds_read_b64_tr_b16 v[208:209], v226 offset:0\n"
"s_waitcnt lgkmcnt(9)\n"
"v_mfma_f32_16x16x32_bf16 v[48:51], v[216:219], a[96:99], v[48:51]\n"
"ds_read_b64_tr_b16 v[210:211], v226 offset:512\n"
"v_sub_f32 v168, v168, v246\n"
"v_mfma_f32_16x16x32_bf16 v[48:51], v[220:223], a[112:115], v[48:51]\n"
"ds_read_b64_tr_b16 v[212:213], v226 offset:4096\n"
"v_sub_f32 v169, v169, v246\n"
"v_mfma_f32_16x16x32_bf16 v[52:55], v[216:219], a[100:103], v[52:55]\n"
"buffer_load_dwordx4 v129, s[20:23], s61 offen offset:1024 lds\n"
"v_mfma_f32_16x16x32_bf16 v[52:55], v[220:223], a[116:119], v[52:55]\n"
"ds_read_b64_tr_b16 v[214:215], v226 offset:4608\n"
"v_sub_f32 v170, v170, v246\n"
"v_mfma_f32_16x16x32_bf16 v[56:59], v[216:219], a[104:107], v[56:59]\n"
"v_sub_f32 v171, v171, v246\n"
"v_sub_f32 v172, v172, v246\n"
"v_mfma_f32_16x16x32_bf16 v[56:59], v[220:223], a[120:123], v[56:59]\n"
"v_sub_f32 v173, v173, v246\n"
"v_sub_f32 v174, v174, v246\n"
"v_mfma_f32_16x16x32_bf16 v[60:63], v[216:219], a[108:111], v[60:63]\n"
"ds_read_b64_tr_b16 v[216:217], v227 offset:0\n"
"v_sub_f32 v175, v175, v246\n"
"v_mfma_f32_16x16x32_bf16 v[60:63], v[220:223], a[124:127], v[60:63]\n"
"ds_read_b64_tr_b16 v[218:219], v227 offset:512\n"
"s_waitcnt lgkmcnt(10)\n"
"v_mfma_f32_16x16x32_bf16 v[64:67], v[192:195], a[96:99], v[64:67]\n"
"buffer_load_dwordx4 v130, s[20:23], s61 offen offset:2048 lds\n"
"v_mfma_f32_16x16x32_bf16 v[64:67], v[196:199], a[112:115], v[64:67]\n"
"ds_read_b64_tr_b16 v[220:221], v227 offset:4096\n"
"v_sub_f32 v176, v176, v246\n"
"v_mfma_f32_16x16x32_bf16 v[68:71], v[192:195], a[100:103], v[68:71]\n"
"ds_read_b64_tr_b16 v[222:223], v227 offset:4608\n"
"v_sub_f32 v177, v177, v246\n"
"v_mfma_f32_16x16x32_bf16 v[68:71], v[196:199], a[116:119], v[68:71]\n"
"v_sub_f32 v178, v178, v246\n"
"v_sub_f32 v179, v179, v246\n"
"v_mfma_f32_16x16x32_bf16 v[72:75], v[192:195], a[104:107], v[72:75]\n"
"buffer_load_dwordx4 v131, s[20:23], s61 offen offset:3072 lds\n"
"v_mfma_f32_16x16x32_bf16 v[72:75], v[196:199], a[120:123], v[72:75]\n"
"s_add_u32 m0, s59, 20480\n"
"s_mov_b32 s61, 33408\n"
"v_mfma_f32_16x16x32_bf16 v[76:79], v[192:195], a[108:111], v[76:79]\n"
"s_waitcnt vmcnt(16)\n"
"s_barrier\n"
"v_mfma_f32_16x16x32_bf16 v[76:79], v[196:199], a[124:127], v[76:79]\n"
"ds_read_b64_tr_b16 v[192:193], v224 offset:8192\n"
"s_waitcnt lgkmcnt(9)\n"
"v_mfma_f32_16x16x32_bf16 v[80:83], v[200:203], a[96:99], v[80:83]\n"
"ds_read_b64_tr_b16 v[194:195], v224 offset:8704\n"
"v_sub_f32 v180, v180, v246\n"
"v_mfma_f32_16x16x32_bf16 v[80:83], v[204:207], a[112:115], v[80:83]\n"
"buffer_load_dwordx4 v128, s[20:23], s61 offen offset:0 lds\n"
"v_mfma_f32_16x16x32_bf16 v[84:87], v[200:203], a[100:103], v[84:87]\n"
"ds_read_b64_tr_b16 v[196:197], v224 offset:12288\n"
"v_sub_f32 v181, v181, v246\n"
"v_mfma_f32_16x16x32_bf16 v[84:87], v[204:207], a[116:119], v[84:87]\n"
"ds_read_b64_tr_b16 v[198:199], v224 offset:12800\n"
"v_sub_f32 v182, v182, v246\n"
"v_mfma_f32_16x16x32_bf16 v[88:91], v[200:203], a[104:107], v[88:91]\n"
"v_sub_f32 v183, v183, v246\n"
"v_sub_f32 v184, v184, v246\n"
"v_mfma_f32_16x16x32_bf16 v[88:91], v[204:207], a[120:123], v[88:91]\n"
"v_sub_f32 v185, v185, v246\n"
"v_sub_f32 v186, v186, v246\n"
"v_mfma_f32_16x16x32_bf16 v[92:95], v[200:203], a[108:111], v[92:95]\n"
"buffer_load_dwordx4 v129, s[20:23], s61 offen offset:1024 lds\n"
"v_mfma_f32_16x16x32_bf16 v[92:95], v[204:207], a[124:127], v[92:95]\n"
"ds_read_b64_tr_b16 v[200:201], v225 offset:8192\n"
"s_waitcnt lgkmcnt(9)\n"
"v_mfma_f32_16x16x32_bf16 v[96:99], v[208:211], a[96:99], v[96:99]\n"
"buffer_load_dwordx2 v[250:251], v134, s[40:43], s49 offen sc1\n"
"ds_read_b64_tr_b16 v[202:203], v225 offset:8704\n"
"v_mfma_f32_16x16x32_bf16 v[96:99], v[212:215], a[112:115], v[96:99]\n"
"ds_read_b64_tr_b16 v[204:205], v225 offset:12288\n"
"v_sub_f32 v187, v187, v246\n"
"v_mfma_f32_16x16x32_bf16 v[100:103], v[208:211], a[100:103], v[100:103]\n"
"ds_read_b64_tr_b16 v[206:207], v225 offset:12800\n"
"v_sub_f32 v188, v188, v246\n"
"v_mfma_f32_16x16x32_bf16 v[100:103], v[212:215], a[116:119], v[100:103]\n"
"buffer_load_dwordx4 v130, s[20:23], s61 offen offset:2048 lds\n"
"v_mfma_f32_16x16x32_bf16 v[104:107], v[208:211], a[104:107], v[104:107]\n"
"s_waitcnt vmcnt(12)\n"
"ds_read_b128 a[128:131], v132 offset:8192\n"
"v_sub_f32 v189, v189, v246\n"
"v_mfma_f32_16x16x32_bf16 v[104:107], v[212:215], a[120:123], v[104:107]\n"
"ds_read_b128 a[144:147], v133 offset:8192\n"
"v_sub_f32 v190, v190, v246\n"
"v_mfma_f32_16x16x32_bf16 v[108:111], v[208:211], a[108:111], v[108:111]\n"
"ds_read_b128 a[132:135], v132 offset:10240\n"
"v_sub_f32 v191, v191, v246\n"
"ds_read_b128 a[148:151], v133 offset:10240\n"
"v_mfma_f32_16x16x32_bf16 v[108:111], v[212:215], a[124:127], v[108:111]\n"
"ds_read_b128 a[136:139], v132 offset:12288\n"
"ds_read_b128 a[152:155], v133 offset:12288\n"
"s_waitcnt lgkmcnt(14)\n"
"v_mfma_f32_16x16x32_bf16 v[112:115], v[216:219], a[96:99], v[112:115]\n"
"ds_read_b128 a[140:143], v132 offset:14336\n"
"v_and_b32 v246, 48, v244\n"
"ds_read_b128 a[156:159], v133 offset:14336\n"
"v_mfma_f32_16x16x32_bf16 v[112:115], v[220:223], a[112:115], v[112:115]\n"
"buffer_load_dwordx4 v131, s[20:23], s61 offen offset:3072 lds\n"
"v_mfma_f32_16x16x32_bf16 v[116:119], v[216:219], a[100:103], v[116:119]\n"
"ds_read_b64_tr_b16 v[208:209], v226 offset:8192\n"
"v_mul_f32 v247, v160, v160\n"
"v_mfma_f32_16x16x32_bf16 v[116:119], v[220:223], a[116:119], v[116:119]\n"
"ds_read_b64_tr_b16 v[210:211], v226 offset:8704\n"
"v_fmac_f32 v247, v161, v161\n"
"v_mfma_f32_16x16x32_bf16 v[120:123], v[216:219], a[104:107], v[120:123]\n"
"ds_read_b64_tr_b16 v[212:213], v226 offset:12288\n"
"v_fmac_f32 v247, v162, v162\n"
"v_mfma_f32_16x16x32_bf16 v[120:123], v[220:223], a[120:123], v[120:123]\n"
"ds_read_b64_tr_b16 v[214:215], v226 offset:12800\n"
"v_fmac_f32 v247, v163, v163\n"
"v_mfma_f32_16x16x32_bf16 v[124:127], v[216:219], a[108:111], v[124:127]\n"
"ds_read_b64_tr_b16 v[216:217], v227 offset:8192\n"
"v_fmac_f32 v247, v164, v164\n"
"v_mfma_f32_16x16x32_bf16 v[124:127], v[220:223], a[124:127], v[124:127]\n"
"ds_read_b64_tr_b16 v[218:219], v227 offset:8704\n"
"s_waitcnt lgkmcnt(6)\n"
"v_mfma_f32_16x16x32_bf16 v[0:3], v[192:195], a[128:131], v[0:3]\n"
"ds_read_b64_tr_b16 v[220:221], v227 offset:12288\n"
"v_fmac_f32 v247, v165, v165\n"
"v_mfma_f32_16x16x32_bf16 v[0:3], v[196:199], a[144:147], v[0:3]\n"
"s_add_u32 m0, s58, 40960\n"
"s_mov_b32 s60, 3145728\n"
"v_mfma_f32_16x16x32_bf16 v[4:7], v[192:195], a[132:135], v[4:7]\n"
"buffer_load_dwordx4 v234, s[24:27], s60 offen offset:0 lds\n"
"v_mfma_f32_16x16x32_bf16 v[4:7], v[196:199], a[148:151], v[4:7]\n"
"ds_read_b64_tr_b16 v[222:223], v227 offset:12800\n"
"v_fmac_f32 v247, v166, v166\n"
"v_mfma_f32_16x16x32_bf16 v[8:11], v[192:195], a[136:139], v[8:11]\n"
"v_fmac_f32 v247, v167, v167\n"
"v_fmac_f32 v247, v168, v168\n"
"v_mfma_f32_16x16x32_bf16 v[8:11], v[196:199], a[152:155], v[8:11]\n"
"v_fmac_f32 v247, v169, v169\n"
"v_fmac_f32 v247, v170, v170\n"
"v_mfma_f32_16x16x32_bf16 v[12:15], v[192:195], a[140:143], v[12:15]\n"
"buffer_load_dwordx4 v235, s[24:27], s60 offen offset:1024 lds\n"
"v_mfma_f32_16x16x32_bf16 v[12:15], v[196:199], a[156:159], v[12:15]\n"
"ds_read_b64_tr_b16 v[192:193], v224 offset:16384\n"
"v_mfma_f32_16x16x32_bf16 v[16:19], v[200:203], a[128:131], v[16:19]\n"
"ds_read_b64_tr_b16 v[194:195], v224 offset:16896\n"
"v_fmac_f32 v247, v171, v171\n"
"v_mfma_f32_16x16x32_bf16 v[16:19], v[204:207], a[144:147], v[16:19]\n"
"ds_read_b64_tr_b16 v[196:197], v224 offset:20480\n"
"v_fmac_f32 v247, v172, v172\n"
"v_mfma_f32_16x16x32_bf16 v[20:23], v[200:203], a[132:135], v[20:23]\n"
"ds_read_b64_tr_b16 v[198:199], v224 offset:20992\n"
"v_fmac_f32 v247, v173, v173\n"
"v_mfma_f32_16x16x32_bf16 v[20:23], v[204:207], a[148:151], v[20:23]\n"
"s_add_u32 m0, s58, 49152\n"
"s_mov_b32 s60, 3145856\n"
"v_mfma_f32_16x16x32_bf16 v[24:27], v[200:203], a[136:139], v[24:27]\n"
"buffer_load_dwordx4 v234, s[24:27], s60 offen offset:0 lds\n"
"v_mfma_f32_16x16x32_bf16 v[24:27], v[204:207], a[152:155], v[24:27]\n"
"v_fmac_f32 v247, v174, v174\n"
"v_fmac_f32 v247, v175, v175\n"
"v_mfma_f32_16x16x32_bf16 v[28:31], v[200:203], a[140:143], v[28:31]\n"
"ds_read_b64_tr_b16 v[200:201], v225 offset:16384\n"
"v_fmac_f32 v247, v176, v176\n"
"v_mfma_f32_16x16x32_bf16 v[28:31], v[204:207], a[156:159], v[28:31]\n"
"ds_read_b64_tr_b16 v[202:203], v225 offset:16896\n"
"s_waitcnt lgkmcnt(10)\n"
"v_mfma_f32_16x16x32_bf16 v[32:35], v[208:211], a[128:131], v[32:35]\n"
"buffer_load_dwordx4 v235, s[24:27], s60 offen offset:1024 lds\n"
"v_mfma_f32_16x16x32_bf16 v[32:35], v[212:215], a[144:147], v[32:35]\n"
"ds_read_b64_tr_b16 v[204:205], v225 offset:20480\n"
"v_fmac_f32 v247, v177, v177\n"
"v_mfma_f32_16x16x32_bf16 v[36:39], v[208:211], a[132:135], v[36:39]\n"
"ds_read_b64_tr_b16 v[206:207], v225 offset:20992\n"
"v_fmac_f32 v247, v178, v178\n"
"v_mfma_f32_16x16x32_bf16 v[36:39], v[212:215], a[148:151], v[36:39]\n"
"v_fmac_f32 v247, v179, v179\n"
"v_fmac_f32 v247, v180, v180\n"
"v_mfma_f32_16x16x32_bf16 v[40:43], v[208:211], a[136:139], v[40:43]\n"
"v_fmac_f32 v247, v181, v181\n"
"v_fmac_f32 v247, v182, v182\n"
"v_mfma_f32_16x16x32_bf16 v[40:43], v[212:215], a[152:155], v[40:43]\n"
"s_add_u32 m0, s59, 0\n"
"s_mov_b32 s61, 768\n"
"v_mfma_f32_16x16x32_bf16 v[44:47], v[208:211], a[140:143], v[44:47]\n"
"buffer_load_dwordx4 v128, s[20:23], s61 offen offset:0 lds\n"
"v_mfma_f32_16x16x32_bf16 v[44:47], v[212:215], a[156:159], v[44:47]\n"
"ds_read_b64_tr_b16 v[208:209], v226 offset:16384\n"
"s_waitcnt vmcnt(7)\n"
"v_cmp_ne_u64 vcc, s[6:7], v[250:251]\n"
"s_waitcnt lgkmcnt(9)\n"
"v_mfma_f32_16x16x32_bf16 v[48:51], v[216:219], a[128:131], v[48:51]\n"
"s_cbranch_vccnz Lpm_%=\n"
"Lpm_ok_%=:\n"
"ds_read_b64_tr_b16 v[210:211], v226 offset:16896\n"
"v_mfma_f32_16x16x32_bf16 v[48:51], v[220:223], a[144:147], v[48:51]\n"
"ds_read_b64_tr_b16 v[212:213], v226 offset:20480\n"
"buffer_load_dwordx4 v[144:147], v232, s[40:43], s48 offen sc1\n"
"v_mfma_f32_16x16x32_bf16 v[52:55], v[216:219], a[132:135], v[52:55]\n"
"buffer_load_dwordx4 v129, s[20:23], s61 offen offset:1024 lds\n"
"v_mfma_f32_16x16x32_bf16 v[52:55], v[220:223], a[148:151], v[52:55]\n"
"ds_read_b64_tr_b16 v[214:215], v226 offset:20992\n"
"s_add_u32 s48, s48, 12288\n"
"v_mfma_f32_16x16x32_bf16 v[56:59], v[216:219], a[136:139], v[56:59]\n"
"s_lshl_b32 s49, s14, 10\n"
"s_add_u32 s49, s49, s15\n"
"v_mfma_f32_16x16x32_bf16 v[56:59], v[220:223], a[152:155], v[56:59]\n"
"buffer_load_dwordx4 v[148:151], v232, s[40:43], s48 offen sc1\n"
"s_add_u32 s48, s48, 12288\n"
"v_mfma_f32_16x16x32_bf16 v[60:63], v[216:219], a[140:143], v[60:63]\n"
"ds_read_b64_tr_b16 v[216:217], v227 offset:16384\n"
"s_add_u32 s49, s49, 8192\n"
"v_mfma_f32_16x16x32_bf16 v[60:63], v[220:223], a[156:159], v[60:63]\n"
"ds_read_b64_tr_b16 v[218:219], v227 offset:16896\n"
"s_waitcnt lgkmcnt(10)\n"
"v_mfma_f32_16x16x32_bf16 v[64:67], v[192:195], a[128:131], v[64:67]\n"
"buffer_load_dwordx4 v130, s[20:23], s61 offen offset:2048 lds\n"
"v_mfma_f32_16x16x32_bf16 v[64:67], v[196:199], a[144:147], v[64:67]\n"
"ds_read_b64_tr_b16 v[220:221], v227 offset:20480\n"
"buffer_load_dwordx4 v[152:155], v232, s[40:43], s48 offen sc1\n"
"v_mfma_f32_16x16x32_bf16 v[68:71], v[192:195], a[132:135], v[68:71]\n"
"ds_read_b64_tr_b16 v[222:223], v227 offset:20992\n"
"s_add_u32 s48, s48, 12288\n"
"v_mfma_f32_16x16x32_bf16 v[68:71], v[196:199], a[148:151], v[68:71]\n"
"v_fmac_f32 v247, v183, v183\n"
"v_fmac_f32 v247, v184, v184\n"
"v_mfma_f32_16x16x32_bf16 v[72:75], v[192:195], a[136:139], v[72:75]\n"
"buffer_load_dwordx4 v131, s[20:23], s61 offen offset:3072 lds\n"
"v_mfma_f32_16x16x32_bf16 v[72:75], v[196:199], a[152:155], v[72:75]\n"
"s_add_u32 m0, s59, 4096\n"
"s_mov_b32 s61, 33536\n"
"v_mfma_f32_16x16x32_bf16 v[76:79], v[192:195], a[140:143], v[76:79]\n"
"s_barrier\n"
"v_mfma_f32_16x16x32_bf16 v[76:79], v[196:199], a[156:159], v[76:79]\n"
"buffer_load_dwordx4 v[156:159], v232, s[40:43], s48 offen sc1\n"
"s_waitcnt lgkmcnt(8)\n"
"v_mfma_f32_16x16x32_bf16 v[80:83], v[200:203], a[128:131], v[80:83]\n"
"ds_read_b64_tr_b16 v[192:193], v224 offset:24576\n"
"buffer_load_dword v140, v135, s[40:43], s49 offen sc1\n"
"v_mfma_f32_16x16x32_bf16 v[80:83], v[204:207], a[144:147], v[80:83]\n"
"buffer_load_dwordx4 v128, s[20:23], s61 offen offset:0 lds\n"
"v_mfma_f32_16x16x32_bf16 v[84:87], v[200:203], a[132:135], v[84:87]\n"
"ds_read_b64_tr_b16 v[194:195], v224 offset:25088\n"
"s_add_u32 s49, s49, 12288\n"
"v_mfma_f32_16x16x32_bf16 v[84:87], v[204:207], a[148:151], v[84:87]\n"
"ds_read_b64_tr_b16 v[196:197], v224 offset:28672\n"
"buffer_load_dword v141, v135, s[40:43], s49 offen sc1\n"
"v_mfma_f32_16x16x32_bf16 v[88:91], v[200:203], a[136:139], v[88:91]\n"
"ds_read_b64_tr_b16 v[198:199], v224 offset:29184\n"
"s_add_u32 s49, s49, 12288\n"
"v_mfma_f32_16x16x32_bf16 v[88:91], v[204:207], a[152:155], v[88:91]\n"
"buffer_load_dword v142, v135, s[40:43], s49 offen sc1\n"
"s_add_u32 s49, s49, 12288\n"
"v_mfma_f32_16x16x32_bf16 v[92:95], v[200:203], a[140:143], v[92:95]\n"
"buffer_load_dwordx4 v129, s[20:23], s61 offen offset:1024 lds\n"
"v_mfma_f32_16x16x32_bf16 v[92:95], v[204:207], a[156:159], v[92:95]\n"
"ds_read_b64_tr_b16 v[200:201], v225 offset:24576\n"
"s_waitcnt lgkmcnt(9)\n"
"v_mfma_f32_16x16x32_bf16 v[96:99], v[208:211], a[128:131], v[96:99]\n"
"ds_read_b64_tr_b16 v[202:203], v225 offset:25088\n"
"buffer_load_dword v143, v135, s[40:43], s49 offen sc1\n"
"v_mfma_f32_16x16x32_bf16 v[96:99], v[212:215], a[144:147], v[96:99]\n"
"ds_read_b64_tr_b16 v[204:205], v225 offset:28672\n"
"v_fmac_f32 v247, v185, v185\n"
"v_mfma_f32_16x16x32_bf16 v[100:103], v[208:211], a[132:135], v[100:103]\n"
"ds_read_b64_tr_b16 v[206:207], v225 offset:29184\n"
"v_fmac_f32 v247, v186, v186\n"
"v_mfma_f32_16x16x32_bf16 v[100:103], v[212:215], a[148:151], v[100:103]\n"
"buffer_load_dwordx4 v130, s[20:23], s61 offen offset:2048 lds\n"
"v_mfma_f32_16x16x32_bf16 v[104:107], v[208:211], a[136:139], v[104:107]\n"
"v_fmac_f32 v247, v187, v187\n"
"v_fmac_f32 v247, v188, v188\n"
"v_mfma_f32_16x16x32_bf16 v[104:107], v[212:215], a[152:155], v[104:107]\n"
"s_waitcnt vmcnt(19)\n"
"ds_read_b128 a[160:163], v132 offset:16384\n"
"v_fmac_f32 v247, v189, v189\n"
"v_mfma_f32_16x16x32_bf16 v[108:111], v[208:211], a[140:143], v[108:111]\n"
"ds_read_b128 a[176:179], v133 offset:16384\n"
"v_fmac_f32 v247, v190, v190\n"
"ds_read_b128 a[164:167], v132 offset:18432\n"
"v_mfma_f32_16x16x32_bf16 v[108:111], v[212:215], a[156:159], v[108:111]\n"
"ds_read_b128 a[180:183], v133 offset:18432\n"
"ds_read_b128 a[168:171], v132 offset:20480\n"
"s_waitcnt lgkmcnt(13)\n"
"v_mfma_f32_16x16x32_bf16 v[112:115], v[216:219], a[128:131], v[112:115]\n"
"ds_read_b128 a[184:187], v133 offset:20480\n"
"v_fmac_f32 v247, v191, v191\n"
"ds_read_b128 a[172:175], v132 offset:22528\n"
"v_mfma_f32_16x16x32_bf16 v[112:115], v[220:223], a[144:147], v[112:115]\n"
"ds_read_b128 a[188:191], v133 offset:22528\n"
"v_add_f32_dpp v247, v247, v247 row_ror:1 row_mask:0xf bank_mask:0xf\n"
"v_mfma_f32_16x16x32_bf16 v[116:119], v[216:219], a[132:135], v[116:119]\n"
"buffer_load_dwordx4 v131, s[20:23], s61 offen offset:3072 lds\n"
"v_mfma_f32_16x16x32_bf16 v[116:119], v[220:223], a[148:151], v[116:119]\n"
"ds_read_b64_tr_b16 v[208:209], v226 offset:24576\n"
"v_add_f32_dpp v247, v247, v247 row_ror:2 row_mask:0xf bank_mask:0xf\n"
"v_mfma_f32_16x16x32_bf16 v[120:123], v[216:219], a[136:139], v[120:123]\n"
"ds_read_b64_tr_b16 v[210:211], v226 offset:25088\n"
"v_add_f32_dpp v247, v247, v247 row_ror:4 row_mask:0xf bank_mask:0xf\n"
"v_mfma_f32_16x16x32_bf16 v[120:123], v[220:223], a[152:155], v[120:123]\n"
"ds_read_b64_tr_b16 v[212:213], v226 offset:28672\n"
"v_add_f32_dpp v247, v247, v247 row_ror:8 row_mask:0xf bank_mask:0xf\n"
"v_mfma_f32_16x16x32_bf16 v[124:127], v[216:219], a[140:143], v[124:127]\n"
"ds_read_b64_tr_b16 v[214:215], v226 offset:29184\n"
"v_mul_f32 v247, 0x3b000000, v247\n"
"v_mfma_f32_16x16x32_bf16 v[124:127], v[220:223], a[156:159], v[124:127]\n"
"ds_read_b64_tr_b16 v[216:217], v227 offset:24576\n"
"s_waitcnt lgkmcnt(5)\n"
"v_mfma_f32_16x16x32_bf16 v[0:3], v[192:195], a[160:163], v[0:3]\n"
"ds_read_b64_tr_b16 v[218:219], v227 offset:25088\n"
"v_add_f32 v247, 0x3727c5ac, v247\n"
"v_mfma_f32_16x16x32_bf16 v[0:3], v[196:199], a[176:179], v[0:3]\n"
"s_add_u32 m0, s58, 0\n"
"s_mov_b32 s60, 3670016\n"
"v_mfma_f32_16x16x32_bf16 v[4:7], v[192:195], a[164:167], v[4:7]\n"
"buffer_load_dwordx4 v234, s[24:27], s60 offen offset:0 lds\n"
"v_mfma_f32_16x16x32_bf16 v[4:7], v[196:199], a[180:183], v[4:7]\n"
"ds_read_b64_tr_b16 v[220:221], v227 offset:28672\n"
"s_add_u32 s48, s48, 12288\n"
"v_mfma_f32_16x16x32_bf16 v[8:11], v[192:195], a[168:171], v[8:11]\n"
"ds_read_b64_tr_b16 v[222:223], v227 offset:29184\n"
"s_lshl_b32 s48, s13, 13\n"
"v_mfma_f32_16x16x32_bf16 v[8:11], v[196:199], a[184:187], v[8:11]\n"
"v_rsq_f32 v247, v247\n"
"v_mfma_f32_16x16x32_bf16 v[12:15], v[192:195], a[172:175], v[12:15]\n"
"buffer_load_dwordx4 v235, s[24:27], s60 offen offset:1024 lds\n"
"v_mfma_f32_16x16x32_bf16 v[12:15], v[196:199], a[188:191], v[12:15]\n"
"ds_read_b64_tr_b16 v[192:193], v224 offset:32768\n"
"v_mfma_f32_16x16x32_bf16 v[16:19], v[200:203], a[160:163], v[16:19]\n"
"ds_read_b64_tr_b16 v[194:195], v224 offset:33280\n"
"v_mul_f32 v160, v160, v247\n"
"v_mfma_f32_16x16x32_bf16 v[16:19], v[204:207], a[176:179], v[16:19]\n"
"ds_read_b64_tr_b16 v[196:197], v224 offset:36864\n"
"s_waitcnt vmcnt(14)\n"
"v_add_f32 v144, v144, v148\n"
"v_mfma_f32_16x16x32_bf16 v[20:23], v[200:203], a[164:167], v[20:23]\n"
"ds_read_b64_tr_b16 v[198:199], v224 offset:37376\n"
"v_add_f32 v145, v145, v149\n"
"v_mfma_f32_16x16x32_bf16 v[20:23], v[204:207], a[180:183], v[20:23]\n"
"s_add_u32 m0, s58, 8192\n"
"s_mov_b32 s60, 3670144\n"
"v_mfma_f32_16x16x32_bf16 v[24:27], v[200:203], a[168:171], v[24:27]\n"
"buffer_load_dwordx4 v234, s[24:27], s60 offen offset:0 lds\n"
"v_mfma_f32_16x16x32_bf16 v[24:27], v[204:207], a[184:187], v[24:27]\n"
"s_waitcnt vmcnt(13)\n"
"v_add_f32 v144, v144, v152\n"
"v_add_f32 v145, v145, v153\n"
"v_mfma_f32_16x16x32_bf16 v[28:31], v[200:203], a[172:175], v[28:31]\n"
"v_add_f32 v146, v146, v150\n"
"v_add_f32 v146, v146, v154\n"
"v_mfma_f32_16x16x32_bf16 v[28:31], v[204:207], a[188:191], v[28:31]\n"
"v_add_f32 v147, v147, v151\n"
"s_waitcnt lgkmcnt(8)\n"
"v_mfma_f32_16x16x32_bf16 v[32:35], v[208:211], a[160:163], v[32:35]\n"
"buffer_load_dwordx4 v235, s[24:27], s60 offen offset:1024 lds\n"
"v_mfma_f32_16x16x32_bf16 v[32:35], v[212:215], a[176:179], v[32:35]\n"
"v_add_f32 v147, v147, v155\n"
"ds_read_b64_tr_b16 v[200:201], v225 offset:32768\n"
"v_mfma_f32_16x16x32_bf16 v[36:39], v[208:211], a[164:167], v[36:39]\n"
"ds_read_b64_tr_b16 v[202:203], v225 offset:33280\n"
"v_mul_f32 v161, v161, v247\n"
"v_mfma_f32_16x16x32_bf16 v[36:39], v[212:215], a[180:183], v[36:39]\n"
"s_waitcnt vmcnt(12)\n"
"v_add_f32 v144, v144, v156\n"
"v_add_f32 v144, s50, v144\n"
"v_mfma_f32_16x16x32_bf16 v[40:43], v[208:211], a[168:171], v[40:43]\n"
"v_mul_f32 v144, 0x3e0293ee, v144\n"
"v_add_f32 v145, v145, v157\n"
"v_mfma_f32_16x16x32_bf16 v[40:43], v[212:215], a[184:187], v[40:43]\n"
"s_add_u32 m0, s59, 8192\n"
"s_mov_b32 s61, 896\n"
"v_mfma_f32_16x16x32_bf16 v[44:47], v[208:211], a[172:175], v[44:47]\n"
"buffer_load_dwordx4 v128, s[20:23], s61 offen offset:0 lds\n"
"v_mfma_f32_16x16x32_bf16 v[44:47], v[212:215], a[188:191], v[44:47]\n"
"v_add_f32 v145, s50, v145\n"
"s_waitcnt lgkmcnt(6)\n"
"v_mfma_f32_16x16x32_bf16 v[48:51], v[216:219], a[160:163], v[48:51]\n"
"v_mul_f32 v145, 0x3e0293ee, v145\n"
"v_add_f32 v146, v146, v158\n"
"v_mfma_f32_16x16x32_bf16 v[48:51], v[220:223], a[176:179], v[48:51]\n"
"v_add_f32 v146, s50, v146\n"
"v_mul_f32 v146, 0x3e0293ee, v146\n"
"v_mfma_f32_16x16x32_bf16 v[52:55], v[216:219], a[164:167], v[52:55]\n"
"v_add_f32 v147, v147, v159\n"
"v_add_f32 v147, s50, v147\n"
"v_mfma_f32_16x16x32_bf16 v[52:55], v[220:223], a[180:183], v[52:55]\n"
"v_mul_f32 v147, 0x3e0293ee, v147\n"
"ds_write_b128 v139, v[144:147]\n"
"v_mfma_f32_16x16x32_bf16 v[56:59], v[216:219], a[168:171], v[56:59]\n"
"buffer_load_dwordx4 v129, s[20:23], s61 offen offset:1024 lds\n"
"v_mfma_f32_16x16x32_bf16 v[56:59], v[220:223], a[184:187], v[56:59]\n"
"ds_read_b64_tr_b16 v[204:205], v225 offset:36864\n"
"v_and_b32 v139, 15, v244\n"
"v_mfma_f32_16x16x32_bf16 v[60:63], v[216:219], a[172:175], v[60:63]\n"
"ds_read_b64_tr_b16 v[206:207], v225 offset:37376\n"
"v_lshl_add_u32 v246, v139, 6, v246\n"
"v_mfma_f32_16x16x32_bf16 v[60:63], v[220:223], a[188:191], v[60:63]\n"
"ds_read_b64_tr_b16 v[208:209], v226 offset:32768\n"
"s_waitcnt lgkmcnt(6)\n"
"v_mfma_f32_16x16x32_bf16 v[64:67], v[192:195], a[160:163], v[64:67]\n"
"buffer_load_dwordx4 v130, s[20:23], s61 offen offset:2048 lds\n"
"v_mfma_f32_16x16x32_bf16 v[64:67], v[196:199], a[176:179], v[64:67]\n"
"ds_read_b64_tr_b16 v[210:211], v226 offset:33280\n"
"v_mul_f32 v162, v162, v247\n"
"v_mfma_f32_16x16x32_bf16 v[68:71], v[192:195], a[164:167], v[68:71]\n"
"ds_read_b64_tr_b16 v[212:213], v226 offset:36864\n"
"v_mul_f32 v163, v163, v247\n"
"v_mfma_f32_16x16x32_bf16 v[68:71], v[196:199], a[180:183], v[68:71]\n"
"ds_read_b64_tr_b16 v[214:215], v226 offset:37376\n"
"v_mul_f32 v164, v164, v247\n"
"v_mfma_f32_16x16x32_bf16 v[72:75], v[192:195], a[168:171], v[72:75]\n"
"buffer_load_dwordx4 v131, s[20:23], s61 offen offset:3072 lds\n"
"v_mfma_f32_16x16x32_bf16 v[72:75], v[196:199], a[184:187], v[72:75]\n"
"s_add_u32 m0, s59, 12288\n"
"s_mov_b32 s61, 33664\n"
"v_mfma_f32_16x16x32_bf16 v[76:79], v[192:195], a[172:175], v[76:79]\n"
"s_waitcnt lgkmcnt(6)\n"
"s_barrier\n"
"v_mfma_f32_16x16x32_bf16 v[76:79], v[196:199], a[188:191], v[76:79]\n"
"ds_read_b64_tr_b16 v[216:217], v227 offset:32768\n"
"s_waitcnt lgkmcnt(5)\n"
"v_mfma_f32_16x16x32_bf16 v[80:83], v[200:203], a[160:163], v[80:83]\n"
"ds_read_b64_tr_b16 v[218:219], v227 offset:33280\n"
"v_mul_f32 v165, v165, v247\n"
"v_mfma_f32_16x16x32_bf16 v[80:83], v[204:207], a[176:179], v[80:83]\n"
"buffer_load_dwordx4 v128, s[20:23], s61 offen offset:0 lds\n"
"v_mfma_f32_16x16x32_bf16 v[84:87], v[200:203], a[164:167], v[84:87]\n"
"ds_read_b64_tr_b16 v[220:221], v227 offset:36864\n"
"v_mul_f32 v166, v166, v247\n"
"v_mfma_f32_16x16x32_bf16 v[84:87], v[204:207], a[180:183], v[84:87]\n"
"ds_read_b64_tr_b16 v[222:223], v227 offset:37376\n"
"v_mul_f32 v167, v167, v247\n"
"v_mfma_f32_16x16x32_bf16 v[88:91], v[200:203], a[168:171], v[88:91]\n"
"ds_read_b64_tr_b16 v[192:193], v224 offset:40960\n"
"v_mul_f32 v168, v168, v247\n"
"v_mfma_f32_16x16x32_bf16 v[88:91], v[204:207], a[184:187], v[88:91]\n"
"ds_read_b64_tr_b16 v[194:195], v224 offset:41472\n"
"v_mul_f32 v169, v169, v247\n"
"v_mfma_f32_16x16x32_bf16 v[92:95], v[200:203], a[172:175], v[92:95]\n"
"buffer_load_dwordx4 v129, s[20:23], s61 offen offset:1024 lds\n"
"v_mfma_f32_16x16x32_bf16 v[92:95], v[204:207], a[188:191], v[92:95]\n"
"ds_read_b64_tr_b16 v[196:197], v224 offset:45056\n"
"s_waitcnt lgkmcnt(7)\n"
"v_mfma_f32_16x16x32_bf16 v[96:99], v[208:211], a[160:163], v[96:99]\n"
"ds_read_b64_tr_b16 v[198:199], v224 offset:45568\n"
"v_mul_f32 v170, v170, v247\n"
"v_mfma_f32_16x16x32_bf16 v[96:99], v[212:215], a[176:179], v[96:99]\n"
"ds_read_b64_tr_b16 v[200:201], v225 offset:40960\n"
"v_mul_f32 v171, v171, v247\n"
"v_mfma_f32_16x16x32_bf16 v[100:103], v[208:211], a[164:167], v[100:103]\n"
"ds_read_b64_tr_b16 v[202:203], v225 offset:41472\n"
"v_mul_f32 v172, v172, v247\n"
"v_mfma_f32_16x16x32_bf16 v[100:103], v[212:215], a[180:183], v[100:103]\n"
"buffer_load_dwordx4 v130, s[20:23], s61 offen offset:2048 lds\n"
"v_mfma_f32_16x16x32_bf16 v[104:107], v[208:211], a[168:171], v[104:107]\n"
"ds_read_b64_tr_b16 v[204:205], v225 offset:45056\n"
"v_mul_f32 v173, v173, v247\n"
"v_mfma_f32_16x16x32_bf16 v[104:107], v[212:215], a[184:187], v[104:107]\n"
"ds_read_b64_tr_b16 v[206:207], v225 offset:45568\n"
"v_mul_f32 v174, v174, v247\n"
"v_mfma_f32_16x16x32_bf16 v[108:111], v[208:211], a[172:175], v[108:111]\n"
"s_waitcnt vmcnt(11)\n"
"ds_read_b128 a[192:195], v132 offset:0\n"
"v_mul_f32 v175, v175, v247\n"
"ds_read_b128 a[208:211], v133 offset:0\n"
"v_mfma_f32_16x16x32_bf16 v[108:111], v[212:215], a[188:191], v[108:111]\n"
"ds_read_b128 a[196:199], v132 offset:2048\n"
"ds_read_b128 a[212:215], v133 offset:2048\n"
"s_waitcnt lgkmcnt(12)\n"
"v_mfma_f32_16x16x32_bf16 v[112:115], v[216:219], a[160:163], v[112:115]\n"
"ds_read_b128 a[200:203], v132 offset:4096\n"
"v_add_f32 v140, v140, v141\n"
"ds_read_b128 a[216:219], v133 offset:4096\n"
"v_mfma_f32_16x16x32_bf16 v[112:115], v[220:223], a[176:179], v[112:115]\n"
"ds_read_b128 a[204:207], v132 offset:6144\n"
"v_add_f32 v140, v140, v142\n"
"ds_read_b128 a[220:223], v133 offset:6144\n"
"v_mfma_f32_16x16x32_bf16 v[116:119], v[216:219], a[164:167], v[116:119]\n"
"buffer_load_dwordx4 v131, s[20:23], s61 offen offset:3072 lds\n"
"v_mfma_f32_16x16x32_bf16 v[116:119], v[220:223], a[180:183], v[116:119]\n"
"ds_read_b64_tr_b16 v[208:209], v226 offset:40960\n"
"v_add_f32 v140, v140, v143\n"
"v_mfma_f32_16x16x32_bf16 v[120:123], v[216:219], a[168:171], v[120:123]\n"
"s_mov_b64 exec, 0xffffffff\n"
"buffer_load_dword v243, v241, s[28:31], 0 offen\n"
"s_mov_b64 exec, -1\n"
"v_mfma_f32_16x16x32_bf16 v[120:123], v[220:223], a[184:187], v[120:123]\n"
"ds_read_b64_tr_b16 v[210:211], v226 offset:41472\n"
"s_lshl_b32 s15, s14, 10\n"
"v_mfma_f32_16x16x32_bf16 v[124:127], v[216:219], a[172:175], v[124:127]\n"
"ds_read_b64_tr_b16 v[212:213], v226 offset:45056\n"
"s_add_u32 s48, s48, s15\n"
"v_mfma_f32_16x16x32_bf16 v[124:127], v[220:223], a[188:191], v[124:127]\n"
"ds_read_b64_tr_b16 v[214:215], v226 offset:45568\n"
"s_waitcnt lgkmcnt(4)\n"
"v_mfma_f32_16x16x32_bf16 v[0:3], v[192:195], a[192:195], v[0:3]\n"
"ds_read_b64_tr_b16 v[216:217], v227 offset:40960\n"
"s_lshl_b32 s15, s11, 8\n"
"v_mfma_f32_16x16x32_bf16 v[0:3], v[196:199], a[208:211], v[0:3]\n"
"s_add_u32 m0, s58, 16384\n"
"s_mov_b32 s60, 0\n"
"v_mfma_f32_16x16x32_bf16 v[4:7], v[192:195], a[196:199], v[4:7]\n"
"buffer_load_dwordx4 v234, s[28:31], s60 offen offset:0 lds\n"
"v_mfma_f32_16x16x32_bf16 v[4:7], v[196:199], a[212:215], v[4:7]\n"
"ds_read_b64_tr_b16 v[218:219], v227 offset:41472\n"
"s_add_u32 s48, s48, s15\n"
"v_mfma_f32_16x16x32_bf16 v[8:11], v[192:195], a[200:203], v[8:11]\n"
"ds_read_b64_tr_b16 v[220:221], v227 offset:45056\n"
"s_add_u32 s48, s48, 524288\n"
"v_mfma_f32_16x16x32_bf16 v[8:11], v[196:199], a[216:219], v[8:11]\n"
"ds_read_b64_tr_b16 v[222:223], v227 offset:45568\n"
"s_lshl_b32 s15, s11, 11\n"
"v_mfma_f32_16x16x32_bf16 v[12:15], v[192:195], a[204:207], v[12:15]\n"
"buffer_load_dwordx4 v235, s[28:31], s60 offen offset:1024 lds\n"
"v_mfma_f32_16x16x32_bf16 v[12:15], v[196:199], a[220:223], v[12:15]\n"
"ds_read_b64_tr_b16 v[192:193], v224 offset:49152\n"
"v_mfma_f32_16x16x32_bf16 v[16:19], v[200:203], a[192:195], v[16:19]\n"
"ds_read_b64_tr_b16 v[194:195], v224 offset:49664\n"
"s_mov_b32 s20, s68\n"
"v_mfma_f32_16x16x32_bf16 v[16:19], v[204:207], a[208:211], v[16:19]\n"
"ds_read_b64_tr_b16 v[196:197], v224 offset:53248\n"
"s_and_b32 s21, s69, 0xffff\n"
"v_mfma_f32_16x16x32_bf16 v[20:23], v[200:203], a[196:199], v[20:23]\n"
"ds_read_b64_tr_b16 v[198:199], v224 offset:53760\n"
"s_mov_b32 s24, s70\n"
"v_mfma_f32_16x16x32_bf16 v[20:23], v[204:207], a[212:215], v[20:23]\n"
"s_add_u32 m0, s58, 24576\n"
"s_mov_b32 s60, 524288\n"
"v_mfma_f32_16x16x32_bf16 v[24:27], v[200:203], a[200:203], v[24:27]\n"
"buffer_load_dwordx4 v234, s[28:31], s60 offen offset:0 lds\n"
"v_mfma_f32_16x16x32_bf16 v[24:27], v[204:207], a[216:219], v[24:27]\n"
"s_cmp_eq_u32 s16, 2\n"
"s_cselect_b32 s50, 1.0, 0\n"
"s_add_u32 s16, s16, 1\n"
"s_bitcmp1_b32 s16, 1\n"
"s_cselect_b32 s51, 0x3fb8aa3b, 1.0\n"
"v_mfma_f32_16x16x32_bf16 v[28:31], v[200:203], a[204:207], v[28:31]\n"
"ds_read_b64_tr_b16 v[200:201], v225 offset:49152\n"
"s_lshl_b32 s16, s17, 10\n"
"v_mfma_f32_16x16x32_bf16 v[28:31], v[204:207], a[220:223], v[28:31]\n"
"ds_read_b64_tr_b16 v[202:203], v225 offset:49664\n"
"s_waitcnt lgkmcnt(10)\n"
"v_mfma_f32_16x16x32_bf16 v[32:35], v[208:211], a[192:195], v[32:35]\n"
"buffer_load_dwordx4 v235, s[28:31], s60 offen offset:1024 lds\n"
"v_mfma_f32_16x16x32_bf16 v[32:35], v[212:215], a[208:211], v[32:35]\n"
"ds_read_b64_tr_b16 v[204:205], v225 offset:53248\n"
"s_add_u32 s16, s16, 61440\n"
"v_mfma_f32_16x16x32_bf16 v[36:39], v[208:211], a[196:199], v[36:39]\n"
"ds_read_b64_tr_b16 v[206:207], v225 offset:53760\n"
"v_lshl_add_u32 v139, v139, 5, s16\n"
"v_mfma_f32_16x16x32_bf16 v[36:39], v[212:215], a[212:215], v[36:39]\n"
"ds_read_b128 v[144:147], v139 offset:2048\n"
"s_lshl_b32 s17, s11, 10\n"
"v_mfma_f32_16x16x32_bf16 v[40:43], v[208:211], a[200:203], v[40:43]\n"
"ds_read_b128 v[152:155], v139 offset:0\n"
"s_add_u32 s17, s17, 57344\n"
"v_mfma_f32_16x16x32_bf16 v[40:43], v[212:215], a[216:219], v[40:43]\n"
"ds_read_b128 v[148:151], v139 offset:2064\n"
"v_add_u32 v246, s17, v246\n"
"v_mfma_f32_16x16x32_bf16 v[44:47], v[208:211], a[204:207], v[44:47]\n"
"ds_read_b128 v[156:159], v139 offset:16\n"
"v_add_u32 v247, s17, v232\n"
"v_mfma_f32_16x16x32_bf16 v[44:47], v[212:215], a[220:223], v[44:47]\n"
"ds_read_b64_tr_b16 v[208:209], v226 offset:49152\n"
"s_waitcnt lgkmcnt(13)\n"
"v_mfma_f32_16x16x32_bf16 v[48:51], v[216:219], a[192:195], v[48:51]\n"
"ds_read_b64_tr_b16 v[210:211], v226 offset:49664\n"
"v_add_f32 v140, s50, v140\n"
"v_mfma_f32_16x16x32_bf16 v[48:51], v[220:223], a[208:211], v[48:51]\n"
"ds_read_b64_tr_b16 v[212:213], v226 offset:53248\n"
"v_mul_f32 v140, s51, v140\n"
"v_mfma_f32_16x16x32_bf16 v[52:55], v[216:219], a[196:199], v[52:55]\n"
"ds_read_b64_tr_b16 v[214:215], v226 offset:53760\n"
"buffer_store_dword v140, v135, s[40:43], s48 offen sc1\n"
"v_mfma_f32_16x16x32_bf16 v[52:55], v[220:223], a[212:215], v[52:55]\n"
"s_lshl_b32 s48, s10, 13\n"
"s_add_u32 s48, s48, s15\n"
"v_mfma_f32_16x16x32_bf16 v[56:59], v[216:219], a[200:203], v[56:59]\n"
"s_add_u32 s48, s48, 1048576\n"
"s_lshl_b32 s50, s10, 8\n"
"v_mfma_f32_16x16x32_bf16 v[56:59], v[220:223], a[216:219], v[56:59]\n"
"s_waitcnt lgkmcnt(6)\n"
"v_fma_f32 v160, v160, v144, v152\n"
"v_fma_f32 v161, v161, v145, v153\n"
"v_mfma_f32_16x16x32_bf16 v[60:63], v[216:219], a[204:207], v[60:63]\n"
"v_fma_f32 v162, v162, v146, v154\n"
"v_fma_f32 v163, v163, v147, v155\n"
"v_mfma_f32_16x16x32_bf16 v[60:63], v[220:223], a[220:223], v[60:63]\n"
"ds_read_b128 v[144:147], v139 offset:2560\n"
"v_mfma_f32_16x16x32_bf16 v[64:67], v[192:195], a[192:195], v[64:67]\n"
"ds_read_b128 v[152:155], v139 offset:512\n"
"s_waitcnt lgkmcnt(6)\n"
"v_fma_f32 v164, v164, v148, v156\n"
"v_mfma_f32_16x16x32_bf16 v[64:67], v[196:199], a[208:211], v[64:67]\n"
"v_fma_f32 v165, v165, v149, v157\n"
"v_fma_f32 v166, v166, v150, v158\n"
"v_mfma_f32_16x16x32_bf16 v[68:71], v[192:195], a[196:199], v[68:71]\n"
"v_fma_f32 v167, v167, v151, v159\n"
"ds_read_b128 v[148:151], v139 offset:2576\n"
"v_mfma_f32_16x16x32_bf16 v[68:71], v[196:199], a[212:215], v[68:71]\n"
"ds_read_b128 v[156:159], v139 offset:528\n"
"v_cvt_pk_bf16_f32 v160, v160, v161\n"
"v_mfma_f32_16x16x32_bf16 v[72:75], v[192:195], a[200:203], v[72:75]\n"
"ds_read_b64_tr_b16 v[216:217], v227 offset:49152\n"
"v_cvt_pk_bf16_f32 v161, v162, v163\n"
"v_mfma_f32_16x16x32_bf16 v[72:75], v[196:199], a[216:219], v[72:75]\n"
"ds_read_b64_tr_b16 v[218:219], v227 offset:49664\n"
"v_cvt_pk_bf16_f32 v162, v164, v165\n"
"v_mfma_f32_16x16x32_bf16 v[76:79], v[192:195], a[204:207], v[76:79]\n"
"s_waitcnt vmcnt(14)\n"
"s_barrier\n"
"v_mfma_f32_16x16x32_bf16 v[76:79], v[196:199], a[220:223], v[76:79]\n"
"ds_read_b64_tr_b16 v[220:221], v227 offset:53248\n"
"v_mfma_f32_16x16x32_bf16 v[80:83], v[200:203], a[192:195], v[80:83]\n"
"ds_read_b64_tr_b16 v[222:223], v227 offset:53760\n"
"v_cvt_pk_bf16_f32 v163, v166, v167\n"
"v_mfma_f32_16x16x32_bf16 v[80:83], v[204:207], a[208:211], v[80:83]\n"
"ds_write_b128 v246, v[160:163]\n"
"s_waitcnt lgkmcnt(7)\n"
"v_fma_f32 v168, v168, v144, v152\n"
"v_mfma_f32_16x16x32_bf16 v[84:87], v[200:203], a[196:199], v[84:87]\n"
"ds_read_b128 v[160:163], v247\n"
"v_fma_f32 v169, v169, v145, v153\n"
"v_mfma_f32_16x16x32_bf16 v[84:87], v[204:207], a[212:215], v[84:87]\n"
"v_fma_f32 v170, v170, v146, v154\n"
"v_fma_f32 v171, v171, v147, v155\n"
"v_mfma_f32_16x16x32_bf16 v[88:91], v[200:203], a[200:203], v[88:91]\n"
"s_waitcnt lgkmcnt(6)\n"
"v_fma_f32 v172, v172, v148, v156\n"
"v_fma_f32 v173, v173, v149, v157\n"
"v_mfma_f32_16x16x32_bf16 v[88:91], v[204:207], a[216:219], v[88:91]\n"
"v_fma_f32 v174, v174, v150, v158\n"
"v_fma_f32 v175, v175, v151, v159\n"
"v_mfma_f32_16x16x32_bf16 v[92:95], v[200:203], a[204:207], v[92:95]\n"
"v_cvt_pk_bf16_f32 v168, v168, v169\n"
"v_cvt_pk_bf16_f32 v169, v170, v171\n"
"v_mfma_f32_16x16x32_bf16 v[92:95], v[204:207], a[220:223], v[92:95]\n"
"v_cvt_pk_bf16_f32 v170, v172, v173\n"
"v_mfma_f32_16x16x32_bf16 v[96:99], v[208:211], a[192:195], v[96:99]\n"
"v_cvt_pk_bf16_f32 v171, v174, v175\n"
"ds_write_b128 v246, v[168:171]\n"
"v_mfma_f32_16x16x32_bf16 v[96:99], v[212:215], a[208:211], v[96:99]\n"
"ds_read_b128 v[168:171], v247\n"
"s_add_u32 s50, s50, 131072\n"
"v_mfma_f32_16x16x32_bf16 v[100:103], v[208:211], a[196:199], v[100:103]\n"
"ds_read_b64_tr_b16 v[192:193], v224 offset:0\n"
"s_lshl_b32 s17, s14, 1\n"
"v_mfma_f32_16x16x32_bf16 v[100:103], v[212:215], a[212:215], v[100:103]\n"
"ds_read_b64_tr_b16 v[194:195], v224 offset:512\n"
"s_waitcnt lgkmcnt(4)\n"
"buffer_store_dwordx4 v[160:163], v232, s[40:43], s48 offen offset:0 sc1\n"
"v_mfma_f32_16x16x32_bf16 v[104:107], v[208:211], a[200:203], v[104:107]\n"
"ds_read_b64_tr_b16 v[196:197], v224 offset:4096\n"
"s_and_b32 s16, s11, 1\n"
"v_mfma_f32_16x16x32_bf16 v[104:107], v[212:215], a[216:219], v[104:107]\n"
"ds_read_b64_tr_b16 v[198:199], v224 offset:4608\n"
"s_add_u32 s16, s16, s17\n"
"v_mfma_f32_16x16x32_bf16 v[108:111], v[208:211], a[204:207], v[108:111]\n"
"s_waitcnt vmcnt(7)\n"
"ds_read_b128 a[224:227], v132 offset:8192\n"
"s_add_u32 m0, s58, 32768\n"
"ds_read_b128 a[240:243], v133 offset:8192\n"
"v_mfma_f32_16x16x32_bf16 v[108:111], v[212:215], a[220:223], v[108:111]\n"
"ds_read_b128 a[228:231], v132 offset:10240\n"
"ds_read_b128 a[244:247], v133 offset:10240\n"
"v_mfma_f32_16x16x32_bf16 v[112:115], v[216:219], a[192:195], v[112:115]\n"
"ds_read_b128 a[232:235], v132 offset:12288\n"
"s_mov_b32 s60, 1048576\n"
"ds_read_b128 a[248:251], v133 offset:12288\n"
"v_mfma_f32_16x16x32_bf16 v[112:115], v[220:223], a[208:211], v[112:115]\n"
"ds_read_b128 a[236:239], v132 offset:14336\n"
"s_sub_u32 s18, 31, s16\n"
"ds_read_b128 a[252:255], v133 offset:14336\n"
"v_mfma_f32_16x16x32_bf16 v[116:119], v[216:219], a[196:199], v[116:119]\n"
"buffer_load_dwordx4 v234, s[28:31], s60 offen offset:0 lds\n"
"v_mfma_f32_16x16x32_bf16 v[116:119], v[220:223], a[212:215], v[116:119]\n"
"buffer_load_dwordx4 v235, s[28:31], s60 offen offset:1024 lds\n"
"v_mfma_f32_16x16x32_bf16 v[120:123], v[216:219], a[200:203], v[120:123]\n"
"s_waitcnt lgkmcnt(12)\n"
"buffer_store_dwordx4 v[168:171], v232, s[40:43], s48 offen offset:1024 sc1\n"
"ds_read_b64_tr_b16 v[200:201], v225 offset:0\n"
"v_mfma_f32_16x16x32_bf16 v[120:123], v[220:223], a[216:219], v[120:123]\n"
"ds_read_b64_tr_b16 v[202:203], v225 offset:512\n"
"s_lshl_b32 s15, s14, 8\n"
"v_mfma_f32_16x16x32_bf16 v[124:127], v[216:219], a[204:207], v[124:127]\n"
"ds_read_b64_tr_b16 v[204:205], v225 offset:4096\n"
"s_add_u32 s49, s49, 12288\n"
"ds_read_b64_tr_b16 v[206:207], v225 offset:4608\n"
"v_mfma_f32_16x16x32_bf16 v[124:127], v[220:223], a[220:223], v[124:127]\n"
"ds_read_b64_tr_b16 v[208:209], v226 offset:0\n"
"s_waitcnt lgkmcnt(5)\n"
"v_mfma_f32_16x16x32_bf16 v[0:3], v[192:195], a[224:227], v[0:3]\n"
"ds_read_b64_tr_b16 v[210:211], v226 offset:512\n"
"s_mov_b32 s22, 4194304\n"
"v_mfma_f32_16x16x32_bf16 v[0:3], v[196:199], a[240:243], v[0:3]\n"
"ds_read_b64_tr_b16 v[212:213], v226 offset:4096\n"
"s_mov_b32 s23, 0x20000\n"
"v_mfma_f32_16x16x32_bf16 v[4:7], v[192:195], a[228:231], v[4:7]\n"
"s_mov_b64 exec, 0xffffffff\n"
"buffer_load_dword v243, v241, s[36:39], 0 offen\n"
"s_mov_b64 exec, -1\n"
"v_mfma_f32_16x16x32_bf16 v[4:7], v[196:199], a[244:247], v[4:7]\n"
"ds_read_b64_tr_b16 v[214:215], v226 offset:4608\n"
"s_and_b32 s25, s71, 0xffff\n"
"v_mfma_f32_16x16x32_bf16 v[8:11], v[192:195], a[232:235], v[8:11]\n"
"ds_read_b64_tr_b16 v[216:217], v227 offset:0\n"
"s_mov_b32 s26, 1835008\n"
"v_mfma_f32_16x16x32_bf16 v[8:11], v[196:199], a[248:251], v[8:11]\n"
"ds_read_b64_tr_b16 v[218:219], v227 offset:512\n"
"s_mov_b32 s27, 0x20000\n"
"v_mfma_f32_16x16x32_bf16 v[12:15], v[192:195], a[236:239], v[12:15]\n"
"s_add_u32 m0, s58, 40960\n"
"s_mov_b32 s60, 1572864\n"
"v_mfma_f32_16x16x32_bf16 v[12:15], v[196:199], a[252:255], v[12:15]\n"
"ds_read_b64_tr_b16 v[220:221], v227 offset:4096\n"
"s_waitcnt lgkmcnt(7)\n"
"v_mfma_f32_16x16x32_bf16 v[16:19], v[200:203], a[224:227], v[16:19]\n"
"buffer_load_dwordx4 v234, s[28:31], s60 offen offset:0 lds\n"
"v_mfma_f32_16x16x32_bf16 v[16:19], v[204:207], a[240:243], v[16:19]\n"
"buffer_load_dwordx4 v235, s[28:31], s60 offen offset:1024 lds\n"
"v_mfma_f32_16x16x32_bf16 v[20:23], v[200:203], a[228:231], v[20:23]\n"
"ds_read_b64_tr_b16 v[222:223], v227 offset:4608\n"
"s_lshl_b32 s61, s11, 11\n"
"v_mfma_f32_16x16x32_bf16 v[20:23], v[204:207], a[244:247], v[20:23]\n"
"ds_read_b64_tr_b16 v[192:193], v224 offset:8192\n"
"s_mul_i32 s59, s10, 7168\n"
"v_mfma_f32_16x16x32_bf16 v[24:27], v[200:203], a[232:235], v[24:27]\n"
"ds_read_b64_tr_b16 v[194:195], v224 offset:8704\n"
"s_lshr_b32 s96, s10, 3\n"
"v_mfma_f32_16x16x32_bf16 v[24:27], v[204:207], a[248:251], v[24:27]\n"
"ds_read_b64_tr_b16 v[196:197], v224 offset:12288\n"
"s_lshl_b32 s96, s96, 2\n"
"v_mfma_f32_16x16x32_bf16 v[28:31], v[200:203], a[236:239], v[28:31]\n"
"ds_read_b64_tr_b16 v[198:199], v224 offset:12800\n"
"s_add_u32 s96, s96, s11\n"
"v_mfma_f32_16x16x32_bf16 v[28:31], v[204:207], a[252:255], v[28:31]\n"
"s_lshl_b32 s96, s96, 7\n"
"s_waitcnt lgkmcnt(8)\n"
"v_mfma_f32_16x16x32_bf16 v[32:35], v[208:211], a[224:227], v[32:35]\n"
"s_add_u32 s96, s96, 2048\n"
"v_lshl_add_u32 v242, v232, 10, s96\n"
"v_mfma_f32_16x16x32_bf16 v[32:35], v[212:215], a[240:243], v[32:35]\n"
"v_min_u32 v242, Lslow_%=-Lct0_%=, v242\n"
"ds_read_b64_tr_b16 v[200:201], v225 offset:8192\n"
"v_mfma_f32_16x16x32_bf16 v[36:39], v[208:211], a[228:231], v[36:39]\n"
"ds_read_b64_tr_b16 v[202:203], v225 offset:8704\n"
"v_and_b32 v245, 15, v244\n"
"v_mfma_f32_16x16x32_bf16 v[36:39], v[212:215], a[244:247], v[36:39]\n"
"ds_read_b64_tr_b16 v[204:205], v225 offset:12288\n"
"v_lshrrev_b32 v246, 4, v244\n"
"v_mfma_f32_16x16x32_bf16 v[40:43], v[208:211], a[232:235], v[40:43]\n"
"ds_read_b64_tr_b16 v[206:207], v225 offset:12800\n"
"v_lshlrev_b32 v246, 2, v246\n"
"v_mfma_f32_16x16x32_bf16 v[40:43], v[212:215], a[248:251], v[40:43]\n"
"s_cmp_lt_u32 s11, 2\n"
"s_cselect_b32 s68, s18, s16\n"
"s_cselect_b32 s69, s17, s16\n"
"s_cselect_b32 s70, 1, 0\n"
"v_mfma_f32_16x16x32_bf16 v[44:47], v[208:211], a[236:239], v[44:47]\n"
"s_getpc_b64 s[96:97]\n"
"Lct0_%=:\n"
"s_mov_b64 exec, 1\n"
"global_load_dword v243, v242, s[96:97]\n"
"s_mov_b64 exec, -1\n"
"v_mfma_f32_16x16x32_bf16 v[44:47], v[212:215], a[252:255], v[44:47]\n"
"ds_read_b64_tr_b16 v[208:209], v226 offset:8192\n"
"s_waitcnt lgkmcnt(9)\n"
"v_mfma_f32_16x16x32_bf16 v[48:51], v[216:219], a[224:227], v[48:51]\n"
"ds_read_b64_tr_b16 v[210:211], v226 offset:8704\n"
"s_lshl_b32 s51, s68, 11\n"
"v_mfma_f32_16x16x32_bf16 v[48:51], v[220:223], a[240:243], v[48:51]\n"
"ds_read_b64_tr_b16 v[212:213], v226 offset:12288\n"
"s_add_u32 s51, s51, 131072\n"
"v_mfma_f32_16x16x32_bf16 v[52:55], v[216:219], a[228:231], v[52:55]\n"
"ds_read_b64_tr_b16 v[214:215], v226 offset:12800\n"
"s_lshl_b32 s62, s68, 16\n"
"v_mfma_f32_16x16x32_bf16 v[52:55], v[220:223], a[244:247], v[52:55]\n"
"s_add_u32 s62, s62, 1048576\n"
"s_add_u32 s63, s62, 16384\n"
"v_mfma_f32_16x16x32_bf16 v[56:59], v[216:219], a[232:235], v[56:59]\n"
"s_add_u32 s64, s62, 32768\n"
"s_add_u32 s65, s62, 49152\n"
"v_mfma_f32_16x16x32_bf16 v[56:59], v[220:223], a[248:251], v[56:59]\n"
"s_add_u32 s49, s70, 1\n"
"s_max_u32 s69, s69, s49\n"
"v_mfma_f32_16x16x32_bf16 v[60:63], v[216:219], a[236:239], v[60:63]\n"
"ds_read_b64_tr_b16 v[216:217], v227 offset:8192\n"
"s_lshl_b32 s17, s69, 15\n"
"v_mfma_f32_16x16x32_bf16 v[60:63], v[220:223], a[252:255], v[60:63]\n"
"ds_read_b64_tr_b16 v[218:219], v227 offset:8704\n"
"s_waitcnt lgkmcnt(10)\n"
"v_mfma_f32_16x16x32_bf16 v[64:67], v[192:195], a[224:227], v[64:67]\n"
"ds_read_b64_tr_b16 v[220:221], v227 offset:12288\n"
"s_lshl_b32 s18, s18, 15\n"
"v_mfma_f32_16x16x32_bf16 v[64:67], v[196:199], a[240:243], v[64:67]\n"
"ds_read_b64_tr_b16 v[222:223], v227 offset:12800\n"
"s_sub_u32 s16, s18, 32768\n"
"v_mfma_f32_16x16x32_bf16 v[68:71], v[192:195], a[228:231], v[68:71]\n"
"s_cmp_eq_u32 s68, 0\n"
"s_cselect_b32 s35, 0xff800000, 0\n"
"s_cselect_b32 s72, 32768, -1\n"
"s_cmp_lt_u32 s11, 2\n"
"s_cselect_b32 s72, s17, s72\n"
"s_cselect_b32 s73, s18, s16\n"
"s_cselect_b32 s48, s17, s16\n"
"v_mfma_f32_16x16x32_bf16 v[68:71], v[196:199], a[244:247], v[68:71]\n"
"s_and_b32 s18, s12, 1\n"
"s_lshr_b32 s16, s10, 5\n"
"v_mfma_f32_16x16x32_bf16 v[72:75], v[192:195], a[232:235], v[72:75]\n"
"v_sub_u32 v245, v245, v246\n"
"v_mov_b32 v255, s35\n"
"v_mfma_f32_16x16x32_bf16 v[72:75], v[196:199], a[248:251], v[72:75]\n"
"v_mov_b32 v244, 0x3f803f80\n"
"v_add_u32 v233, 0x1000, v232\n"
"v_mfma_f32_16x16x32_bf16 v[76:79], v[192:195], a[236:239], v[76:79]\n"
"s_waitcnt vmcnt(4)\n"
"s_barrier\n"
"v_mfma_f32_16x16x32_bf16 v[76:79], v[196:199], a[252:255], v[76:79]\n"
"ds_read_b64_tr_b16 v[192:193], v224 offset:16384\n"
"s_waitcnt lgkmcnt(9)\n"
"v_mfma_f32_16x16x32_bf16 v[80:83], v[200:203], a[224:227], v[80:83]\n"
"ds_read_b64_tr_b16 v[194:195], v224 offset:16896\n"
"s_sub_u32 s69, s69, 1\n"
"v_mfma_f32_16x16x32_bf16 v[80:83], v[204:207], a[240:243], v[80:83]\n"
"ds_read_b64_tr_b16 v[196:197], v224 offset:20480\n"
"s_sub_u32 s49, 14, s69\n"
"v_mfma_f32_16x16x32_bf16 v[84:87], v[200:203], a[228:231], v[84:87]\n"
"ds_read_b64_tr_b16 v[198:199], v224 offset:20992\n"
"s_min_u32 s34, s68, 1\n"
"v_mfma_f32_16x16x32_bf16 v[84:87], v[204:207], a[244:247], v[84:87]\n"
"s_mov_b32 s71, 0\n"
"s_mov_b32 s77, 32768\n"
"v_mfma_f32_16x16x32_bf16 v[88:91], v[200:203], a[232:235], v[88:91]\n"
"s_add_u32 s74, s71, 24576\n"
"s_add_u32 s71, s71, s77\n"
"v_mfma_f32_16x16x32_bf16 v[88:91], v[204:207], a[248:251], v[88:91]\n"
"s_cmp_eq_u32 s71, s72\n"
"s_cselect_b32 s71, s73, s71\n"
"v_mfma_f32_16x16x32_bf16 v[92:95], v[200:203], a[236:239], v[92:95]\n"
"ds_read_b64_tr_b16 v[200:201], v225 offset:16384\n"
"s_add_u32 s75, s71, 8192\n"
"v_mfma_f32_16x16x32_bf16 v[92:95], v[204:207], a[252:255], v[92:95]\n"
"v_mov_b32 v250, s6\n"
"s_waitcnt lgkmcnt(9)\n"
"v_mfma_f32_16x16x32_bf16 v[96:99], v[208:211], a[224:227], v[96:99]\n"
"v_mov_b32 v251, s7\n"
"ds_read_b64_tr_b16 v[202:203], v225 offset:16896\n"
"v_mfma_f32_16x16x32_bf16 v[96:99], v[212:215], a[240:243], v[96:99]\n"
"ds_read_b64_tr_b16 v[204:205], v225 offset:20480\n"
"s_add_u32 s76, s71, 16384\n"
"v_mfma_f32_16x16x32_bf16 v[100:103], v[208:211], a[228:231], v[100:103]\n"
"ds_read_b64_tr_b16 v[206:207], v225 offset:20992\n"
"v_mfma_f32_16x16x32_bf16 v[100:103], v[212:215], a[244:247], v[100:103]\n"
"v_mfma_f32_16x16x32_bf16 v[104:107], v[208:211], a[232:235], v[104:107]\n"
"v_mfma_f32_16x16x32_bf16 v[104:107], v[212:215], a[248:251], v[104:107]\n"
"buffer_store_dwordx2 v[250:251], v237, s[40:43], s50 offen sc1\n"
"v_mfma_f32_16x16x32_bf16 v[108:111], v[208:211], a[236:239], v[108:111]\n"
"s_add_u32 m0, s58, 49152\n"
"s_mov_b32 s60, 2097152\n"
"v_mfma_f32_16x16x32_bf16 v[108:111], v[212:215], a[252:255], v[108:111]\n"
"ds_read_b64_tr_b16 v[208:209], v226 offset:16384\n"
"s_waitcnt lgkmcnt(9)\n"
"v_mfma_f32_16x16x32_bf16 v[112:115], v[216:219], a[224:227], v[112:115]\n"
"buffer_load_dwordx4 v234, s[28:31], s60 offen offset:0 lds\n"
"v_mfma_f32_16x16x32_bf16 v[112:115], v[220:223], a[240:243], v[112:115]\n"
"buffer_load_dwordx4 v235, s[28:31], s60 offen offset:1024 lds\n"
"v_mfma_f32_16x16x32_bf16 v[116:119], v[216:219], a[228:231], v[116:119]\n"
"ds_read_b64_tr_b16 v[210:211], v226 offset:16896\n"
"v_mfma_f32_16x16x32_bf16 v[116:119], v[220:223], a[244:247], v[116:119]\n"
"ds_read_b64_tr_b16 v[212:213], v226 offset:20480\n"
"v_mfma_f32_16x16x32_bf16 v[120:123], v[216:219], a[232:235], v[120:123]\n"
"ds_read_b64_tr_b16 v[214:215], v226 offset:20992\n"
"v_mfma_f32_16x16x32_bf16 v[120:123], v[220:223], a[248:251], v[120:123]\n"
"v_mfma_f32_16x16x32_bf16 v[124:127], v[216:219], a[236:239], v[124:127]\n"
"ds_read_b64_tr_b16 v[216:217], v227 offset:16384\n"
"v_mfma_f32_16x16x32_bf16 v[124:127], v[220:223], a[252:255], v[124:127]\n"
"ds_read_b64_tr_b16 v[218:219], v227 offset:16896\n"
"s_waitcnt lgkmcnt(10)\n"
"v_mfma_f32_16x16x32_bf16 v[128:131], a[0:3], v[192:195], 0\n"
"ds_read_b64_tr_b16 v[220:221], v227 offset:20480\n"
"v_mfma_f32_16x16x32_bf16 v[128:131], a[16:19], v[196:199], v[128:131]\n"
"ds_read_b64_tr_b16 v[222:223], v227 offset:20992\n"
"v_mfma_f32_16x16x32_bf16 v[132:135], a[4:7], v[192:195], 0\n"
"v_mfma_f32_16x16x32_bf16 v[132:135], a[20:23], v[196:199], v[132:135]\n"
"v_mfma_f32_16x16x32_bf16 v[136:139], a[8:11], v[192:195], 0\n"
"v_cvt_pk_bf16_f32 v0, v0, v1\n"
"v_cvt_pk_bf16_f32 v1, v2, v3\n"
"v_mfma_f32_16x16x32_bf16 v[136:139], a[24:27], v[196:199], v[136:139]\n"
"v_cvt_pk_bf16_f32 v2, v16, v17\n"
"v_cvt_pk_bf16_f32 v3, v18, v19\n"
"v_mfma_f32_16x16x32_bf16 v[140:143], a[12:15], v[192:195], 0\n"
"s_add_u32 m0, s58, 0\n"
"s_mov_b32 s60, 2621440\n"
"v_mfma_f32_16x16x32_bf16 v[140:143], a[28:31], v[196:199], v[140:143]\n"
"ds_read_b64_tr_b16 v[192:193], v224 offset:24576\n"
"s_waitcnt lgkmcnt(9)\n"
"v_mfma_f32_16x16x32_bf16 v[144:147], a[0:3], v[200:203], 0\n"
"buffer_load_dwordx4 v234, s[28:31], s60 offen offset:0 lds\n"
"v_mfma_f32_16x16x32_bf16 v[144:147], a[16:19], v[204:207], v[144:147]\n"
"buffer_load_dwordx4 v235, s[28:31], s60 offen offset:1024 lds\n"
"v_mfma_f32_16x16x32_bf16 v[148:151], a[4:7], v[200:203], 0\n"
"ds_read_b64_tr_b16 v[194:195], v224 offset:25088\n"
"s_add_u32 s67, s66, 0\n"
"v_mfma_f32_16x16x32_bf16 v[148:151], a[20:23], v[204:207], v[148:151]\n"
"ds_read_b64_tr_b16 v[196:197], v224 offset:28672\n"
"v_mfma_f32_16x16x32_bf16 v[152:155], a[8:11], v[200:203], 0\n"
"ds_read_b64_tr_b16 v[198:199], v224 offset:29184\n"
"v_mfma_f32_16x16x32_bf16 v[152:155], a[24:27], v[204:207], v[152:155]\n"
"buffer_store_dwordx4 v[0:3], v232, s[44:47], s67 offen sc1\n"
"v_mfma_f32_16x16x32_bf16 v[156:159], a[12:15], v[200:203], 0\n"
"ds_read_b64_tr_b16 v[200:201], v225 offset:24576\n"
"v_mfma_f32_16x16x32_bf16 v[156:159], a[28:31], v[204:207], v[156:159]\n"
"ds_read_b64_tr_b16 v[202:203], v225 offset:25088\n"
"s_waitcnt lgkmcnt(10)\n"
"v_mfma_f32_16x16x32_bf16 v[160:163], a[0:3], v[208:211], 0\n"
"ds_read_b64_tr_b16 v[204:205], v225 offset:28672\n"
"v_mfma_f32_16x16x32_bf16 v[160:163], a[16:19], v[212:215], v[160:163]\n"
"ds_read_b64_tr_b16 v[206:207], v225 offset:29184\n"
"v_mfma_f32_16x16x32_bf16 v[164:167], a[4:7], v[208:211], 0\n"
"v_mfma_f32_16x16x32_bf16 v[164:167], a[20:23], v[212:215], v[164:167]\n"
"v_mfma_f32_16x16x32_bf16 v[168:171], a[8:11], v[208:211], 0\n"
"v_mfma_f32_16x16x32_bf16 v[168:171], a[24:27], v[212:215], v[168:171]\n"
"v_mfma_f32_16x16x32_bf16 v[172:175], a[12:15], v[208:211], 0\n"
"s_add_u32 m0, s58, 65536\n"
"s_mov_b32 s60, 3145728\n"
"v_mfma_f32_16x16x32_bf16 v[172:175], a[28:31], v[212:215], v[172:175]\n"
"ds_read_b64_tr_b16 v[208:209], v226 offset:24576\n"
"s_waitcnt lgkmcnt(9)\n"
"v_mfma_f32_16x16x32_bf16 v[176:179], a[0:3], v[216:219], 0\n"
"buffer_load_dwordx4 v234, s[28:31], s60 offen offset:0 lds\n"
"v_mfma_f32_16x16x32_bf16 v[176:179], a[16:19], v[220:223], v[176:179]\n"
"buffer_load_dwordx4 v235, s[28:31], s60 offen offset:1024 lds\n"
"v_mfma_f32_16x16x32_bf16 v[180:183], a[4:7], v[216:219], 0\n"
"ds_read_b64_tr_b16 v[210:211], v226 offset:25088\n"
"v_cvt_pk_bf16_f32 v4, v4, v5\n"
"v_mfma_f32_16x16x32_bf16 v[180:183], a[20:23], v[220:223], v[180:183]\n"
"ds_read_b64_tr_b16 v[212:213], v226 offset:28672\n"
"v_cvt_pk_bf16_f32 v5, v6, v7\n"
"v_mfma_f32_16x16x32_bf16 v[184:187], a[8:11], v[216:219], 0\n"
"ds_read_b64_tr_b16 v[214:215], v226 offset:29184\n"
"v_cvt_pk_bf16_f32 v6, v20, v21\n"
"v_mfma_f32_16x16x32_bf16 v[184:187], a[24:27], v[220:223], v[184:187]\n"
"v_cvt_pk_bf16_f32 v7, v22, v23\n"
"s_add_u32 s67, s66, 4096\n"
"v_mfma_f32_16x16x32_bf16 v[188:191], a[12:15], v[216:219], 0\n"
"ds_read_b64_tr_b16 v[216:217], v227 offset:24576\n"
"v_mfma_f32_16x16x32_bf16 v[188:191], a[28:31], v[220:223], v[188:191]\n"
"ds_read_b64_tr_b16 v[218:219], v227 offset:25088\n"
"s_waitcnt lgkmcnt(10)\n"
"v_mfma_f32_16x16x32_bf16 v[128:131], a[32:35], v[192:195], v[128:131]\n"
"ds_read_b64_tr_b16 v[220:221], v227 offset:28672\n"
"v_mfma_f32_16x16x32_bf16 v[128:131], a[48:51], v[196:199], v[128:131]\n"
"ds_read_b64_tr_b16 v[222:223], v227 offset:29184\n"
"v_mfma_f32_16x16x32_bf16 v[132:135], a[36:39], v[192:195], v[132:135]\n"
"buffer_store_dwordx4 v[4:7], v232, s[44:47], s67 offen sc1\n"
"v_mfma_f32_16x16x32_bf16 v[132:135], a[52:55], v[196:199], v[132:135]\n"
"v_mfma_f32_16x16x32_bf16 v[136:139], a[40:43], v[192:195], v[136:139]\n"
"v_mfma_f32_16x16x32_bf16 v[136:139], a[56:59], v[196:199], v[136:139]\n"
"v_mfma_f32_16x16x32_bf16 v[140:143], a[44:47], v[192:195], v[140:143]\n"
"s_waitcnt vmcnt(10)\n"
"s_barrier\n"
"v_mfma_f32_16x16x32_bf16 v[140:143], a[60:63], v[196:199], v[140:143]\n"
"s_add_u32 m0, s58, 73728\n"
"s_waitcnt lgkmcnt(8)\n"
"v_mfma_f32_16x16x32_bf16 v[144:147], a[32:35], v[200:203], v[144:147]\n"
"s_mov_b32 s60, 3670016\n"
"ds_read_b64_tr_b16 v[192:193], v224 offset:32768\n"
"v_mfma_f32_16x16x32_bf16 v[144:147], a[48:51], v[204:207], v[144:147]\n"
"buffer_load_dwordx4 v234, s[28:31], s60 offen offset:0 lds\n"
"v_mfma_f32_16x16x32_bf16 v[148:151], a[36:39], v[200:203], v[148:151]\n"
"buffer_load_dwordx4 v235, s[28:31], s60 offen offset:1024 lds\n"
"v_mfma_f32_16x16x32_bf16 v[148:151], a[52:55], v[204:207], v[148:151]\n"
"ds_read_b64_tr_b16 v[194:195], v224 offset:33280\n"
"v_mfma_f32_16x16x32_bf16 v[152:155], a[40:43], v[200:203], v[152:155]\n"
"ds_read_b64_tr_b16 v[196:197], v224 offset:36864\n"
"v_cvt_pk_bf16_f32 v8, v8, v9\n"
"v_mfma_f32_16x16x32_bf16 v[152:155], a[56:59], v[204:207], v[152:155]\n"
"ds_read_b64_tr_b16 v[198:199], v224 offset:37376\n"
"v_cvt_pk_bf16_f32 v9, v10, v11\n"
"v_mfma_f32_16x16x32_bf16 v[156:159], a[44:47], v[200:203], v[156:159]\n"
"ds_read_b64_tr_b16 v[200:201], v225 offset:32768\n"
"v_cvt_pk_bf16_f32 v10, v24, v25\n"
"v_mfma_f32_16x16x32_bf16 v[156:159], a[60:63], v[204:207], v[156:159]\n"
"ds_read_b64_tr_b16 v[202:203], v225 offset:33280\n"
"s_waitcnt lgkmcnt(10)\n"
"v_mfma_f32_16x16x32_bf16 v[160:163], a[32:35], v[208:211], v[160:163]\n"
"ds_read_b64_tr_b16 v[204:205], v225 offset:36864\n"
"v_cvt_pk_bf16_f32 v11, v26, v27\n"
"v_mfma_f32_16x16x32_bf16 v[160:163], a[48:51], v[212:215], v[160:163]\n"
"ds_read_b64_tr_b16 v[206:207], v225 offset:37376\n"
"s_add_u32 s67, s66, 16384\n"
"v_mfma_f32_16x16x32_bf16 v[164:167], a[36:39], v[208:211], v[164:167]\n"
"buffer_store_dwordx4 v[8:11], v232, s[44:47], s67 offen sc1\n"
"v_mfma_f32_16x16x32_bf16 v[164:167], a[52:55], v[212:215], v[164:167]\n"
"v_mfma_f32_16x16x32_bf16 v[168:171], a[40:43], v[208:211], v[168:171]\n"
"v_mfma_f32_16x16x32_bf16 v[168:171], a[56:59], v[212:215], v[168:171]\n"
"v_mfma_f32_16x16x32_bf16 v[172:175], a[44:47], v[208:211], v[172:175]\n"
"s_add_u32 m0, s58, 81920\n"
"s_mov_b32 s60, 128\n"
"v_mfma_f32_16x16x32_bf16 v[172:175], a[60:63], v[212:215], v[172:175]\n"
"ds_read_b64_tr_b16 v[208:209], v226 offset:32768\n"
"s_waitcnt lgkmcnt(9)\n"
"v_mfma_f32_16x16x32_bf16 v[176:179], a[32:35], v[216:219], v[176:179]\n"
"buffer_load_dwordx4 v234, s[28:31], s60 offen offset:0 lds\n"
"v_mfma_f32_16x16x32_bf16 v[176:179], a[48:51], v[220:223], v[176:179]\n"
"buffer_load_dwordx4 v235, s[28:31], s60 offen offset:1024 lds\n"
"v_mfma_f32_16x16x32_bf16 v[180:183], a[36:39], v[216:219], v[180:183]\n"
"ds_read_b64_tr_b16 v[210:211], v226 offset:33280\n"
"v_mfma_f32_16x16x32_bf16 v[180:183], a[52:55], v[220:223], v[180:183]\n"
"ds_read_b64_tr_b16 v[212:213], v226 offset:36864\n"
"v_mfma_f32_16x16x32_bf16 v[184:187], a[40:43], v[216:219], v[184:187]\n"
"ds_read_b64_tr_b16 v[214:215], v226 offset:37376\n"
"v_mfma_f32_16x16x32_bf16 v[184:187], a[56:59], v[220:223], v[184:187]\n"
"v_mfma_f32_16x16x32_bf16 v[188:191], a[44:47], v[216:219], v[188:191]\n"
"ds_read_b64_tr_b16 v[216:217], v227 offset:32768\n"
"v_mfma_f32_16x16x32_bf16 v[188:191], a[60:63], v[220:223], v[188:191]\n"
"ds_read_b64_tr_b16 v[218:219], v227 offset:33280\n"
"s_waitcnt lgkmcnt(10)\n"
"v_mfma_f32_16x16x32_bf16 v[128:131], a[64:67], v[192:195], v[128:131]\n"
"ds_read_b64_tr_b16 v[220:221], v227 offset:36864\n"
"v_cvt_pk_bf16_f32 v12, v12, v13\n"
"v_mfma_f32_16x16x32_bf16 v[128:131], a[80:83], v[196:199], v[128:131]\n"
"ds_read_b64_tr_b16 v[222:223], v227 offset:37376\n"
"v_cvt_pk_bf16_f32 v13, v14, v15\n"
"v_mfma_f32_16x16x32_bf16 v[132:135], a[68:71], v[192:195], v[132:135]\n"
"v_cvt_pk_bf16_f32 v14, v28, v29\n"
"v_cvt_pk_bf16_f32 v15, v30, v31\n"
"v_mfma_f32_16x16x32_bf16 v[132:135], a[84:87], v[196:199], v[132:135]\n"
"s_add_u32 s67, s66, 20480\n"
"v_mfma_f32_16x16x32_bf16 v[136:139], a[72:75], v[192:195], v[136:139]\n"
"buffer_store_dwordx4 v[12:15], v232, s[44:47], s67 offen sc1\n"
"v_mfma_f32_16x16x32_bf16 v[136:139], a[88:91], v[196:199], v[136:139]\n"
"v_mfma_f32_16x16x32_bf16 v[140:143], a[76:79], v[192:195], v[140:143]\n"
"s_add_u32 m0, s58, 90112\n"
"s_mov_b32 s60, 524416\n"
"v_mfma_f32_16x16x32_bf16 v[140:143], a[92:95], v[196:199], v[140:143]\n"
"ds_read_b64_tr_b16 v[192:193], v224 offset:40960\n"
"s_waitcnt lgkmcnt(9)\n"
"v_mfma_f32_16x16x32_bf16 v[144:147], a[64:67], v[200:203], v[144:147]\n"
"buffer_load_dwordx4 v234, s[28:31], s60 offen offset:0 lds\n"
"v_mfma_f32_16x16x32_bf16 v[144:147], a[80:83], v[204:207], v[144:147]\n"
"buffer_load_dwordx4 v235, s[28:31], s60 offen offset:1024 lds\n"
"v_mfma_f32_16x16x32_bf16 v[148:151], a[68:71], v[200:203], v[148:151]\n"
"ds_read_b64_tr_b16 v[194:195], v224 offset:41472\n"
"v_mfma_f32_16x16x32_bf16 v[148:151], a[84:87], v[204:207], v[148:151]\n"
"ds_read_b64_tr_b16 v[196:197], v224 offset:45056\n"
"v_mfma_f32_16x16x32_bf16 v[152:155], a[72:75], v[200:203], v[152:155]\n"
"ds_read_b64_tr_b16 v[198:199], v224 offset:45568\n"
"v_mfma_f32_16x16x32_bf16 v[152:155], a[88:91], v[204:207], v[152:155]\n"
"v_mfma_f32_16x16x32_bf16 v[156:159], a[76:79], v[200:203], v[156:159]\n"
"ds_read_b64_tr_b16 v[200:201], v225 offset:40960\n"
"v_mfma_f32_16x16x32_bf16 v[156:159], a[92:95], v[204:207], v[156:159]\n"
"ds_read_b64_tr_b16 v[202:203], v225 offset:41472\n"
"s_waitcnt lgkmcnt(10)\n"
"v_mfma_f32_16x16x32_bf16 v[160:163], a[64:67], v[208:211], v[160:163]\n"
"ds_read_b64_tr_b16 v[204:205], v225 offset:45056\n"
"v_mfma_f32_16x16x32_bf16 v[160:163], a[80:83], v[212:215], v[160:163]\n"
"ds_read_b64_tr_b16 v[206:207], v225 offset:45568\n"
"v_mfma_f32_16x16x32_bf16 v[164:167], a[68:71], v[208:211], v[164:167]\n"
"v_mfma_f32_16x16x32_bf16 v[164:167], a[84:87], v[212:215], v[164:167]\n"
"v_mfma_f32_16x16x32_bf16 v[168:171], a[72:75], v[208:211], v[168:171]\n"
"v_cvt_pk_bf16_f32 v32, v32, v33\n"
"v_cvt_pk_bf16_f32 v33, v34, v35\n"
"v_mfma_f32_16x16x32_bf16 v[168:171], a[88:91], v[212:215], v[168:171]\n"
"v_cvt_pk_bf16_f32 v34, v48, v49\n"
"v_cvt_pk_bf16_f32 v35, v50, v51\n"
"v_mfma_f32_16x16x32_bf16 v[172:175], a[76:79], v[208:211], v[172:175]\n"
"s_add_u32 m0, s58, 98304\n"
"s_mov_b32 s60, 1048704\n"
"v_mfma_f32_16x16x32_bf16 v[172:175], a[92:95], v[212:215], v[172:175]\n"
"ds_read_b64_tr_b16 v[208:209], v226 offset:40960\n"
"s_waitcnt lgkmcnt(9)\n"
"v_mfma_f32_16x16x32_bf16 v[176:179], a[64:67], v[216:219], v[176:179]\n"
"buffer_load_dwordx4 v234, s[28:31], s60 offen offset:0 lds\n"
"v_mfma_f32_16x16x32_bf16 v[176:179], a[80:83], v[220:223], v[176:179]\n"
"buffer_load_dwordx4 v235, s[28:31], s60 offen offset:1024 lds\n"
"v_mfma_f32_16x16x32_bf16 v[180:183], a[68:71], v[216:219], v[180:183]\n"
"ds_read_b64_tr_b16 v[210:211], v226 offset:41472\n"
"s_add_u32 s67, s66, 1024\n"
"v_mfma_f32_16x16x32_bf16 v[180:183], a[84:87], v[220:223], v[180:183]\n"
"ds_read_b64_tr_b16 v[212:213], v226 offset:45056\n"
"v_mfma_f32_16x16x32_bf16 v[184:187], a[72:75], v[216:219], v[184:187]\n"
"ds_read_b64_tr_b16 v[214:215], v226 offset:45568\n"
"v_mfma_f32_16x16x32_bf16 v[184:187], a[88:91], v[220:223], v[184:187]\n"
"buffer_store_dwordx4 v[32:35], v232, s[44:47], s67 offen sc1\n"
"v_mfma_f32_16x16x32_bf16 v[188:191], a[76:79], v[216:219], v[188:191]\n"
"ds_read_b64_tr_b16 v[216:217], v227 offset:40960\n"
"v_mfma_f32_16x16x32_bf16 v[188:191], a[92:95], v[220:223], v[188:191]\n"
"ds_read_b64_tr_b16 v[218:219], v227 offset:41472\n"
"s_waitcnt lgkmcnt(10)\n"
"v_mfma_f32_16x16x32_bf16 v[128:131], a[96:99], v[192:195], v[128:131]\n"
"ds_read_b64_tr_b16 v[220:221], v227 offset:45056\n"
"v_mfma_f32_16x16x32_bf16 v[128:131], a[112:115], v[196:199], v[128:131]\n"
"ds_read_b64_tr_b16 v[222:223], v227 offset:45568\n"
"v_mfma_f32_16x16x32_bf16 v[132:135], a[100:103], v[192:195], v[132:135]\n"
"v_mfma_f32_16x16x32_bf16 v[132:135], a[116:119], v[196:199], v[132:135]\n"
"v_mfma_f32_16x16x32_bf16 v[136:139], a[104:107], v[192:195], v[136:139]\n"
"v_mfma_f32_16x16x32_bf16 v[136:139], a[120:123], v[196:199], v[136:139]\n"
"v_mfma_f32_16x16x32_bf16 v[140:143], a[108:111], v[192:195], v[140:143]\n"
"s_waitcnt vmcnt(9)\n"
"s_barrier\n"
"v_mfma_f32_16x16x32_bf16 v[140:143], a[124:127], v[196:199], v[140:143]\n"
"s_add_u32 m0, s58, 106496\n"
"s_waitcnt lgkmcnt(8)\n"
"v_mfma_f32_16x16x32_bf16 v[144:147], a[96:99], v[200:203], v[144:147]\n"
"s_mov_b32 s60, 1572992\n"
"ds_read_b64_tr_b16 v[192:193], v224 offset:49152\n"
"v_mfma_f32_16x16x32_bf16 v[144:147], a[112:115], v[204:207], v[144:147]\n"
"buffer_load_dwordx4 v234, s[28:31], s60 offen offset:0 lds\n"
"v_mfma_f32_16x16x32_bf16 v[148:151], a[100:103], v[200:203], v[148:151]\n"
"buffer_load_dwordx4 v235, s[28:31], s60 offen offset:1024 lds\n"
"v_mfma_f32_16x16x32_bf16 v[148:151], a[116:119], v[204:207], v[148:151]\n"
"ds_read_b64_tr_b16 v[194:195], v224 offset:49664\n"
"v_cvt_pk_bf16_f32 v36, v36, v37\n"
"v_mfma_f32_16x16x32_bf16 v[152:155], a[104:107], v[200:203], v[152:155]\n"
"ds_read_b64_tr_b16 v[196:197], v224 offset:53248\n"
"v_cvt_pk_bf16_f32 v37, v38, v39\n"
"v_mfma_f32_16x16x32_bf16 v[152:155], a[120:123], v[204:207], v[152:155]\n"
"ds_read_b64_tr_b16 v[198:199], v224 offset:53760\n"
"v_cvt_pk_bf16_f32 v38, v52, v53\n"
"v_mfma_f32_16x16x32_bf16 v[156:159], a[108:111], v[200:203], v[156:159]\n"
"ds_read_b64_tr_b16 v[200:201], v225 offset:49152\n"
"v_cvt_pk_bf16_f32 v39, v54, v55\n"
"v_mfma_f32_16x16x32_bf16 v[156:159], a[124:127], v[204:207], v[156:159]\n"
"ds_read_b64_tr_b16 v[202:203], v225 offset:49664\n"
"s_waitcnt lgkmcnt(10)\n"
"v_mfma_f32_16x16x32_bf16 v[160:163], a[96:99], v[208:211], v[160:163]\n"
"ds_read_b64_tr_b16 v[204:205], v225 offset:53248\n"
"s_add_u32 s67, s66, 5120\n"
"v_mfma_f32_16x16x32_bf16 v[160:163], a[112:115], v[212:215], v[160:163]\n"
"ds_read_b64_tr_b16 v[206:207], v225 offset:53760\n"
"v_mfma_f32_16x16x32_bf16 v[164:167], a[100:103], v[208:211], v[164:167]\n"
"buffer_store_dwordx4 v[36:39], v232, s[44:47], s67 offen sc1\n"
"v_mfma_f32_16x16x32_bf16 v[164:167], a[116:119], v[212:215], v[164:167]\n"
"v_mfma_f32_16x16x32_bf16 v[168:171], a[104:107], v[208:211], v[168:171]\n"
"v_mfma_f32_16x16x32_bf16 v[168:171], a[120:123], v[212:215], v[168:171]\n"
"v_mfma_f32_16x16x32_bf16 v[172:175], a[108:111], v[208:211], v[172:175]\n"
"s_add_u32 m0, s58, 114688\n"
"s_mov_b32 s60, 2097280\n"
"v_mfma_f32_16x16x32_bf16 v[172:175], a[124:127], v[212:215], v[172:175]\n"
"ds_read_b64_tr_b16 v[208:209], v226 offset:49152\n"
"s_waitcnt lgkmcnt(9)\n"
"v_mfma_f32_16x16x32_bf16 v[176:179], a[96:99], v[216:219], v[176:179]\n"
"buffer_load_dwordx4 v234, s[28:31], s60 offen offset:0 lds\n"
"v_mfma_f32_16x16x32_bf16 v[176:179], a[112:115], v[220:223], v[176:179]\n"
"buffer_load_dwordx4 v235, s[28:31], s60 offen offset:1024 lds\n"
"v_mfma_f32_16x16x32_bf16 v[180:183], a[100:103], v[216:219], v[180:183]\n"
"ds_read_b64_tr_b16 v[210:211], v226 offset:49664\n"
"v_mfma_f32_16x16x32_bf16 v[180:183], a[116:119], v[220:223], v[180:183]\n"
"ds_read_b64_tr_b16 v[212:213], v226 offset:53248\n"
"v_mfma_f32_16x16x32_bf16 v[184:187], a[104:107], v[216:219], v[184:187]\n"
"ds_read_b64_tr_b16 v[214:215], v226 offset:53760\n"
"v_cvt_pk_bf16_f32 v40, v40, v41\n"
"v_mfma_f32_16x16x32_bf16 v[184:187], a[120:123], v[220:223], v[184:187]\n"
"v_cvt_pk_bf16_f32 v41, v42, v43\n"
"v_cvt_pk_bf16_f32 v42, v56, v57\n"
"v_mfma_f32_16x16x32_bf16 v[188:191], a[108:111], v[216:219], v[188:191]\n"
"ds_read_b64_tr_b16 v[216:217], v227 offset:49152\n"
"v_cvt_pk_bf16_f32 v43, v58, v59\n"
"v_mfma_f32_16x16x32_bf16 v[188:191], a[124:127], v[220:223], v[188:191]\n"
"ds_read_b64_tr_b16 v[218:219], v227 offset:49664\n"
"s_waitcnt lgkmcnt(10)\n"
"v_mfma_f32_16x16x32_bf16 v[128:131], a[128:131], v[192:195], v[128:131]\n"
"ds_read_b64_tr_b16 v[220:221], v227 offset:53248\n"
"s_add_u32 s67, s66, 17408\n"
"v_mfma_f32_16x16x32_bf16 v[128:131], a[144:147], v[196:199], v[128:131]\n"
"ds_read_b64_tr_b16 v[222:223], v227 offset:53760\n"
"v_mfma_f32_16x16x32_bf16 v[132:135], a[132:135], v[192:195], v[132:135]\n"
"buffer_store_dwordx4 v[40:43], v232, s[44:47], s67 offen sc1\n"
"v_mfma_f32_16x16x32_bf16 v[132:135], a[148:151], v[196:199], v[132:135]\n"
"v_mfma_f32_16x16x32_bf16 v[136:139], a[136:139], v[192:195], v[136:139]\n"
"v_mfma_f32_16x16x32_bf16 v[136:139], a[152:155], v[196:199], v[136:139]\n"
"v_mfma_f32_16x16x32_bf16 v[140:143], a[140:143], v[192:195], v[140:143]\n"
"s_add_u32 m0, s58, 8192\n"
"s_mov_b32 s60, 2621568\n"
"v_mfma_f32_16x16x32_bf16 v[140:143], a[156:159], v[196:199], v[140:143]\n"
"ds_read_b64_tr_b16 v[192:193], v224 offset:0\n"
"s_waitcnt lgkmcnt(9)\n"
"v_mfma_f32_16x16x32_bf16 v[144:147], a[128:131], v[200:203], v[144:147]\n"
"buffer_load_dwordx4 v234, s[28:31], s60 offen offset:0 lds\n"
"v_mfma_f32_16x16x32_bf16 v[144:147], a[144:147], v[204:207], v[144:147]\n"
"buffer_load_dwordx4 v235, s[28:31], s60 offen offset:1024 lds\n"
"v_mfma_f32_16x16x32_bf16 v[148:151], a[132:135], v[200:203], v[148:151]\n"
"ds_read_b64_tr_b16 v[194:195], v224 offset:512\n"
"v_mfma_f32_16x16x32_bf16 v[148:151], a[148:151], v[204:207], v[148:151]\n"
"ds_read_b64_tr_b16 v[196:197], v224 offset:4096\n"
"v_mfma_f32_16x16x32_bf16 v[152:155], a[136:139], v[200:203], v[152:155]\n"
"ds_read_b64_tr_b16 v[198:199], v224 offset:4608\n"
"v_mfma_f32_16x16x32_bf16 v[152:155], a[152:155], v[204:207], v[152:155]\n"
"v_mfma_f32_16x16x32_bf16 v[156:159], a[140:143], v[200:203], v[156:159]\n"
"ds_read_b64_tr_b16 v[200:201], v225 offset:0\n"
"v_mfma_f32_16x16x32_bf16 v[156:159], a[156:159], v[204:207], v[156:159]\n"
"ds_read_b64_tr_b16 v[202:203], v225 offset:512\n"
"s_waitcnt lgkmcnt(10)\n"
"v_mfma_f32_16x16x32_bf16 v[160:163], a[128:131], v[208:211], v[160:163]\n"
"ds_read_b64_tr_b16 v[204:205], v225 offset:4096\n"
"v_cvt_pk_bf16_f32 v44, v44, v45\n"
"v_mfma_f32_16x16x32_bf16 v[160:163], a[144:147], v[212:215], v[160:163]\n"
"ds_read_b64_tr_b16 v[206:207], v225 offset:4608\n"
"v_cvt_pk_bf16_f32 v45, v46, v47\n"
"v_mfma_f32_16x16x32_bf16 v[164:167], a[132:135], v[208:211], v[164:167]\n"
"v_cvt_pk_bf16_f32 v46, v60, v61\n"
"v_cvt_pk_bf16_f32 v47, v62, v63\n"
"v_mfma_f32_16x16x32_bf16 v[164:167], a[148:151], v[212:215], v[164:167]\n"
"s_add_u32 s67, s66, 21504\n"
"v_mfma_f32_16x16x32_bf16 v[168:171], a[136:139], v[208:211], v[168:171]\n"
"buffer_store_dwordx4 v[44:47], v232, s[44:47], s67 offen sc1\n"
"v_mfma_f32_16x16x32_bf16 v[168:171], a[152:155], v[212:215], v[168:171]\n"
"v_mfma_f32_16x16x32_bf16 v[172:175], a[140:143], v[208:211], v[172:175]\n"
"s_add_u32 m0, s58, 16384\n"
"s_mov_b32 s60, 3145856\n"
"v_mfma_f32_16x16x32_bf16 v[172:175], a[156:159], v[212:215], v[172:175]\n"
"ds_read_b64_tr_b16 v[208:209], v226 offset:0\n"
"s_waitcnt lgkmcnt(9)\n"
"v_mfma_f32_16x16x32_bf16 v[176:179], a[128:131], v[216:219], v[176:179]\n"
"buffer_load_dwordx4 v234, s[28:31], s60 offen offset:0 lds\n"
"v_mfma_f32_16x16x32_bf16 v[176:179], a[144:147], v[220:223], v[176:179]\n"
"buffer_load_dwordx4 v235, s[28:31], s60 offen offset:1024 lds\n"
"v_mfma_f32_16x16x32_bf16 v[180:183], a[132:135], v[216:219], v[180:183]\n"
"ds_read_b64_tr_b16 v[210:211], v226 offset:512\n"
"v_mfma_f32_16x16x32_bf16 v[180:183], a[148:151], v[220:223], v[180:183]\n"
"ds_read_b64_tr_b16 v[212:213], v226 offset:4096\n"
"v_mfma_f32_16x16x32_bf16 v[184:187], a[136:139], v[216:219], v[184:187]\n"
"ds_read_b64_tr_b16 v[214:215], v226 offset:4608\n"
"v_mfma_f32_16x16x32_bf16 v[184:187], a[152:155], v[220:223], v[184:187]\n"
"v_mfma_f32_16x16x32_bf16 v[188:191], a[140:143], v[216:219], v[188:191]\n"
"ds_read_b64_tr_b16 v[216:217], v227 offset:0\n"
"v_mfma_f32_16x16x32_bf16 v[188:191], a[156:159], v[220:223], v[188:191]\n"
"ds_read_b64_tr_b16 v[218:219], v227 offset:512\n"
"s_waitcnt lgkmcnt(10)\n"
"v_mfma_f32_16x16x32_bf16 v[128:131], a[160:163], v[192:195], v[128:131]\n"
"ds_read_b64_tr_b16 v[220:221], v227 offset:4096\n"
"v_mfma_f32_16x16x32_bf16 v[128:131], a[176:179], v[196:199], v[128:131]\n"
"ds_read_b64_tr_b16 v[222:223], v227 offset:4608\n"
"v_mfma_f32_16x16x32_bf16 v[132:135], a[164:167], v[192:195], v[132:135]\n"
"v_mfma_f32_16x16x32_bf16 v[132:135], a[180:183], v[196:199], v[132:135]\n"
"v_mfma_f32_16x16x32_bf16 v[136:139], a[168:171], v[192:195], v[136:139]\n"
"v_cvt_pk_bf16_f32 v64, v64, v65\n"
"v_cvt_pk_bf16_f32 v65, v66, v67\n"
"v_mfma_f32_16x16x32_bf16 v[136:139], a[184:187], v[196:199], v[136:139]\n"
"v_cvt_pk_bf16_f32 v66, v80, v81\n"
"v_cvt_pk_bf16_f32 v67, v82, v83\n"
"v_mfma_f32_16x16x32_bf16 v[140:143], a[172:175], v[192:195], v[140:143]\n"
"s_add_u32 m0, s58, 24576\n"
"s_mov_b32 s60, 3670144\n"
"v_mfma_f32_16x16x32_bf16 v[140:143], a[188:191], v[196:199], v[140:143]\n"
"ds_read_b64_tr_b16 v[192:193], v228 offset:0\n"
"s_waitcnt lgkmcnt(9)\n"
"v_mfma_f32_16x16x32_bf16 v[144:147], a[160:163], v[200:203], v[144:147]\n"
"buffer_load_dwordx4 v234, s[28:31], s60 offen offset:0 lds\n"
"v_mfma_f32_16x16x32_bf16 v[144:147], a[176:179], v[204:207], v[144:147]\n"
"buffer_load_dwordx4 v235, s[28:31], s60 offen offset:1024 lds\n"
"v_mfma_f32_16x16x32_bf16 v[148:151], a[164:167], v[200:203], v[148:151]\n"
"ds_read_b64_tr_b16 v[194:195], v228 offset:512\n"
"s_add_u32 s67, s66, 2048\n"
"v_mfma_f32_16x16x32_bf16 v[148:151], a[180:183], v[204:207], v[148:151]\n"
"ds_read_b64_tr_b16 v[196:197], v228 offset:4096\n"
"s_mov_b32 s28, s78\n"
"v_mfma_f32_16x16x32_bf16 v[152:155], a[168:171], v[200:203], v[152:155]\n"
"ds_read_b64_tr_b16 v[198:199], v228 offset:4608\n"
"s_and_b32 s29, s79, 0xffff\n"
"v_mfma_f32_16x16x32_bf16 v[152:155], a[184:187], v[204:207], v[152:155]\n"
"buffer_store_dwordx4 v[64:67], v232, s[44:47], s67 offen sc1\n"
"v_mfma_f32_16x16x32_bf16 v[156:159], a[172:175], v[200:203], v[156:159]\n"
"ds_read_b64_tr_b16 v[200:201], v229 offset:0\n"
"s_mov_b32 s30, 1835008\n"
"v_mfma_f32_16x16x32_bf16 v[156:159], a[188:191], v[204:207], v[156:159]\n"
"ds_read_b64_tr_b16 v[202:203], v229 offset:512\n"
"s_waitcnt lgkmcnt(10)\n"
"v_mfma_f32_16x16x32_bf16 v[160:163], a[160:163], v[208:211], v[160:163]\n"
"ds_read_b64_tr_b16 v[204:205], v229 offset:4096\n"
"s_mov_b32 s31, 0x20000\n"
"v_mfma_f32_16x16x32_bf16 v[160:163], a[176:179], v[212:215], v[160:163]\n"
"ds_read_b64_tr_b16 v[206:207], v229 offset:4608\n"
"v_mfma_f32_16x16x32_bf16 v[164:167], a[164:167], v[208:211], v[164:167]\n"
"v_mfma_f32_16x16x32_bf16 v[164:167], a[180:183], v[212:215], v[164:167]\n"
"v_mfma_f32_16x16x32_bf16 v[168:171], a[168:171], v[208:211], v[168:171]\n"
"v_mfma_f32_16x16x32_bf16 v[168:171], a[184:187], v[212:215], v[168:171]\n"
"v_mfma_f32_16x16x32_bf16 v[172:175], a[172:175], v[208:211], v[172:175]\n"
"s_add_u32 m0, s58, 32768\n"
"s_mov_b32 s60, 0\n"
"v_mfma_f32_16x16x32_bf16 v[172:175], a[188:191], v[212:215], v[172:175]\n"
"ds_read_b64_tr_b16 v[208:209], v230 offset:0\n"
"s_waitcnt lgkmcnt(9)\n"
"v_mfma_f32_16x16x32_bf16 v[176:179], a[160:163], v[216:219], v[176:179]\n"
"buffer_load_dwordx4 v234, s[36:39], s60 offen offset:0 lds\n"
"v_mfma_f32_16x16x32_bf16 v[176:179], a[176:179], v[220:223], v[176:179]\n"
"buffer_load_dwordx4 v235, s[36:39], s60 offen offset:1024 lds\n"
"v_mfma_f32_16x16x32_bf16 v[180:183], a[164:167], v[216:219], v[180:183]\n"
"ds_read_b64_tr_b16 v[210:211], v230 offset:512\n"
"v_cvt_pk_bf16_f32 v68, v68, v69\n"
"v_mfma_f32_16x16x32_bf16 v[180:183], a[180:183], v[220:223], v[180:183]\n"
"ds_read_b64_tr_b16 v[212:213], v230 offset:4096\n"
"v_cvt_pk_bf16_f32 v69, v70, v71\n"
"v_mfma_f32_16x16x32_bf16 v[184:187], a[168:171], v[216:219], v[184:187]\n"
"ds_read_b64_tr_b16 v[214:215], v230 offset:4608\n"
"v_cvt_pk_bf16_f32 v70, v84, v85\n"
"v_mfma_f32_16x16x32_bf16 v[184:187], a[184:187], v[220:223], v[184:187]\n"
"v_cvt_pk_bf16_f32 v71, v86, v87\n"
"s_add_u32 s67, s66, 6144\n"
"v_mfma_f32_16x16x32_bf16 v[188:191], a[172:175], v[216:219], v[188:191]\n"
"ds_read_b64_tr_b16 v[216:217], v231 offset:0\n"
"v_mfma_f32_16x16x32_bf16 v[188:191], a[188:191], v[220:223], v[188:191]\n"
"ds_read_b64_tr_b16 v[218:219], v231 offset:512\n"
"s_waitcnt lgkmcnt(10)\n"
"v_mfma_f32_16x16x32_bf16 v[128:131], a[192:195], v[192:195], v[128:131]\n"
"ds_read_b64_tr_b16 v[220:221], v231 offset:4096\n"
"v_mfma_f32_16x16x32_bf16 v[128:131], a[208:211], v[196:199], v[128:131]\n"
"ds_read_b64_tr_b16 v[222:223], v231 offset:4608\n"
"v_mfma_f32_16x16x32_bf16 v[132:135], a[196:199], v[192:195], v[132:135]\n"
"buffer_store_dwordx4 v[68:71], v232, s[44:47], s67 offen sc1\n"
"v_mfma_f32_16x16x32_bf16 v[132:135], a[212:215], v[196:199], v[132:135]\n"
"v_mfma_f32_16x16x32_bf16 v[136:139], a[200:203], v[192:195], v[136:139]\n"
"v_mfma_f32_16x16x32_bf16 v[136:139], a[216:219], v[196:199], v[136:139]\n"
"v_mfma_f32_16x16x32_bf16 v[140:143], a[204:207], v[192:195], v[140:143]\n"
"ds_read_b64_tr_b16 v[192:193], v228 offset:8192\n"
"v_mfma_f32_16x16x32_bf16 v[140:143], a[220:223], v[196:199], v[140:143]\n"
"ds_read_b64_tr_b16 v[194:195], v228 offset:8704\n"
"s_waitcnt lgkmcnt(10)\n"
"v_mfma_f32_16x16x32_bf16 v[144:147], a[192:195], v[200:203], v[144:147]\n"
"ds_read_b64_tr_b16 v[196:197], v228 offset:12288\n"
"v_mfma_f32_16x16x32_bf16 v[144:147], a[208:211], v[204:207], v[144:147]\n"
"ds_read_b64_tr_b16 v[198:199], v228 offset:12800\n"
"v_mfma_f32_16x16x32_bf16 v[148:151], a[196:199], v[200:203], v[148:151]\n"
"v_mfma_f32_16x16x32_bf16 v[148:151], a[212:215], v[204:207], v[148:151]\n"
"v_mfma_f32_16x16x32_bf16 v[152:155], a[200:203], v[200:203], v[152:155]\n"
"v_cvt_pk_bf16_f32 v72, v72, v73\n"
"v_cvt_pk_bf16_f32 v73, v74, v75\n"
"v_mfma_f32_16x16x32_bf16 v[152:155], a[216:219], v[204:207], v[152:155]\n"
"v_cvt_pk_bf16_f32 v74, v88, v89\n"
"v_cvt_pk_bf16_f32 v75, v90, v91\n"
"v_mfma_f32_16x16x32_bf16 v[156:159], a[204:207], v[200:203], v[156:159]\n"
"ds_read_b64_tr_b16 v[200:201], v229 offset:8192\n"
"s_add_u32 s67, s66, 18432\n"
"v_mfma_f32_16x16x32_bf16 v[156:159], a[220:223], v[204:207], v[156:159]\n"
"ds_read_b64_tr_b16 v[202:203], v229 offset:8704\n"
"s_waitcnt lgkmcnt(10)\n"
"v_mfma_f32_16x16x32_bf16 v[160:163], a[192:195], v[208:211], v[160:163]\n"
"ds_read_b64_tr_b16 v[204:205], v229 offset:12288\n"
"v_mfma_f32_16x16x32_bf16 v[160:163], a[208:211], v[212:215], v[160:163]\n"
"ds_read_b64_tr_b16 v[206:207], v229 offset:12800\n"
"v_mfma_f32_16x16x32_bf16 v[164:167], a[196:199], v[208:211], v[164:167]\n"
"buffer_store_dwordx4 v[72:75], v232, s[44:47], s67 offen sc1\n"
"v_mfma_f32_16x16x32_bf16 v[164:167], a[212:215], v[212:215], v[164:167]\n"
"v_mfma_f32_16x16x32_bf16 v[168:171], a[200:203], v[208:211], v[168:171]\n"
"v_mfma_f32_16x16x32_bf16 v[168:171], a[216:219], v[212:215], v[168:171]\n"
"v_mfma_f32_16x16x32_bf16 v[172:175], a[204:207], v[208:211], v[172:175]\n"
"ds_read_b64_tr_b16 v[208:209], v230 offset:8192\n"
"v_mfma_f32_16x16x32_bf16 v[172:175], a[220:223], v[212:215], v[172:175]\n"
"ds_read_b64_tr_b16 v[210:211], v230 offset:8704\n"
"s_waitcnt lgkmcnt(10)\n"
"v_mfma_f32_16x16x32_bf16 v[176:179], a[192:195], v[216:219], v[176:179]\n"
"ds_read_b64_tr_b16 v[212:213], v230 offset:12288\n"
"v_mfma_f32_16x16x32_bf16 v[176:179], a[208:211], v[220:223], v[176:179]\n"
"ds_read_b64_tr_b16 v[214:215], v230 offset:12800\n"
"v_mfma_f32_16x16x32_bf16 v[180:183], a[196:199], v[216:219], v[180:183]\n"
"v_mfma_f32_16x16x32_bf16 v[180:183], a[212:215], v[220:223], v[180:183]\n"
"v_mfma_f32_16x16x32_bf16 v[184:187], a[200:203], v[216:219], v[184:187]\n"
"v_mfma_f32_16x16x32_bf16 v[184:187], a[216:219], v[220:223], v[184:187]\n"
"v_mfma_f32_16x16x32_bf16 v[188:191], a[204:207], v[216:219], v[188:191]\n"
"ds_read_b64_tr_b16 v[216:217], v231 offset:8192\n"
"v_mfma_f32_16x16x32_bf16 v[188:191], a[220:223], v[220:223], v[188:191]\n"
"ds_read_b64_tr_b16 v[218:219], v231 offset:8704\n"
"s_waitcnt lgkmcnt(10)\n"
"v_mfma_f32_16x16x32_bf16 v[128:131], a[224:227], v[192:195], v[128:131]\n"
"ds_read_b64_tr_b16 v[220:221], v231 offset:12288\n"
"v_cvt_pk_bf16_f32 v76, v76, v77\n"
"v_mfma_f32_16x16x32_bf16 v[128:131], a[240:243], v[196:199], v[128:131]\n"
"ds_read_b64_tr_b16 v[222:223], v231 offset:12800\n"
"v_cvt_pk_bf16_f32 v77, v78, v79\n"
"v_mfma_f32_16x16x32_bf16 v[132:135], a[228:231], v[192:195], v[132:135]\n"
"v_cvt_pk_bf16_f32 v78, v92, v93\n"
"v_cvt_pk_bf16_f32 v79, v94, v95\n"
"v_mfma_f32_16x16x32_bf16 v[132:135], a[244:247], v[196:199], v[132:135]\n"
"s_add_u32 s67, s66, 22528\n"
"v_mfma_f32_16x16x32_bf16 v[136:139], a[232:235], v[192:195], v[136:139]\n"
"buffer_store_dwordx4 v[76:79], v232, s[44:47], s67 offen sc1\n"
"v_mfma_f32_16x16x32_bf16 v[136:139], a[248:251], v[196:199], v[136:139]\n"
"v_mfma_f32_16x16x32_bf16 v[140:143], a[236:239], v[192:195], v[140:143]\n"
"s_waitcnt vmcnt(17)\n"
"s_barrier\n"
"v_mfma_f32_16x16x32_bf16 v[140:143], a[252:255], v[196:199], v[140:143]\n"
"ds_read_b64_tr_b16 v[192:193], v228 offset:16384\n"
"s_waitcnt lgkmcnt(9)\n"
"v_mfma_f32_16x16x32_bf16 v[144:147], a[224:227], v[200:203], v[144:147]\n"
"ds_read_b64_tr_b16 v[194:195], v228 offset:16896\n"
"v_mfma_f32_16x16x32_bf16 v[144:147], a[240:243], v[204:207], v[144:147]\n"
"ds_read_b64_tr_b16 v[196:197], v228 offset:20480\n"
"v_mfma_f32_16x16x32_bf16 v[148:151], a[228:231], v[200:203], v[148:151]\n"
"ds_read_b64_tr_b16 v[198:199], v228 offset:20992\n"
"v_mfma_f32_16x16x32_bf16 v[148:151], a[244:247], v[204:207], v[148:151]\n"
"v_mfma_f32_16x16x32_bf16 v[152:155], a[232:235], v[200:203], v[152:155]\n"
"v_mfma_f32_16x16x32_bf16 v[152:155], a[248:251], v[204:207], v[152:155]\n"
"v_mfma_f32_16x16x32_bf16 v[156:159], a[236:239], v[200:203], v[156:159]\n"
"ds_read_b64_tr_b16 v[200:201], v229 offset:16384\n"
"v_mfma_f32_16x16x32_bf16 v[156:159], a[252:255], v[204:207], v[156:159]\n"
"buffer_load_dwordx2 v[250:251], v239, s[40:43], s51 offen sc1\n"
"s_waitcnt lgkmcnt(9)\n"
"v_mfma_f32_16x16x32_bf16 v[160:163], a[224:227], v[208:211], v[160:163]\n"
"ds_read_b64_tr_b16 v[202:203], v229 offset:16896\n"
"v_mfma_f32_16x16x32_bf16 v[160:163], a[240:243], v[212:215], v[160:163]\n"
"ds_read_b64_tr_b16 v[204:205], v229 offset:20480\n"
"v_mfma_f32_16x16x32_bf16 v[164:167], a[228:231], v[208:211], v[164:167]\n"
"ds_read_b64_tr_b16 v[206:207], v229 offset:20992\n"
"v_mfma_f32_16x16x32_bf16 v[164:167], a[244:247], v[212:215], v[164:167]\n"
"v_mfma_f32_16x16x32_bf16 v[168:171], a[232:235], v[208:211], v[168:171]\n"
"v_cvt_pk_bf16_f32 v96, v96, v97\n"
"v_cvt_pk_bf16_f32 v97, v98, v99\n"
"v_mfma_f32_16x16x32_bf16 v[168:171], a[248:251], v[212:215], v[168:171]\n"
"v_cvt_pk_bf16_f32 v98, v112, v113\n"
"v_cvt_pk_bf16_f32 v99, v114, v115\n"
"v_mfma_f32_16x16x32_bf16 v[172:175], a[236:239], v[208:211], v[172:175]\n"
"s_add_u32 m0, s58, 40960\n"
"s_mov_b32 s60, 128\n"
"v_mfma_f32_16x16x32_bf16 v[172:175], a[252:255], v[212:215], v[172:175]\n"
"ds_read_b64_tr_b16 v[208:209], v230 offset:16384\n"
"s_waitcnt lgkmcnt(9)\n"
"v_mfma_f32_16x16x32_bf16 v[176:179], a[224:227], v[216:219], v[176:179]\n"
"buffer_load_dwordx4 v234, s[36:39], s60 offen offset:0 lds\n"
"v_mfma_f32_16x16x32_bf16 v[176:179], a[240:243], v[220:223], v[176:179]\n"
"buffer_load_dwordx4 v235, s[36:39], s60 offen offset:1024 lds\n"
"v_mfma_f32_16x16x32_bf16 v[180:183], a[228:231], v[216:219], v[180:183]\n"
"ds_read_b64_tr_b16 v[210:211], v230 offset:16896\n"
"s_add_u32 s67, s66, 3072\n"
"v_mfma_f32_16x16x32_bf16 v[180:183], a[244:247], v[220:223], v[180:183]\n"
"ds_read_b64_tr_b16 v[212:213], v230 offset:20480\n"
"v_mfma_f32_16x16x32_bf16 v[184:187], a[232:235], v[216:219], v[184:187]\n"
"ds_read_b64_tr_b16 v[214:215], v230 offset:20992\n"
"v_mfma_f32_16x16x32_bf16 v[184:187], a[248:251], v[220:223], v[184:187]\n"
"buffer_store_dwordx4 v[96:99], v232, s[44:47], s67 offen sc1\n"
"v_mfma_f32_16x16x32_bf16 v[188:191], a[236:239], v[216:219], v[188:191]\n"
"ds_read_b64_tr_b16 v[216:217], v231 offset:16384\n"
"v_mfma_f32_16x16x32_bf16 v[188:191], a[252:255], v[220:223], v[188:191]\n"
"ds_read_b64_tr_b16 v[218:219], v231 offset:16896\n"
"s_waitcnt lgkmcnt(10)\n"
"v_mfma_f32_16x16x32_bf16 v[0:3], a[0:3], v[192:195], 0\n"
"ds_read_b64_tr_b16 v[220:221], v231 offset:20480\n"
"v_mfma_f32_16x16x32_bf16 v[0:3], a[16:19], v[196:199], v[0:3]\n"
"ds_read_b64_tr_b16 v[222:223], v231 offset:20992\n"
"v_mfma_f32_16x16x32_bf16 v[4:7], a[4:7], v[192:195], 0\n"
"v_mfma_f32_16x16x32_bf16 v[4:7], a[20:23], v[196:199], v[4:7]\n"
"v_mfma_f32_16x16x32_bf16 v[8:11], a[8:11], v[192:195], 0\n"
"v_mfma_f32_16x16x32_bf16 v[8:11], a[24:27], v[196:199], v[8:11]\n"
"v_mfma_f32_16x16x32_bf16 v[12:15], a[12:15], v[192:195], 0\n"
"s_add_u32 m0, s58, 49152\n"
"s_mov_b32 s60, 524288\n"
"v_mfma_f32_16x16x32_bf16 v[12:15], a[28:31], v[196:199], v[12:15]\n"
"ds_read_b64_tr_b16 v[192:193], v228 offset:24576\n"
"s_waitcnt lgkmcnt(9)\n"
"v_mfma_f32_16x16x32_bf16 v[16:19], a[0:3], v[200:203], 0\n"
"buffer_load_dwordx4 v234, s[36:39], s60 offen offset:0 lds\n"
"v_mfma_f32_16x16x32_bf16 v[16:19], a[16:19], v[204:207], v[16:19]\n"
"buffer_load_dwordx4 v235, s[36:39], s60 offen offset:1024 lds\n"
"v_mfma_f32_16x16x32_bf16 v[20:23], a[4:7], v[200:203], 0\n"
"ds_read_b64_tr_b16 v[194:195], v228 offset:25088\n"
"v_cvt_pk_bf16_f32 v100, v100, v101\n"
"v_mfma_f32_16x16x32_bf16 v[20:23], a[20:23], v[204:207], v[20:23]\n"
"ds_read_b64_tr_b16 v[196:197], v228 offset:28672\n"
"v_cvt_pk_bf16_f32 v101, v102, v103\n"
"v_mfma_f32_16x16x32_bf16 v[24:27], a[8:11], v[200:203], 0\n"
"ds_read_b64_tr_b16 v[198:199], v228 offset:29184\n"
"v_cvt_pk_bf16_f32 v102, v116, v117\n"
"v_mfma_f32_16x16x32_bf16 v[24:27], a[24:27], v[204:207], v[24:27]\n"
"v_cvt_pk_bf16_f32 v103, v118, v119\n"
"s_add_u32 s67, s66, 7168\n"
"v_mfma_f32_16x16x32_bf16 v[28:31], a[12:15], v[200:203], 0\n"
"ds_read_b64_tr_b16 v[200:201], v229 offset:24576\n"
"v_mfma_f32_16x16x32_bf16 v[28:31], a[28:31], v[204:207], v[28:31]\n"
"ds_read_b64_tr_b16 v[202:203], v229 offset:25088\n"
"s_waitcnt lgkmcnt(10)\n"
"v_mfma_f32_16x16x32_bf16 v[32:35], a[0:3], v[208:211], 0\n"
"ds_read_b64_tr_b16 v[204:205], v229 offset:28672\n"
"v_mfma_f32_16x16x32_bf16 v[32:35], a[16:19], v[212:215], v[32:35]\n"
"ds_read_b64_tr_b16 v[206:207], v229 offset:29184\n"
"v_mfma_f32_16x16x32_bf16 v[36:39], a[4:7], v[208:211], 0\n"
"buffer_store_dwordx4 v[100:103], v232, s[44:47], s67 offen sc1\n"
"v_mfma_f32_16x16x32_bf16 v[36:39], a[20:23], v[212:215], v[36:39]\n"
"v_mfma_f32_16x16x32_bf16 v[40:43], a[8:11], v[208:211], 0\n"
"v_mfma_f32_16x16x32_bf16 v[40:43], a[24:27], v[212:215], v[40:43]\n"
"v_mfma_f32_16x16x32_bf16 v[44:47], a[12:15], v[208:211], 0\n"
"s_add_u32 m0, s58, 0\n"
"s_mov_b32 s60, 524416\n"
"v_mfma_f32_16x16x32_bf16 v[44:47], a[28:31], v[212:215], v[44:47]\n"
"ds_read_b64_tr_b16 v[208:209], v230 offset:24576\n"
"s_waitcnt lgkmcnt(9)\n"
"v_mfma_f32_16x16x32_bf16 v[48:51], a[0:3], v[216:219], 0\n"
"buffer_load_dwordx4 v234, s[36:39], s60 offen offset:0 lds\n"
"v_mfma_f32_16x16x32_bf16 v[48:51], a[16:19], v[220:223], v[48:51]\n"
"buffer_load_dwordx4 v235, s[36:39], s60 offen offset:1024 lds\n"
"v_mfma_f32_16x16x32_bf16 v[52:55], a[4:7], v[216:219], 0\n"
"ds_read_b64_tr_b16 v[210:211], v230 offset:25088\n"
"v_mfma_f32_16x16x32_bf16 v[52:55], a[20:23], v[220:223], v[52:55]\n"
"ds_read_b64_tr_b16 v[212:213], v230 offset:28672\n"
"s_waitcnt vmcnt(8)\n"
"v_cmp_ne_u64 vcc, s[6:7], v[250:251]\n"
"v_mfma_f32_16x16x32_bf16 v[56:59], a[8:11], v[216:219], 0\n"
"s_cbranch_vccnz Lph_%=\n"
"Lph_ok_%=:\n"
"ds_read_b64_tr_b16 v[214:215], v230 offset:29184\n"
"v_mfma_f32_16x16x32_bf16 v[56:59], a[24:27], v[220:223], v[56:59]\n"
"v_cvt_pk_bf16_f32 v104, v104, v105\n"
"v_cvt_pk_bf16_f32 v105, v106, v107\n"
"v_mfma_f32_16x16x32_bf16 v[60:63], a[12:15], v[216:219], 0\n"
"ds_read_b64_tr_b16 v[216:217], v231 offset:24576\n"
"v_cvt_pk_bf16_f32 v106, v120, v121\n"
"v_mfma_f32_16x16x32_bf16 v[60:63], a[28:31], v[220:223], v[60:63]\n"
"ds_read_b64_tr_b16 v[218:219], v231 offset:25088\n"
"s_waitcnt lgkmcnt(10)\n"
"v_mfma_f32_16x16x32_bf16 v[0:3], a[32:35], v[192:195], v[0:3]\n"
"ds_read_b64_tr_b16 v[220:221], v231 offset:28672\n"
"buffer_load_dwordx4 a[0:3], v236, s[40:43], s62 offen offset:0 sc1\n"
"v_mfma_f32_16x16x32_bf16 v[0:3], a[48:51], v[196:199], v[0:3]\n"
"ds_read_b64_tr_b16 v[222:223], v231 offset:29184\n"
"v_cvt_pk_bf16_f32 v107, v122, v123\n"
"v_mfma_f32_16x16x32_bf16 v[4:7], a[36:39], v[192:195], v[4:7]\n"
"s_add_u32 s67, s66, 19456\n"
"s_lshl_b32 s51, s13, 11\n"
"v_mfma_f32_16x16x32_bf16 v[4:7], a[52:55], v[196:199], v[4:7]\n"
"buffer_store_dwordx4 v[104:107], v232, s[44:47], s67 offen sc1\n"
"v_mfma_f32_16x16x32_bf16 v[8:11], a[40:43], v[192:195], v[8:11]\n"
"s_add_u32 s51, s51, 196608\n"
"v_mfma_f32_16x16x32_bf16 v[8:11], a[56:59], v[196:199], v[8:11]\n"
"v_mfma_f32_16x16x32_bf16 v[12:15], a[44:47], v[192:195], v[12:15]\n"
"s_add_u32 m0, s58, 65536\n"
"s_mov_b32 s60, 1048576\n"
"v_mfma_f32_16x16x32_bf16 v[12:15], a[60:63], v[196:199], v[12:15]\n"
"ds_read_b64_tr_b16 v[192:193], v228 offset:32768\n"
"s_waitcnt lgkmcnt(9)\n"
"v_mfma_f32_16x16x32_bf16 v[16:19], a[32:35], v[200:203], v[16:19]\n"
"buffer_load_dwordx4 v234, s[36:39], s60 offen offset:0 lds\n"
"v_mfma_f32_16x16x32_bf16 v[16:19], a[48:51], v[204:207], v[16:19]\n"
"buffer_load_dwordx4 v235, s[36:39], s60 offen offset:1024 lds\n"
"v_mfma_f32_16x16x32_bf16 v[20:23], a[36:39], v[200:203], v[20:23]\n"
"ds_read_b64_tr_b16 v[194:195], v228 offset:33280\n"
"buffer_load_dwordx4 a[16:19], v236, s[40:43], s62 offen offset:256 sc1\n"
"v_mfma_f32_16x16x32_bf16 v[20:23], a[52:55], v[204:207], v[20:23]\n"
"ds_read_b64_tr_b16 v[196:197], v228 offset:36864\n"
"v_mfma_f32_16x16x32_bf16 v[24:27], a[40:43], v[200:203], v[24:27]\n"
"ds_read_b64_tr_b16 v[198:199], v228 offset:37376\n"
"v_mfma_f32_16x16x32_bf16 v[24:27], a[56:59], v[204:207], v[24:27]\n"
"v_mfma_f32_16x16x32_bf16 v[28:31], a[44:47], v[200:203], v[28:31]\n"
"ds_read_b64_tr_b16 v[200:201], v229 offset:32768\n"
"v_mfma_f32_16x16x32_bf16 v[28:31], a[60:63], v[204:207], v[28:31]\n"
"ds_read_b64_tr_b16 v[202:203], v229 offset:33280\n"
"s_waitcnt lgkmcnt(10)\n"
"v_mfma_f32_16x16x32_bf16 v[32:35], a[32:35], v[208:211], v[32:35]\n"
"ds_read_b64_tr_b16 v[204:205], v229 offset:36864\n"
"v_cvt_pk_bf16_f32 v108, v108, v109\n"
"v_mfma_f32_16x16x32_bf16 v[32:35], a[48:51], v[212:215], v[32:35]\n"
"ds_read_b64_tr_b16 v[206:207], v229 offset:37376\n"
"v_cvt_pk_bf16_f32 v109, v110, v111\n"
"v_mfma_f32_16x16x32_bf16 v[36:39], a[36:39], v[208:211], v[36:39]\n"
"v_cvt_pk_bf16_f32 v110, v124, v125\n"
"v_cvt_pk_bf16_f32 v111, v126, v127\n"
"v_mfma_f32_16x16x32_bf16 v[36:39], a[52:55], v[212:215], v[36:39]\n"
"buffer_load_dwordx4 a[4:7], v236, s[40:43], s63 offen offset:0 sc1\n"
"s_add_u32 s67, s66, 23552\n"
"v_mfma_f32_16x16x32_bf16 v[40:43], a[40:43], v[208:211], v[40:43]\n"
"buffer_store_dwordx4 v[108:111], v232, s[44:47], s67 offen sc1\n"
"v_mfma_f32_16x16x32_bf16 v[40:43], a[56:59], v[212:215], v[40:43]\n"
"v_mfma_f32_16x16x32_bf16 v[44:47], a[44:47], v[208:211], v[44:47]\n"
"ds_read_b64_tr_b16 v[208:209], v230 offset:32768\n"
"v_mfma_f32_16x16x32_bf16 v[44:47], a[60:63], v[212:215], v[44:47]\n"
"ds_read_b64_tr_b16 v[210:211], v230 offset:33280\n"
"s_waitcnt lgkmcnt(10)\n"
"v_mfma_f32_16x16x32_bf16 v[48:51], a[32:35], v[216:219], v[48:51]\n"
"ds_read_b64_tr_b16 v[212:213], v230 offset:36864\n"
"v_mfma_f32_16x16x32_bf16 v[48:51], a[48:51], v[220:223], v[48:51]\n"
"ds_read_b64_tr_b16 v[214:215], v230 offset:37376\n"
"v_mfma_f32_16x16x32_bf16 v[52:55], a[36:39], v[216:219], v[52:55]\n"
"v_mfma_f32_16x16x32_bf16 v[52:55], a[52:55], v[220:223], v[52:55]\n"
"v_mfma_f32_16x16x32_bf16 v[56:59], a[40:43], v[216:219], v[56:59]\n"
"buffer_load_dwordx4 a[20:23], v236, s[40:43], s63 offen offset:256 sc1\n"
"v_mfma_f32_16x16x32_bf16 v[56:59], a[56:59], v[220:223], v[56:59]\n"
"v_mfma_f32_16x16x32_bf16 v[60:63], a[44:47], v[216:219], v[60:63]\n"
"ds_read_b64_tr_b16 v[216:217], v231 offset:32768\n"
"v_mfma_f32_16x16x32_bf16 v[60:63], a[60:63], v[220:223], v[60:63]\n"
"ds_read_b64_tr_b16 v[218:219], v231 offset:33280\n"
"s_waitcnt lgkmcnt(10)\n"
"v_mfma_f32_16x16x32_bf16 v[0:3], a[64:67], v[192:195], v[0:3]\n"
"ds_read_b64_tr_b16 v[220:221], v231 offset:36864\n"
"v_mfma_f32_16x16x32_bf16 v[0:3], a[80:83], v[196:199], v[0:3]\n"
"ds_read_b64_tr_b16 v[222:223], v231 offset:37376\n"
"v_mfma_f32_16x16x32_bf16 v[4:7], a[68:71], v[192:195], v[4:7]\n"
"v_mfma_f32_16x16x32_bf16 v[4:7], a[84:87], v[196:199], v[4:7]\n"
"v_mfma_f32_16x16x32_bf16 v[8:11], a[72:75], v[192:195], v[8:11]\n"
"v_cvt_pk_bf16_f32 v128, v128, v129\n"
"v_cvt_pk_bf16_f32 v129, v130, v131\n"
"v_mfma_f32_16x16x32_bf16 v[8:11], a[88:91], v[196:199], v[8:11]\n"
"v_cvt_pk_bf16_f32 v130, v132, v133\n"
"v_cvt_pk_bf16_f32 v131, v134, v135\n"
"v_mfma_f32_16x16x32_bf16 v[12:15], a[76:79], v[192:195], v[12:15]\n"
"ds_read_b64_tr_b16 v[192:193], v228 offset:40960\n"
"buffer_load_dwordx4 a[8:11], v236, s[40:43], s64 offen offset:0 sc1\n"
"v_mfma_f32_16x16x32_bf16 v[12:15], a[92:95], v[196:199], v[12:15]\n"
"ds_read_b64_tr_b16 v[194:195], v228 offset:41472\n"
"s_waitcnt lgkmcnt(10)\n"
"v_mfma_f32_16x16x32_bf16 v[16:19], a[64:67], v[200:203], v[16:19]\n"
"ds_read_b64_tr_b16 v[196:197], v228 offset:45056\n"
"s_add_u32 s67, s66, 8192\n"
"v_mfma_f32_16x16x32_bf16 v[16:19], a[80:83], v[204:207], v[16:19]\n"
"ds_read_b64_tr_b16 v[198:199], v228 offset:45568\n"
"v_mfma_f32_16x16x32_bf16 v[20:23], a[68:71], v[200:203], v[20:23]\n"
"buffer_store_dwordx4 v[128:131], v232, s[44:47], s67 offen sc1\n"
"v_mfma_f32_16x16x32_bf16 v[20:23], a[84:87], v[204:207], v[20:23]\n"
"v_mfma_f32_16x16x32_bf16 v[24:27], a[72:75], v[200:203], v[24:27]\n"
"v_mfma_f32_16x16x32_bf16 v[24:27], a[88:91], v[204:207], v[24:27]\n"
"v_mfma_f32_16x16x32_bf16 v[28:31], a[76:79], v[200:203], v[28:31]\n"
"ds_read_b64_tr_b16 v[200:201], v229 offset:40960\n"
"v_mfma_f32_16x16x32_bf16 v[28:31], a[92:95], v[204:207], v[28:31]\n"
"ds_read_b64_tr_b16 v[202:203], v229 offset:41472\n"
"s_waitcnt lgkmcnt(10)\n"
"v_mfma_f32_16x16x32_bf16 v[32:35], a[64:67], v[208:211], v[32:35]\n"
"ds_read_b64_tr_b16 v[204:205], v229 offset:45056\n"
"buffer_load_dwordx4 a[24:27], v236, s[40:43], s64 offen offset:256 sc1\n"
"v_mfma_f32_16x16x32_bf16 v[32:35], a[80:83], v[212:215], v[32:35]\n"
"ds_read_b64_tr_b16 v[206:207], v229 offset:45568\n"
"v_mfma_f32_16x16x32_bf16 v[36:39], a[68:71], v[208:211], v[36:39]\n"
"v_mfma_f32_16x16x32_bf16 v[36:39], a[84:87], v[212:215], v[36:39]\n"
"v_mfma_f32_16x16x32_bf16 v[40:43], a[72:75], v[208:211], v[40:43]\n"
"v_mfma_f32_16x16x32_bf16 v[40:43], a[88:91], v[212:215], v[40:43]\n"
"v_mfma_f32_16x16x32_bf16 v[44:47], a[76:79], v[208:211], v[44:47]\n"
"ds_read_b64_tr_b16 v[208:209], v230 offset:40960\n"
"v_mfma_f32_16x16x32_bf16 v[44:47], a[92:95], v[212:215], v[44:47]\n"
"ds_read_b64_tr_b16 v[210:211], v230 offset:41472\n"
"s_waitcnt lgkmcnt(10)\n"
"v_mfma_f32_16x16x32_bf16 v[48:51], a[64:67], v[216:219], v[48:51]\n"
"ds_read_b64_tr_b16 v[212:213], v230 offset:45056\n"
"v_cvt_pk_bf16_f32 v136, v136, v137\n"
"v_mfma_f32_16x16x32_bf16 v[48:51], a[80:83], v[220:223], v[48:51]\n"
"ds_read_b64_tr_b16 v[214:215], v230 offset:45568\n"
"buffer_load_dwordx4 a[12:15], v236, s[40:43], s65 offen offset:0 sc1\n"
"v_mfma_f32_16x16x32_bf16 v[52:55], a[68:71], v[216:219], v[52:55]\n"
"v_cvt_pk_bf16_f32 v137, v138, v139\n"
"v_cvt_pk_bf16_f32 v138, v140, v141\n"
"v_mfma_f32_16x16x32_bf16 v[52:55], a[84:87], v[220:223], v[52:55]\n"
"v_cvt_pk_bf16_f32 v139, v142, v143\n"
"s_add_u32 s67, s66, 24576\n"
"v_mfma_f32_16x16x32_bf16 v[56:59], a[72:75], v[216:219], v[56:59]\n"
"buffer_store_dwordx4 v[136:139], v232, s[44:47], s67 offen sc1\n"
"v_mfma_f32_16x16x32_bf16 v[56:59], a[88:91], v[220:223], v[56:59]\n"
"v_mfma_f32_16x16x32_bf16 v[60:63], a[76:79], v[216:219], v[60:63]\n"
"ds_read_b64_tr_b16 v[216:217], v231 offset:40960\n"
"v_mfma_f32_16x16x32_bf16 v[60:63], a[92:95], v[220:223], v[60:63]\n"
"ds_read_b64_tr_b16 v[218:219], v231 offset:41472\n"
"s_waitcnt lgkmcnt(10)\n"
"v_mfma_f32_16x16x32_bf16 v[0:3], a[96:99], v[192:195], v[0:3]\n"
"ds_read_b64_tr_b16 v[220:221], v231 offset:45056\n"
"v_mfma_f32_16x16x32_bf16 v[0:3], a[112:115], v[196:199], v[0:3]\n"
"ds_read_b64_tr_b16 v[222:223], v231 offset:45568\n"
"v_mfma_f32_16x16x32_bf16 v[4:7], a[100:103], v[192:195], v[4:7]\n"
"buffer_load_dwordx4 a[28:31], v236, s[40:43], s65 offen offset:256 sc1\n"
"v_mfma_f32_16x16x32_bf16 v[4:7], a[116:119], v[196:199], v[4:7]\n"
"v_mfma_f32_16x16x32_bf16 v[8:11], a[104:107], v[192:195], v[8:11]\n"
"v_mfma_f32_16x16x32_bf16 v[8:11], a[120:123], v[196:199], v[8:11]\n"
"v_mfma_f32_16x16x32_bf16 v[12:15], a[108:111], v[192:195], v[12:15]\n"
"s_barrier\n"
"v_mfma_f32_16x16x32_bf16 v[12:15], a[124:127], v[196:199], v[12:15]\n"
"ds_read_b64_tr_b16 v[192:193], v228 offset:49152\n"
"s_waitcnt lgkmcnt(9)\n"
"v_mfma_f32_16x16x32_bf16 v[16:19], a[96:99], v[200:203], v[16:19]\n"
"ds_read_b64_tr_b16 v[194:195], v228 offset:49664\n"
"v_mfma_f32_16x16x32_bf16 v[16:19], a[112:115], v[204:207], v[16:19]\n"
"ds_read_b64_tr_b16 v[196:197], v228 offset:53248\n"
"v_mfma_f32_16x16x32_bf16 v[20:23], a[100:103], v[200:203], v[20:23]\n"
"ds_read_b64_tr_b16 v[198:199], v228 offset:53760\n"
"v_mfma_f32_16x16x32_bf16 v[20:23], a[116:119], v[204:207], v[20:23]\n"
"v_mfma_f32_16x16x32_bf16 v[24:27], a[104:107], v[200:203], v[24:27]\n"
"buffer_load_dwordx4 a[32:35], v236, s[40:43], s62 offen offset:512 sc1\n"
"v_cvt_pk_bf16_f32 v144, v144, v145\n"
"v_mfma_f32_16x16x32_bf16 v[24:27], a[120:123], v[204:207], v[24:27]\n"
"v_cvt_pk_bf16_f32 v145, v146, v147\n"
"v_cvt_pk_bf16_f32 v146, v148, v149\n"
"v_mfma_f32_16x16x32_bf16 v[28:31], a[108:111], v[200:203], v[28:31]\n"
"ds_read_b64_tr_b16 v[200:201], v229 offset:49152\n"
"v_cvt_pk_bf16_f32 v147, v150, v151\n"
"v_mfma_f32_16x16x32_bf16 v[28:31], a[124:127], v[204:207], v[28:31]\n"
"ds_read_b64_tr_b16 v[202:203], v229 offset:49664\n"
"s_waitcnt lgkmcnt(10)\n"
"v_mfma_f32_16x16x32_bf16 v[32:35], a[96:99], v[208:211], v[32:35]\n"
"ds_read_b64_tr_b16 v[204:205], v229 offset:53248\n"
"s_add_u32 s67, s66, 9216\n"
"v_mfma_f32_16x16x32_bf16 v[32:35], a[112:115], v[212:215], v[32:35]\n"
"ds_read_b64_tr_b16 v[206:207], v229 offset:53760\n"
"v_mfma_f32_16x16x32_bf16 v[36:39], a[100:103], v[208:211], v[36:39]\n"
"buffer_store_dwordx4 v[144:147], v232, s[44:47], s67 offen sc1\n"
"v_mfma_f32_16x16x32_bf16 v[36:39], a[116:119], v[212:215], v[36:39]\n"
"v_mfma_f32_16x16x32_bf16 v[40:43], a[104:107], v[208:211], v[40:43]\n"
"v_mfma_f32_16x16x32_bf16 v[40:43], a[120:123], v[212:215], v[40:43]\n"
"buffer_load_dwordx4 a[48:51], v236, s[40:43], s62 offen offset:768 sc1\n"
"v_mfma_f32_16x16x32_bf16 v[44:47], a[108:111], v[208:211], v[44:47]\n"
"s_add_u32 m0, s58, 73728\n"
"s_mov_b32 s60, 1048704\n"
"v_mfma_f32_16x16x32_bf16 v[44:47], a[124:127], v[212:215], v[44:47]\n"
"ds_read_b64_tr_b16 v[208:209], v230 offset:49152\n"
"s_waitcnt lgkmcnt(9)\n"
"v_mfma_f32_16x16x32_bf16 v[48:51], a[96:99], v[216:219], v[48:51]\n"
"buffer_load_dwordx4 v234, s[36:39], s60 offen offset:0 lds\n"
"v_mfma_f32_16x16x32_bf16 v[48:51], a[112:115], v[220:223], v[48:51]\n"
"buffer_load_dwordx4 v235, s[36:39], s60 offen offset:1024 lds\n"
"v_mfma_f32_16x16x32_bf16 v[52:55], a[100:103], v[216:219], v[52:55]\n"
"ds_read_b64_tr_b16 v[210:211], v230 offset:49664\n"
"v_mfma_f32_16x16x32_bf16 v[52:55], a[116:119], v[220:223], v[52:55]\n"
"ds_read_b64_tr_b16 v[212:213], v230 offset:53248\n"
"v_mfma_f32_16x16x32_bf16 v[56:59], a[104:107], v[216:219], v[56:59]\n"
"ds_read_b64_tr_b16 v[214:215], v230 offset:53760\n"
"v_mfma_f32_16x16x32_bf16 v[56:59], a[120:123], v[220:223], v[56:59]\n"
"v_mfma_f32_16x16x32_bf16 v[60:63], a[108:111], v[216:219], v[60:63]\n"
"ds_read_b64_tr_b16 v[216:217], v231 offset:49152\n"
"v_mfma_f32_16x16x32_bf16 v[60:63], a[124:127], v[220:223], v[60:63]\n"
"ds_read_b64_tr_b16 v[218:219], v231 offset:49664\n"
"s_waitcnt lgkmcnt(10)\n"
"v_mfma_f32_16x16x32_bf16 v[0:3], a[128:131], v[192:195], v[0:3]\n"
"ds_read_b64_tr_b16 v[220:221], v231 offset:53248\n"
"buffer_load_dwordx4 a[36:39], v236, s[40:43], s63 offen offset:512 sc1\n"
"v_mfma_f32_16x16x32_bf16 v[0:3], a[144:147], v[196:199], v[0:3]\n"
"ds_read_b64_tr_b16 v[222:223], v231 offset:53760\n"
"v_cvt_pk_bf16_f32 v152, v152, v153\n"
"v_mfma_f32_16x16x32_bf16 v[4:7], a[132:135], v[192:195], v[4:7]\n"
"v_cvt_pk_bf16_f32 v153, v154, v155\n"
"v_cvt_pk_bf16_f32 v154, v156, v157\n"
"v_mfma_f32_16x16x32_bf16 v[4:7], a[148:151], v[196:199], v[4:7]\n"
"v_cvt_pk_bf16_f32 v155, v158, v159\n"
"s_add_u32 s67, s66, 25600\n"
"v_mfma_f32_16x16x32_bf16 v[8:11], a[136:139], v[192:195], v[8:11]\n"
"buffer_store_dwordx4 v[152:155], v232, s[44:47], s67 offen sc1\n"
"v_mfma_f32_16x16x32_bf16 v[8:11], a[152:155], v[196:199], v[8:11]\n"
"v_mfma_f32_16x16x32_bf16 v[12:15], a[140:143], v[192:195], v[12:15]\n"
"s_add_u32 m0, s58, 81920\n"
"s_mov_b32 s60, 1572864\n"
"v_mfma_f32_16x16x32_bf16 v[12:15], a[156:159], v[196:199], v[12:15]\n"
"ds_read_b64_tr_b16 v[192:193], v224 offset:8192\n"
"s_waitcnt lgkmcnt(9)\n"
"v_mfma_f32_16x16x32_bf16 v[16:19], a[128:131], v[200:203], v[16:19]\n"
"buffer_load_dwordx4 v234, s[36:39], s60 offen offset:0 lds\n"
"v_mfma_f32_16x16x32_bf16 v[16:19], a[144:147], v[204:207], v[16:19]\n"
"buffer_load_dwordx4 v235, s[36:39], s60 offen offset:1024 lds\n"
"v_mfma_f32_16x16x32_bf16 v[20:23], a[132:135], v[200:203], v[20:23]\n"
"ds_read_b64_tr_b16 v[194:195], v224 offset:8704\n"
"buffer_load_dwordx4 a[52:55], v236, s[40:43], s63 offen offset:768 sc1\n"
"v_mfma_f32_16x16x32_bf16 v[20:23], a[148:151], v[204:207], v[20:23]\n"
"ds_read_b64_tr_b16 v[196:197], v224 offset:12288\n"
"v_mfma_f32_16x16x32_bf16 v[24:27], a[136:139], v[200:203], v[24:27]\n"
"ds_read_b64_tr_b16 v[198:199], v224 offset:12800\n"
"v_mfma_f32_16x16x32_bf16 v[24:27], a[152:155], v[204:207], v[24:27]\n"
"v_mfma_f32_16x16x32_bf16 v[28:31], a[140:143], v[200:203], v[28:31]\n"
"ds_read_b64_tr_b16 v[200:201], v225 offset:8192\n"
"v_mfma_f32_16x16x32_bf16 v[28:31], a[156:159], v[204:207], v[28:31]\n"
"ds_read_b64_tr_b16 v[202:203], v225 offset:8704\n"
"s_waitcnt lgkmcnt(10)\n"
"v_mfma_f32_16x16x32_bf16 v[32:35], a[128:131], v[208:211], v[32:35]\n"
"ds_read_b64_tr_b16 v[204:205], v225 offset:12288\n"
"v_mfma_f32_16x16x32_bf16 v[32:35], a[144:147], v[212:215], v[32:35]\n"
"ds_read_b64_tr_b16 v[206:207], v225 offset:12800\n"
"v_mfma_f32_16x16x32_bf16 v[36:39], a[132:135], v[208:211], v[36:39]\n"
"buffer_load_dwordx4 a[40:43], v236, s[40:43], s64 offen offset:512 sc1\n"
"v_mfma_f32_16x16x32_bf16 v[36:39], a[148:151], v[212:215], v[36:39]\n"
"v_mfma_f32_16x16x32_bf16 v[40:43], a[136:139], v[208:211], v[40:43]\n"
"v_cvt_pk_bf16_f32 v160, v160, v161\n"
"v_cvt_pk_bf16_f32 v161, v162, v163\n"
"v_mfma_f32_16x16x32_bf16 v[40:43], a[152:155], v[212:215], v[40:43]\n"
"v_cvt_pk_bf16_f32 v162, v164, v165\n"
"v_cvt_pk_bf16_f32 v163, v166, v167\n"
"v_mfma_f32_16x16x32_bf16 v[44:47], a[140:143], v[208:211], v[44:47]\n"
"s_add_u32 m0, s58, 90112\n"
"s_mov_b32 s60, 1572992\n"
"v_mfma_f32_16x16x32_bf16 v[44:47], a[156:159], v[212:215], v[44:47]\n"
"ds_read_b64_tr_b16 v[208:209], v226 offset:8192\n"
"s_waitcnt lgkmcnt(9)\n"
"v_mfma_f32_16x16x32_bf16 v[48:51], a[128:131], v[216:219], v[48:51]\n"
"buffer_load_dwordx4 v234, s[36:39], s60 offen offset:0 lds\n"
"v_mfma_f32_16x16x32_bf16 v[48:51], a[144:147], v[220:223], v[48:51]\n"
"buffer_load_dwordx4 v235, s[36:39], s60 offen offset:1024 lds\n"
"v_mfma_f32_16x16x32_bf16 v[52:55], a[132:135], v[216:219], v[52:55]\n"
"ds_read_b64_tr_b16 v[210:211], v226 offset:8704\n"
"s_add_u32 s67, s66, 10240\n"
"v_mfma_f32_16x16x32_bf16 v[52:55], a[148:151], v[220:223], v[52:55]\n"
"ds_read_b64_tr_b16 v[212:213], v226 offset:12288\n"
"buffer_load_dwordx4 a[56:59], v236, s[40:43], s64 offen offset:768 sc1\n"
"v_mfma_f32_16x16x32_bf16 v[56:59], a[136:139], v[216:219], v[56:59]\n"
"ds_read_b64_tr_b16 v[214:215], v226 offset:12800\n"
"v_mfma_f32_16x16x32_bf16 v[56:59], a[152:155], v[220:223], v[56:59]\n"
"buffer_store_dwordx4 v[160:163], v232, s[44:47], s67 offen sc1\n"
"v_mfma_f32_16x16x32_bf16 v[60:63], a[140:143], v[216:219], v[60:63]\n"
"ds_read_b64_tr_b16 v[216:217], v227 offset:8192\n"
"v_mfma_f32_16x16x32_bf16 v[60:63], a[156:159], v[220:223], v[60:63]\n"
"ds_read_b64_tr_b16 v[218:219], v227 offset:8704\n"
"s_waitcnt lgkmcnt(10)\n"
"v_mfma_f32_16x16x32_bf16 v[0:3], a[160:163], v[192:195], v[0:3]\n"
"ds_read_b64_tr_b16 v[220:221], v227 offset:12288\n"
"v_mfma_f32_16x16x32_bf16 v[0:3], a[176:179], v[196:199], v[0:3]\n"
"ds_read_b64_tr_b16 v[222:223], v227 offset:12800\n"
"v_mfma_f32_16x16x32_bf16 v[4:7], a[164:167], v[192:195], v[4:7]\n"
"v_mfma_f32_16x16x32_bf16 v[4:7], a[180:183], v[196:199], v[4:7]\n"
"v_mfma_f32_16x16x32_bf16 v[8:11], a[168:171], v[192:195], v[8:11]\n"
"v_mfma_f32_16x16x32_bf16 v[8:11], a[184:187], v[196:199], v[8:11]\n"
"buffer_load_dwordx4 a[44:47], v236, s[40:43], s65 offen offset:512 sc1\n"
"v_mfma_f32_16x16x32_bf16 v[12:15], a[172:175], v[192:195], v[12:15]\n"
"s_add_u32 m0, s58, 98304\n"
"s_mov_b32 s60, 2097152\n"
"v_mfma_f32_16x16x32_bf16 v[12:15], a[188:191], v[196:199], v[12:15]\n"
"ds_read_b64_tr_b16 v[192:193], v224 offset:16384\n"
"s_waitcnt lgkmcnt(9)\n"
"v_mfma_f32_16x16x32_bf16 v[16:19], a[160:163], v[200:203], v[16:19]\n"
"buffer_load_dwordx4 v234, s[36:39], s60 offen offset:0 lds\n"
"v_mfma_f32_16x16x32_bf16 v[16:19], a[176:179], v[204:207], v[16:19]\n"
"buffer_load_dwordx4 v235, s[36:39], s60 offen offset:1024 lds\n"
"v_mfma_f32_16x16x32_bf16 v[20:23], a[164:167], v[200:203], v[20:23]\n"
"ds_read_b64_tr_b16 v[194:195], v224 offset:16896\n"
"v_cvt_pk_bf16_f32 v168, v168, v169\n"
"v_mfma_f32_16x16x32_bf16 v[20:23], a[180:183], v[204:207], v[20:23]\n"
"ds_read_b64_tr_b16 v[196:197], v224 offset:20480\n"
"v_cvt_pk_bf16_f32 v169, v170, v171\n"
"v_mfma_f32_16x16x32_bf16 v[24:27], a[168:171], v[200:203], v[24:27]\n"
"ds_read_b64_tr_b16 v[198:199], v224 offset:20992\n"
"v_cvt_pk_bf16_f32 v170, v172, v173\n"
"v_mfma_f32_16x16x32_bf16 v[24:27], a[184:187], v[204:207], v[24:27]\n"
"v_cvt_pk_bf16_f32 v171, v174, v175\n"
"s_add_u32 s67, s66, 26624\n"
"v_mfma_f32_16x16x32_bf16 v[28:31], a[172:175], v[200:203], v[28:31]\n"
"ds_read_b64_tr_b16 v[200:201], v225 offset:16384\n"
"buffer_load_dwordx4 a[60:63], v236, s[40:43], s65 offen offset:768 sc1\n"
"v_mfma_f32_16x16x32_bf16 v[28:31], a[188:191], v[204:207], v[28:31]\n"
"ds_read_b64_tr_b16 v[202:203], v225 offset:16896\n"
"s_waitcnt lgkmcnt(10)\n"
"v_mfma_f32_16x16x32_bf16 v[32:35], a[160:163], v[208:211], v[32:35]\n"
"ds_read_b64_tr_b16 v[204:205], v225 offset:20480\n"
"v_mfma_f32_16x16x32_bf16 v[32:35], a[176:179], v[212:215], v[32:35]\n"
"ds_read_b64_tr_b16 v[206:207], v225 offset:20992\n"
"v_mfma_f32_16x16x32_bf16 v[36:39], a[164:167], v[208:211], v[36:39]\n"
"buffer_store_dwordx4 v[168:171], v232, s[44:47], s67 offen sc1\n"
"v_mfma_f32_16x16x32_bf16 v[36:39], a[180:183], v[212:215], v[36:39]\n"
"v_mfma_f32_16x16x32_bf16 v[40:43], a[168:171], v[208:211], v[40:43]\n"
"v_mfma_f32_16x16x32_bf16 v[40:43], a[184:187], v[212:215], v[40:43]\n"
"v_mfma_f32_16x16x32_bf16 v[44:47], a[172:175], v[208:211], v[44:47]\n"
"ds_read_b64_tr_b16 v[208:209], v226 offset:16384\n"
"v_mfma_f32_16x16x32_bf16 v[44:47], a[188:191], v[212:215], v[44:47]\n"
"ds_read_b64_tr_b16 v[210:211], v226 offset:16896\n"
"s_waitcnt lgkmcnt(10)\n"
"v_mfma_f32_16x16x32_bf16 v[48:51], a[160:163], v[216:219], v[48:51]\n"
"ds_read_b64_tr_b16 v[212:213], v226 offset:20480\n"
"buffer_load_dwordx4 a[64:67], v236, s[40:43], s62 offen offset:1024 sc1\n"
"v_mfma_f32_16x16x32_bf16 v[48:51], a[176:179], v[220:223], v[48:51]\n"
"ds_read_b64_tr_b16 v[214:215], v226 offset:20992\n"
"v_mfma_f32_16x16x32_bf16 v[52:55], a[164:167], v[216:219], v[52:55]\n"
"v_mfma_f32_16x16x32_bf16 v[52:55], a[180:183], v[220:223], v[52:55]\n"
"v_mfma_f32_16x16x32_bf16 v[56:59], a[168:171], v[216:219], v[56:59]\n"
"v_cvt_pk_bf16_f32 v176, v176, v177\n"
"v_cvt_pk_bf16_f32 v177, v178, v179\n"
"v_mfma_f32_16x16x32_bf16 v[56:59], a[184:187], v[220:223], v[56:59]\n"
"v_cvt_pk_bf16_f32 v178, v180, v181\n"
"v_cvt_pk_bf16_f32 v179, v182, v183\n"
"v_mfma_f32_16x16x32_bf16 v[60:63], a[172:175], v[216:219], v[60:63]\n"
"ds_read_b64_tr_b16 v[216:217], v227 offset:16384\n"
"s_add_u32 s67, s66, 11264\n"
"v_mfma_f32_16x16x32_bf16 v[60:63], a[188:191], v[220:223], v[60:63]\n"
"ds_read_b64_tr_b16 v[218:219], v227 offset:16896\n"
"s_waitcnt lgkmcnt(10)\n"
"v_mfma_f32_16x16x32_bf16 v[0:3], a[192:195], v[192:195], v[0:3]\n"
"ds_read_b64_tr_b16 v[220:221], v227 offset:20480\n"
"v_mfma_f32_16x16x32_bf16 v[0:3], a[208:211], v[196:199], v[0:3]\n"
"ds_read_b64_tr_b16 v[222:223], v227 offset:20992\n"
"buffer_load_dwordx4 a[80:83], v236, s[40:43], s62 offen offset:1280 sc1\n"
"v_mfma_f32_16x16x32_bf16 v[4:7], a[196:199], v[192:195], v[4:7]\n"
"buffer_store_dwordx4 v[176:179], v232, s[44:47], s67 offen sc1\n"
"v_mfma_f32_16x16x32_bf16 v[4:7], a[212:215], v[196:199], v[4:7]\n"
"v_mfma_f32_16x16x32_bf16 v[8:11], a[200:203], v[192:195], v[8:11]\n"
"v_mfma_f32_16x16x32_bf16 v[8:11], a[216:219], v[196:199], v[8:11]\n"
"v_mfma_f32_16x16x32_bf16 v[12:15], a[204:207], v[192:195], v[12:15]\n"
"ds_read_b64_tr_b16 v[192:193], v224 offset:24576\n"
"v_mfma_f32_16x16x32_bf16 v[12:15], a[220:223], v[196:199], v[12:15]\n"
"ds_read_b64_tr_b16 v[194:195], v224 offset:25088\n"
"s_waitcnt lgkmcnt(10)\n"
"v_mfma_f32_16x16x32_bf16 v[16:19], a[192:195], v[200:203], v[16:19]\n"
"ds_read_b64_tr_b16 v[196:197], v224 offset:28672\n"
"v_mfma_f32_16x16x32_bf16 v[16:19], a[208:211], v[204:207], v[16:19]\n"
"ds_read_b64_tr_b16 v[198:199], v224 offset:29184\n"
"v_mfma_f32_16x16x32_bf16 v[20:23], a[196:199], v[200:203], v[20:23]\n"
"v_mfma_f32_16x16x32_bf16 v[20:23], a[212:215], v[204:207], v[20:23]\n"
"buffer_load_dwordx4 a[68:71], v236, s[40:43], s63 offen offset:1024 sc1\n"
"v_mfma_f32_16x16x32_bf16 v[24:27], a[200:203], v[200:203], v[24:27]\n"
"v_mfma_f32_16x16x32_bf16 v[24:27], a[216:219], v[204:207], v[24:27]\n"
"v_mfma_f32_16x16x32_bf16 v[28:31], a[204:207], v[200:203], v[28:31]\n"
"ds_read_b64_tr_b16 v[200:201], v225 offset:24576\n"
"v_mfma_f32_16x16x32_bf16 v[28:31], a[220:223], v[204:207], v[28:31]\n"
"ds_read_b64_tr_b16 v[202:203], v225 offset:25088\n"
"s_waitcnt lgkmcnt(10)\n"
"v_mfma_f32_16x16x32_bf16 v[32:35], a[192:195], v[208:211], v[32:35]\n"
"ds_read_b64_tr_b16 v[204:205], v225 offset:28672\n"
"v_cvt_pk_bf16_f32 v184, v184, v185\n"
"v_mfma_f32_16x16x32_bf16 v[32:35], a[208:211], v[212:215], v[32:35]\n"
"ds_read_b64_tr_b16 v[206:207], v225 offset:29184\n"
"v_cvt_pk_bf16_f32 v185, v186, v187\n"
"v_mfma_f32_16x16x32_bf16 v[36:39], a[196:199], v[208:211], v[36:39]\n"
"v_cvt_pk_bf16_f32 v186, v188, v189\n"
"v_cvt_pk_bf16_f32 v187, v190, v191\n"
"v_mfma_f32_16x16x32_bf16 v[36:39], a[212:215], v[212:215], v[36:39]\n"
"s_add_u32 s67, s66, 27648\n"
"v_mfma_f32_16x16x32_bf16 v[40:43], a[200:203], v[208:211], v[40:43]\n"
"buffer_store_dwordx4 v[184:187], v232, s[44:47], s67 offen sc1\n"
"v_mfma_f32_16x16x32_bf16 v[40:43], a[216:219], v[212:215], v[40:43]\n"
"buffer_load_dwordx4 a[84:87], v236, s[40:43], s63 offen offset:1280 sc1\n"
"v_mfma_f32_16x16x32_bf16 v[44:47], a[204:207], v[208:211], v[44:47]\n"
"ds_read_b64_tr_b16 v[208:209], v226 offset:24576\n"
"v_mfma_f32_16x16x32_bf16 v[44:47], a[220:223], v[212:215], v[44:47]\n"
"ds_read_b64_tr_b16 v[210:211], v226 offset:25088\n"
"s_waitcnt lgkmcnt(10)\n"
"v_mfma_f32_16x16x32_bf16 v[48:51], a[192:195], v[216:219], v[48:51]\n"
"ds_read_b64_tr_b16 v[212:213], v226 offset:28672\n"
"v_mfma_f32_16x16x32_bf16 v[48:51], a[208:211], v[220:223], v[48:51]\n"
"ds_read_b64_tr_b16 v[214:215], v226 offset:29184\n"
"v_mfma_f32_16x16x32_bf16 v[52:55], a[196:199], v[216:219], v[52:55]\n"
"v_mfma_f32_16x16x32_bf16 v[52:55], a[212:215], v[220:223], v[52:55]\n"
"v_mfma_f32_16x16x32_bf16 v[56:59], a[200:203], v[216:219], v[56:59]\n"
"v_mfma_f32_16x16x32_bf16 v[56:59], a[216:219], v[220:223], v[56:59]\n"
"v_mfma_f32_16x16x32_bf16 v[60:63], a[204:207], v[216:219], v[60:63]\n"
"ds_read_b64_tr_b16 v[216:217], v227 offset:24576\n"
"buffer_load_dwordx4 a[72:75], v236, s[40:43], s64 offen offset:1024 sc1\n"
"v_mfma_f32_16x16x32_bf16 v[60:63], a[220:223], v[220:223], v[60:63]\n"
"ds_read_b64_tr_b16 v[218:219], v227 offset:25088\n"
"s_waitcnt lgkmcnt(10)\n"
"v_mfma_f32_16x16x32_bf16 v[0:3], a[224:227], v[192:195], v[0:3]\n"
"ds_read_b64_tr_b16 v[220:221], v227 offset:28672\n"
"v_mfma_f32_16x16x32_bf16 v[0:3], a[240:243], v[196:199], v[0:3]\n"
"ds_read_b64_tr_b16 v[222:223], v227 offset:29184\n"
"v_mfma_f32_16x16x32_bf16 v[4:7], a[228:231], v[192:195], v[4:7]\n"
"v_mfma_f32_16x16x32_bf16 v[4:7], a[244:247], v[196:199], v[4:7]\n"
"v_mfma_f32_16x16x32_bf16 v[8:11], a[232:235], v[192:195], v[8:11]\n"
"v_mfma_f32_16x16x32_bf16 v[8:11], a[248:251], v[196:199], v[8:11]\n"
"v_mfma_f32_16x16x32_bf16 v[12:15], a[236:239], v[192:195], v[12:15]\n"
"s_waitcnt vmcnt(41)\n"
"s_barrier\n"
"v_mfma_f32_16x16x32_bf16 v[12:15], a[252:255], v[196:199], v[12:15]\n"
"ds_read_b64_tr_b16 v[192:193], v224 offset:32768\n"
"s_waitcnt lgkmcnt(9)\n"
"v_mfma_f32_16x16x32_bf16 v[16:19], a[224:227], v[200:203], v[16:19]\n"
"ds_read_b64_tr_b16 v[194:195], v224 offset:33280\n"
"buffer_load_dwordx4 a[88:91], v236, s[40:43], s64 offen offset:1280 sc1\n"
"v_mfma_f32_16x16x32_bf16 v[16:19], a[240:243], v[204:207], v[16:19]\n"
"ds_read_b64_tr_b16 v[196:197], v224 offset:36864\n"
"v_mfma_f32_16x16x32_bf16 v[20:23], a[228:231], v[200:203], v[20:23]\n"
"ds_read_b64_tr_b16 v[198:199], v224 offset:37376\n"
"v_mfma_f32_16x16x32_bf16 v[20:23], a[244:247], v[204:207], v[20:23]\n"
"v_mfma_f32_16x16x32_bf16 v[24:27], a[232:235], v[200:203], v[24:27]\n"
"v_mfma_f32_16x16x32_bf16 v[24:27], a[248:251], v[204:207], v[24:27]\n"
"v_mfma_f32_16x16x32_bf16 v[28:31], a[236:239], v[200:203], v[28:31]\n"
"ds_read_b64_tr_b16 v[200:201], v225 offset:32768\n"
"v_mfma_f32_16x16x32_bf16 v[28:31], a[252:255], v[204:207], v[28:31]\n"
"ds_read_b64_tr_b16 v[202:203], v225 offset:33280\n"
"s_waitcnt lgkmcnt(10)\n"
"v_mfma_f32_16x16x32_bf16 v[32:35], a[224:227], v[208:211], v[32:35]\n"
"ds_read_b64_tr_b16 v[204:205], v225 offset:36864\n"
"v_mfma_f32_16x16x32_bf16 v[32:35], a[240:243], v[212:215], v[32:35]\n"
"ds_read_b64_tr_b16 v[206:207], v225 offset:37376\n"
"buffer_load_dwordx4 a[76:79], v236, s[40:43], s65 offen offset:1024 sc1\n"
"v_mfma_f32_16x16x32_bf16 v[36:39], a[228:231], v[208:211], v[36:39]\n"
"v_mfma_f32_16x16x32_bf16 v[36:39], a[244:247], v[212:215], v[36:39]\n"
"v_mfma_f32_16x16x32_bf16 v[40:43], a[232:235], v[208:211], v[40:43]\n"
"v_mfma_f32_16x16x32_bf16 v[40:43], a[248:251], v[212:215], v[40:43]\n"
"v_mfma_f32_16x16x32_bf16 v[44:47], a[236:239], v[208:211], v[44:47]\n"
"s_add_u32 m0, s58, 106496\n"
"s_mov_b32 s60, 2097280\n"
"v_mfma_f32_16x16x32_bf16 v[44:47], a[252:255], v[212:215], v[44:47]\n"
"ds_read_b64_tr_b16 v[208:209], v226 offset:32768\n"
"s_waitcnt lgkmcnt(9)\n"
"v_mfma_f32_16x16x32_bf16 v[48:51], a[224:227], v[216:219], v[48:51]\n"
"buffer_load_dwordx4 v234, s[36:39], s60 offen offset:0 lds\n"
"v_mfma_f32_16x16x32_bf16 v[48:51], a[240:243], v[220:223], v[48:51]\n"
"buffer_load_dwordx4 v235, s[36:39], s60 offen offset:1024 lds\n"
"v_mfma_f32_16x16x32_bf16 v[52:55], a[228:231], v[216:219], v[52:55]\n"
"ds_read_b64_tr_b16 v[210:211], v226 offset:33280\n"
"buffer_load_dwordx4 a[92:95], v236, s[40:43], s65 offen offset:1280 sc1\n"
"v_mfma_f32_16x16x32_bf16 v[52:55], a[244:247], v[220:223], v[52:55]\n"
"ds_read_b64_tr_b16 v[212:213], v226 offset:36864\n"
"v_mfma_f32_16x16x32_bf16 v[56:59], a[232:235], v[216:219], v[56:59]\n"
"ds_read_b64_tr_b16 v[214:215], v226 offset:37376\n"
"v_mfma_f32_16x16x32_bf16 v[56:59], a[248:251], v[220:223], v[56:59]\n"
"v_mfma_f32_16x16x32_bf16 v[60:63], a[236:239], v[216:219], v[60:63]\n"
"ds_read_b64_tr_b16 v[216:217], v227 offset:32768\n"
"v_mfma_f32_16x16x32_bf16 v[60:63], a[252:255], v[220:223], v[60:63]\n"
"ds_read_b64_tr_b16 v[218:219], v227 offset:33280\n"
"s_waitcnt vmcnt(32) lgkmcnt(10)\n"
"v_mfma_f32_16x16x32_bf16 v[64:67], v[192:195], a[0:3], 0\n"
"ds_read_b64_tr_b16 v[220:221], v227 offset:36864\n"
"v_mfma_f32_16x16x32_bf16 v[64:67], v[196:199], a[16:19], v[64:67]\n"
"ds_read_b64_tr_b16 v[222:223], v227 offset:37376\n"
"v_mfma_f32_16x16x32_bf16 v[68:71], v[192:195], a[4:7], 0\n"
"v_mfma_f32_16x16x32_bf16 v[68:71], v[196:199], a[20:23], v[68:71]\n"
"v_mfma_f32_16x16x32_bf16 v[72:75], v[192:195], a[8:11], 0\n"
"buffer_load_dwordx4 a[96:99], v236, s[40:43], s62 offen offset:1536 sc1\n"
"v_cvt_pk_bf16_f32 v0, v0, v1\n"
"v_mfma_f32_16x16x32_bf16 v[72:75], v[196:199], a[24:27], v[72:75]\n"
"v_cvt_pk_bf16_f32 v1, v2, v3\n"
"v_cvt_pk_bf16_f32 v2, v4, v5\n"
"v_mfma_f32_16x16x32_bf16 v[76:79], v[192:195], a[12:15], 0\n"
"s_add_u32 m0, s58, 114688\n"
"s_mov_b32 s60, 2621440\n"
"v_mfma_f32_16x16x32_bf16 v[76:79], v[196:199], a[28:31], v[76:79]\n"
"ds_read_b64_tr_b16 v[192:193], v224 offset:40960\n"
"s_waitcnt lgkmcnt(9)\n"
"v_mfma_f32_16x16x32_bf16 v[80:83], v[200:203], a[0:3], 0\n"
"buffer_load_dwordx4 v234, s[36:39], s60 offen offset:0 lds\n"
"v_mfma_f32_16x16x32_bf16 v[80:83], v[204:207], a[16:19], v[80:83]\n"
"buffer_load_dwordx4 v235, s[36:39], s60 offen offset:1024 lds\n"
"v_mfma_f32_16x16x32_bf16 v[84:87], v[200:203], a[4:7], 0\n"
"ds_read_b64_tr_b16 v[194:195], v224 offset:41472\n"
"v_cvt_pk_bf16_f32 v3, v6, v7\n"
"v_mfma_f32_16x16x32_bf16 v[84:87], v[204:207], a[20:23], v[84:87]\n"
"ds_read_b64_tr_b16 v[196:197], v224 offset:45056\n"
"s_add_u32 s67, s66, 12288\n"
"v_mfma_f32_16x16x32_bf16 v[88:91], v[200:203], a[8:11], 0\n"
"ds_read_b64_tr_b16 v[198:199], v224 offset:45568\n"
"v_mfma_f32_16x16x32_bf16 v[88:91], v[204:207], a[24:27], v[88:91]\n"
"buffer_load_dwordx4 a[112:115], v236, s[40:43], s62 offen offset:1792 sc1\n"
"v_mfma_f32_16x16x32_bf16 v[92:95], v[200:203], a[12:15], 0\n"
"ds_read_b64_tr_b16 v[200:201], v225 offset:40960\n"
"v_mfma_f32_16x16x32_bf16 v[92:95], v[204:207], a[28:31], v[92:95]\n"
"ds_read_b64_tr_b16 v[202:203], v225 offset:41472\n"
"s_waitcnt lgkmcnt(10)\n"
"v_mfma_f32_16x16x32_bf16 v[96:99], v[208:211], a[0:3], 0\n"
"ds_read_b64_tr_b16 v[204:205], v225 offset:45056\n"
"v_mfma_f32_16x16x32_bf16 v[96:99], v[212:215], a[16:19], v[96:99]\n"
"ds_read_b64_tr_b16 v[206:207], v225 offset:45568\n"
"v_mfma_f32_16x16x32_bf16 v[100:103], v[208:211], a[4:7], 0\n"
"buffer_store_dwordx4 v[0:3], v232, s[44:47], s67 offen sc1\n"
"v_mfma_f32_16x16x32_bf16 v[100:103], v[212:215], a[20:23], v[100:103]\n"
"v_mfma_f32_16x16x32_bf16 v[104:107], v[208:211], a[8:11], 0\n"
"v_mfma_f32_16x16x32_bf16 v[104:107], v[212:215], a[24:27], v[104:107]\n"
"v_mfma_f32_16x16x32_bf16 v[108:111], v[208:211], a[12:15], 0\n"
"s_add_u32 m0, s58, 8192\n"
"s_mov_b32 s60, 2621568\n"
"v_mfma_f32_16x16x32_bf16 v[108:111], v[212:215], a[28:31], v[108:111]\n"
"ds_read_b64_tr_b16 v[208:209], v226 offset:40960\n"
"s_waitcnt lgkmcnt(9)\n"
"v_mfma_f32_16x16x32_bf16 v[112:115], v[216:219], a[0:3], 0\n"
"buffer_load_dwordx4 v234, s[36:39], s60 offen offset:0 lds\n"
"v_mfma_f32_16x16x32_bf16 v[112:115], v[220:223], a[16:19], v[112:115]\n"
"buffer_load_dwordx4 v235, s[36:39], s60 offen offset:1024 lds\n"
"v_mfma_f32_16x16x32_bf16 v[116:119], v[216:219], a[4:7], 0\n"
"ds_read_b64_tr_b16 v[210:211], v226 offset:41472\n"
"buffer_load_dwordx4 a[100:103], v236, s[40:43], s63 offen offset:1536 sc1\n"
"v_mfma_f32_16x16x32_bf16 v[116:119], v[220:223], a[20:23], v[116:119]\n"
"ds_read_b64_tr_b16 v[212:213], v226 offset:45056\n"
"v_cvt_pk_bf16_f32 v8, v8, v9\n"
"v_mfma_f32_16x16x32_bf16 v[120:123], v[216:219], a[8:11], 0\n"
"ds_read_b64_tr_b16 v[214:215], v226 offset:45568\n"
"v_cvt_pk_bf16_f32 v9, v10, v11\n"
"v_mfma_f32_16x16x32_bf16 v[120:123], v[220:223], a[24:27], v[120:123]\n"
"v_cvt_pk_bf16_f32 v10, v12, v13\n"
"v_cvt_pk_bf16_f32 v11, v14, v15\n"
"v_mfma_f32_16x16x32_bf16 v[124:127], v[216:219], a[12:15], 0\n"
"ds_read_b64_tr_b16 v[216:217], v227 offset:40960\n"
"s_add_u32 s67, s66, 28672\n"
"v_mfma_f32_16x16x32_bf16 v[124:127], v[220:223], a[28:31], v[124:127]\n"
"ds_read_b64_tr_b16 v[218:219], v227 offset:41472\n"
"s_waitcnt lgkmcnt(10)\n"
"v_mfma_f32_16x16x32_bf16 v[128:131], v[192:195], a[0:3], 0\n"
"ds_read_b64_tr_b16 v[220:221], v227 offset:45056\n"
"buffer_load_dwordx4 a[116:119], v236, s[40:43], s63 offen offset:1792 sc1\n"
"v_mfma_f32_16x16x32_bf16 v[128:131], v[196:199], a[16:19], v[128:131]\n"
"ds_read_b64_tr_b16 v[222:223], v227 offset:45568\n"
"v_mfma_f32_16x16x32_bf16 v[132:135], v[192:195], a[4:7], 0\n"
"buffer_store_dwordx4 v[8:11], v232, s[44:47], s67 offen sc1\n"
"v_mfma_f32_16x16x32_bf16 v[132:135], v[196:199], a[20:23], v[132:135]\n"
"v_mfma_f32_16x16x32_bf16 v[136:139], v[192:195], a[8:11], 0\n"
"v_mfma_f32_16x16x32_bf16 v[136:139], v[196:199], a[24:27], v[136:139]\n"
"v_mfma_f32_16x16x32_bf16 v[140:143], v[192:195], a[12:15], 0\n"
"s_add_u32 m0, s58, 16384\n"
"s_mov_b32 s60, 3145728\n"
"v_mfma_f32_16x16x32_bf16 v[140:143], v[196:199], a[28:31], v[140:143]\n"
"ds_read_b64_tr_b16 v[192:193], v224 offset:49152\n"
"s_waitcnt lgkmcnt(9)\n"
"v_mfma_f32_16x16x32_bf16 v[144:147], v[200:203], a[0:3], 0\n"
"buffer_load_dwordx4 v234, s[36:39], s60 offen offset:0 lds\n"
"v_mfma_f32_16x16x32_bf16 v[144:147], v[204:207], a[16:19], v[144:147]\n"
"buffer_load_dwordx4 v235, s[36:39], s60 offen offset:1024 lds\n"
"v_mfma_f32_16x16x32_bf16 v[148:151], v[200:203], a[4:7], 0\n"
"ds_read_b64_tr_b16 v[194:195], v224 offset:49664\n"
"buffer_load_dwordx4 a[104:107], v236, s[40:43], s64 offen offset:1536 sc1\n"
"v_mfma_f32_16x16x32_bf16 v[148:151], v[204:207], a[20:23], v[148:151]\n"
"ds_read_b64_tr_b16 v[196:197], v224 offset:53248\n"
"v_mfma_f32_16x16x32_bf16 v[152:155], v[200:203], a[8:11], 0\n"
"ds_read_b64_tr_b16 v[198:199], v224 offset:53760\n"
"v_mfma_f32_16x16x32_bf16 v[152:155], v[204:207], a[24:27], v[152:155]\n"
"v_mfma_f32_16x16x32_bf16 v[156:159], v[200:203], a[12:15], 0\n"
"ds_read_b64_tr_b16 v[200:201], v225 offset:49152\n"
"v_mfma_f32_16x16x32_bf16 v[156:159], v[204:207], a[28:31], v[156:159]\n"
"ds_read_b64_tr_b16 v[202:203], v225 offset:49664\n"
"s_waitcnt lgkmcnt(10)\n"
"v_mfma_f32_16x16x32_bf16 v[160:163], v[208:211], a[0:3], 0\n"
"ds_read_b64_tr_b16 v[204:205], v225 offset:53248\n"
"v_cvt_pk_bf16_f32 v16, v16, v17\n"
"v_mfma_f32_16x16x32_bf16 v[160:163], v[212:215], a[16:19], v[160:163]\n"
"ds_read_b64_tr_b16 v[206:207], v225 offset:53760\n"
"v_cvt_pk_bf16_f32 v17, v18, v19\n"
"v_mfma_f32_16x16x32_bf16 v[164:167], v[208:211], a[4:7], 0\n"
"v_cvt_pk_bf16_f32 v18, v20, v21\n"
"v_cvt_pk_bf16_f32 v19, v22, v23\n"
"v_mfma_f32_16x16x32_bf16 v[164:167], v[212:215], a[20:23], v[164:167]\n"
"buffer_load_dwordx4 a[120:123], v236, s[40:43], s64 offen offset:1792 sc1\n"
"s_add_u32 s67, s66, 13312\n"
"v_mfma_f32_16x16x32_bf16 v[168:171], v[208:211], a[8:11], 0\n"
"buffer_store_dwordx4 v[16:19], v232, s[44:47], s67 offen sc1\n"
"v_mfma_f32_16x16x32_bf16 v[168:171], v[212:215], a[24:27], v[168:171]\n"
"v_mfma_f32_16x16x32_bf16 v[172:175], v[208:211], a[12:15], 0\n"
"ds_read_b64_tr_b16 v[208:209], v226 offset:49152\n"
"v_mfma_f32_16x16x32_bf16 v[172:175], v[212:215], a[28:31], v[172:175]\n"
"ds_read_b64_tr_b16 v[210:211], v226 offset:49664\n"
"s_waitcnt lgkmcnt(10)\n"
"v_mfma_f32_16x16x32_bf16 v[176:179], v[216:219], a[0:3], 0\n"
"ds_read_b64_tr_b16 v[212:213], v226 offset:53248\n"
"v_mfma_f32_16x16x32_bf16 v[176:179], v[220:223], a[16:19], v[176:179]\n"
"ds_read_b64_tr_b16 v[214:215], v226 offset:53760\n"
"v_mfma_f32_16x16x32_bf16 v[180:183], v[216:219], a[4:7], 0\n"
"v_mfma_f32_16x16x32_bf16 v[180:183], v[220:223], a[20:23], v[180:183]\n"
"v_mfma_f32_16x16x32_bf16 v[184:187], v[216:219], a[8:11], 0\n"
"v_mfma_f32_16x16x32_bf16 v[184:187], v[220:223], a[24:27], v[184:187]\n"
"buffer_load_dwordx4 a[108:111], v236, s[40:43], s65 offen offset:1536 sc1\n"
"v_mfma_f32_16x16x32_bf16 v[188:191], v[216:219], a[12:15], 0\n"
"ds_read_b64_tr_b16 v[216:217], v227 offset:49152\n"
"v_mfma_f32_16x16x32_bf16 v[188:191], v[220:223], a[28:31], v[188:191]\n"
"ds_read_b64_tr_b16 v[218:219], v227 offset:49664\n"
"s_waitcnt vmcnt(29) lgkmcnt(10)\n"
"v_mfma_f32_16x16x32_bf16 v[64:67], v[192:195], a[32:35], v[64:67]\n"
"ds_read_b64_tr_b16 v[220:221], v227 offset:53248\n"
"v_mfma_f32_16x16x32_bf16 v[64:67], v[196:199], a[48:51], v[64:67]\n"
"ds_read_b64_tr_b16 v[222:223], v227 offset:53760\n"
"v_mfma_f32_16x16x32_bf16 v[68:71], v[192:195], a[36:39], v[68:71]\n"
"s_lshr_b32 s96, s10, 3\n"
"s_lshl_b32 s96, s96, 2\n"
"v_mfma_f32_16x16x32_bf16 v[68:71], v[196:199], a[52:55], v[68:71]\n"
"s_add_u32 s96, s96, s11\n"
"s_lshl_b32 s96, s96, 7\n"
"v_mfma_f32_16x16x32_bf16 v[72:75], v[192:195], a[40:43], v[72:75]\n"
"s_add_u32 s96, s96, 3072\n"
"v_lshl_add_u32 v242, v232, 10, s96\n"
"v_mfma_f32_16x16x32_bf16 v[72:75], v[196:199], a[56:59], v[72:75]\n"
"v_min_u32 v242, Lslow_%=-Lct1_%=, v242\n"
"v_mfma_f32_16x16x32_bf16 v[76:79], v[192:195], a[44:47], v[76:79]\n"
"ds_read_b64_tr_b16 v[192:193], v224 offset:0\n"
"buffer_load_dwordx4 a[124:127], v236, s[40:43], s65 offen offset:1792 sc1\n"
"v_mfma_f32_16x16x32_bf16 v[76:79], v[196:199], a[60:63], v[76:79]\n"
"ds_read_b64_tr_b16 v[194:195], v224 offset:512\n"
"s_waitcnt lgkmcnt(10)\n"
"v_mfma_f32_16x16x32_bf16 v[80:83], v[200:203], a[32:35], v[80:83]\n"
"ds_read_b64_tr_b16 v[196:197], v224 offset:4096\n"
"v_cvt_pk_bf16_f32 v24, v24, v25\n"
"v_mfma_f32_16x16x32_bf16 v[80:83], v[204:207], a[48:51], v[80:83]\n"
"ds_read_b64_tr_b16 v[198:199], v224 offset:4608\n"
"v_cvt_pk_bf16_f32 v25, v26, v27\n"
"v_mfma_f32_16x16x32_bf16 v[84:87], v[200:203], a[36:39], v[84:87]\n"
"v_cvt_pk_bf16_f32 v26, v28, v29\n"
"v_cvt_pk_bf16_f32 v27, v30, v31\n"
"v_mfma_f32_16x16x32_bf16 v[84:87], v[204:207], a[52:55], v[84:87]\n"
"s_add_u32 s67, s66, 29696\n"
"v_mfma_f32_16x16x32_bf16 v[88:91], v[200:203], a[40:43], v[88:91]\n"
"s_getpc_b64 s[96:97]\n"
"Lct1_%=:\n"
"s_mov_b64 exec, 1\n"
"global_load_dword v243, v242, s[96:97]\n"
"s_mov_b64 exec, -1\n"
"v_mfma_f32_16x16x32_bf16 v[88:91], v[204:207], a[56:59], v[88:91]\n"
"buffer_store_dwordx4 v[24:27], v232, s[44:47], s67 offen sc1\n"
"v_mfma_f32_16x16x32_bf16 v[92:95], v[200:203], a[44:47], v[92:95]\n"
"ds_read_b64_tr_b16 v[200:201], v225 offset:0\n"
"v_mfma_f32_16x16x32_bf16 v[92:95], v[204:207], a[60:63], v[92:95]\n"
"ds_read_b64_tr_b16 v[202:203], v225 offset:512\n"
"s_waitcnt lgkmcnt(10)\n"
"v_mfma_f32_16x16x32_bf16 v[96:99], v[208:211], a[32:35], v[96:99]\n"
"ds_read_b64_tr_b16 v[204:205], v225 offset:4096\n"
"buffer_load_dwordx4 a[128:131], v236, s[40:43], s62 offen offset:2048 sc1\n"
"v_mfma_f32_16x16x32_bf16 v[96:99], v[212:215], a[48:51], v[96:99]\n"
"ds_read_b64_tr_b16 v[206:207], v225 offset:4608\n"
"v_mfma_f32_16x16x32_bf16 v[100:103], v[208:211], a[36:39], v[100:103]\n"
"v_mfma_f32_16x16x32_bf16 v[100:103], v[212:215], a[52:55], v[100:103]\n"
"v_mfma_f32_16x16x32_bf16 v[104:107], v[208:211], a[40:43], v[104:107]\n"
"v_mfma_f32_16x16x32_bf16 v[104:107], v[212:215], a[56:59], v[104:107]\n"
"v_mfma_f32_16x16x32_bf16 v[108:111], v[208:211], a[44:47], v[108:111]\n"
"ds_read_b64_tr_b16 v[208:209], v226 offset:0\n"
"v_mfma_f32_16x16x32_bf16 v[108:111], v[212:215], a[60:63], v[108:111]\n"
"ds_read_b64_tr_b16 v[210:211], v226 offset:512\n"
"s_waitcnt lgkmcnt(10)\n"
"v_mfma_f32_16x16x32_bf16 v[112:115], v[216:219], a[32:35], v[112:115]\n"
"ds_read_b64_tr_b16 v[212:213], v226 offset:4096\n"
"v_mfma_f32_16x16x32_bf16 v[112:115], v[220:223], a[48:51], v[112:115]\n"
"ds_read_b64_tr_b16 v[214:215], v226 offset:4608\n"
"buffer_load_dwordx4 a[144:147], v236, s[40:43], s62 offen offset:2304 sc1\n"
"v_mfma_f32_16x16x32_bf16 v[116:119], v[216:219], a[36:39], v[116:119]\n"
"v_mfma_f32_16x16x32_bf16 v[116:119], v[220:223], a[52:55], v[116:119]\n"
"v_mfma_f32_16x16x32_bf16 v[120:123], v[216:219], a[40:43], v[120:123]\n"
"v_cvt_pk_bf16_f32 v32, v32, v33\n"
"v_cvt_pk_bf16_f32 v33, v34, v35\n"
"v_mfma_f32_16x16x32_bf16 v[120:123], v[220:223], a[56:59], v[120:123]\n"
"v_cvt_pk_bf16_f32 v34, v36, v37\n"
"v_cvt_pk_bf16_f32 v35, v38, v39\n"
"v_mfma_f32_16x16x32_bf16 v[124:127], v[216:219], a[44:47], v[124:127]\n"
"ds_read_b64_tr_b16 v[216:217], v227 offset:0\n"
"s_add_u32 s67, s66, 14336\n"
"v_mfma_f32_16x16x32_bf16 v[124:127], v[220:223], a[60:63], v[124:127]\n"
"ds_read_b64_tr_b16 v[218:219], v227 offset:512\n"
"s_waitcnt lgkmcnt(10)\n"
"v_mfma_f32_16x16x32_bf16 v[128:131], v[192:195], a[32:35], v[128:131]\n"
"ds_read_b64_tr_b16 v[220:221], v227 offset:4096\n"
"v_mfma_f32_16x16x32_bf16 v[128:131], v[196:199], a[48:51], v[128:131]\n"
"ds_read_b64_tr_b16 v[222:223], v227 offset:4608\n"
"v_mfma_f32_16x16x32_bf16 v[132:135], v[192:195], a[36:39], v[132:135]\n"
"buffer_store_dwordx4 v[32:35], v232, s[44:47], s67 offen sc1\n"
"v_mfma_f32_16x16x32_bf16 v[132:135], v[196:199], a[52:55], v[132:135]\n"
"buffer_load_dwordx4 a[132:135], v236, s[40:43], s63 offen offset:2048 sc1\n"
"v_mfma_f32_16x16x32_bf16 v[136:139], v[192:195], a[40:43], v[136:139]\n"
"v_mfma_f32_16x16x32_bf16 v[136:139], v[196:199], a[56:59], v[136:139]\n"
"v_mfma_f32_16x16x32_bf16 v[140:143], v[192:195], a[44:47], v[140:143]\n"
"s_barrier\n"
"v_mfma_f32_16x16x32_bf16 v[140:143], v[196:199], a[60:63], v[140:143]\n"
"ds_read_b64_tr_b16 v[192:193], v228 offset:0\n"
"s_waitcnt lgkmcnt(9)\n"
"v_mfma_f32_16x16x32_bf16 v[144:147], v[200:203], a[32:35], v[144:147]\n"
"ds_read_b64_tr_b16 v[194:195], v228 offset:512\n"
"v_mfma_f32_16x16x32_bf16 v[144:147], v[204:207], a[48:51], v[144:147]\n"
"ds_read_b64_tr_b16 v[196:197], v228 offset:4096\n"
"v_mfma_f32_16x16x32_bf16 v[148:151], v[200:203], a[36:39], v[148:151]\n"
"ds_read_b64_tr_b16 v[198:199], v228 offset:4608\n"
"v_mfma_f32_16x16x32_bf16 v[148:151], v[204:207], a[52:55], v[148:151]\n"
"v_mfma_f32_16x16x32_bf16 v[152:155], v[200:203], a[40:43], v[152:155]\n"
"buffer_load_dwordx4 a[148:151], v236, s[40:43], s63 offen offset:2304 sc1\n"
"v_mfma_f32_16x16x32_bf16 v[152:155], v[204:207], a[56:59], v[152:155]\n"
"v_mfma_f32_16x16x32_bf16 v[156:159], v[200:203], a[44:47], v[156:159]\n"
"ds_read_b64_tr_b16 v[200:201], v229 offset:0\n"
"v_mfma_f32_16x16x32_bf16 v[156:159], v[204:207], a[60:63], v[156:159]\n"
"ds_read_b64_tr_b16 v[202:203], v229 offset:512\n"
"s_waitcnt lgkmcnt(10)\n"
"v_mfma_f32_16x16x32_bf16 v[160:163], v[208:211], a[32:35], v[160:163]\n"
"ds_read_b64_tr_b16 v[204:205], v229 offset:4096\n"
"v_mfma_f32_16x16x32_bf16 v[160:163], v[212:215], a[48:51], v[160:163]\n"
"ds_read_b64_tr_b16 v[206:207], v229 offset:4608\n"
"v_mfma_f32_16x16x32_bf16 v[164:167], v[208:211], a[36:39], v[164:167]\n"
"v_cvt_pk_bf16_f32 v40, v40, v41\n"
"v_cvt_pk_bf16_f32 v41, v42, v43\n"
"v_mfma_f32_16x16x32_bf16 v[164:167], v[212:215], a[52:55], v[164:167]\n"
"v_cvt_pk_bf16_f32 v42, v44, v45\n"
"v_cvt_pk_bf16_f32 v43, v46, v47\n"
"v_mfma_f32_16x16x32_bf16 v[168:171], v[208:211], a[40:43], v[168:171]\n"
"s_add_u32 s67, s66, 30720\n"
"v_mfma_f32_16x16x32_bf16 v[168:171], v[212:215], a[56:59], v[168:171]\n"
"buffer_store_dwordx4 v[40:43], v232, s[44:47], s67 offen sc1\n"
"v_mfma_f32_16x16x32_bf16 v[172:175], v[208:211], a[44:47], v[172:175]\n"
"s_add_u32 m0, s58, 24576\n"
"s_mov_b32 s60, 3145856\n"
"v_mfma_f32_16x16x32_bf16 v[172:175], v[212:215], a[60:63], v[172:175]\n"
"ds_read_b64_tr_b16 v[208:209], v230 offset:0\n"
"s_waitcnt lgkmcnt(9)\n"
"v_mfma_f32_16x16x32_bf16 v[176:179], v[216:219], a[32:35], v[176:179]\n"
"buffer_load_dwordx4 v234, s[36:39], s60 offen offset:0 lds\n"
"v_mfma_f32_16x16x32_bf16 v[176:179], v[220:223], a[48:51], v[176:179]\n"
"buffer_load_dwordx4 v235, s[36:39], s60 offen offset:1024 lds\n"
"v_mfma_f32_16x16x32_bf16 v[180:183], v[216:219], a[36:39], v[180:183]\n"
"ds_read_b64_tr_b16 v[210:211], v230 offset:512\n"
"buffer_load_dwordx4 a[136:139], v236, s[40:43], s64 offen offset:2048 sc1\n"
"v_mfma_f32_16x16x32_bf16 v[180:183], v[220:223], a[52:55], v[180:183]\n"
"ds_read_b64_tr_b16 v[212:213], v230 offset:4096\n"
"v_mfma_f32_16x16x32_bf16 v[184:187], v[216:219], a[40:43], v[184:187]\n"
"ds_read_b64_tr_b16 v[214:215], v230 offset:4608\n"
"v_mfma_f32_16x16x32_bf16 v[184:187], v[220:223], a[56:59], v[184:187]\n"
"v_mfma_f32_16x16x32_bf16 v[188:191], v[216:219], a[44:47], v[188:191]\n"
"ds_read_b64_tr_b16 v[216:217], v231 offset:0\n"
"v_mfma_f32_16x16x32_bf16 v[188:191], v[220:223], a[60:63], v[188:191]\n"
"ds_read_b64_tr_b16 v[218:219], v231 offset:512\n"
"s_waitcnt vmcnt(28) lgkmcnt(10)\n"
"v_mfma_f32_16x16x32_bf16 v[64:67], v[192:195], a[64:67], v[64:67]\n"
"ds_read_b64_tr_b16 v[220:221], v231 offset:4096\n"
"buffer_load_dwordx4 a[152:155], v236, s[40:43], s64 offen offset:2304 sc1\n"
"v_mfma_f32_16x16x32_bf16 v[64:67], v[196:199], a[80:83], v[64:67]\n"
"ds_read_b64_tr_b16 v[222:223], v231 offset:4608\n"
"v_mfma_f32_16x16x32_bf16 v[68:71], v[192:195], a[68:71], v[68:71]\n"
"v_mfma_f32_16x16x32_bf16 v[68:71], v[196:199], a[84:87], v[68:71]\n"
"v_mfma_f32_16x16x32_bf16 v[72:75], v[192:195], a[72:75], v[72:75]\n"
"v_mfma_f32_16x16x32_bf16 v[72:75], v[196:199], a[88:91], v[72:75]\n"
"v_mfma_f32_16x16x32_bf16 v[76:79], v[192:195], a[76:79], v[76:79]\n"
"s_add_u32 m0, s58, 32768\n"
"s_mov_b32 s60, 3670016\n"
"v_mfma_f32_16x16x32_bf16 v[76:79], v[196:199], a[92:95], v[76:79]\n"
"ds_read_b64_tr_b16 v[192:193], v228 offset:8192\n"
"s_waitcnt lgkmcnt(9)\n"
"v_mfma_f32_16x16x32_bf16 v[80:83], v[200:203], a[64:67], v[80:83]\n"
"buffer_load_dwordx4 v234, s[36:39], s60 offen offset:0 lds\n"
"v_mfma_f32_16x16x32_bf16 v[80:83], v[204:207], a[80:83], v[80:83]\n"
"buffer_load_dwordx4 v235, s[36:39], s60 offen offset:1024 lds\n"
"v_mfma_f32_16x16x32_bf16 v[84:87], v[200:203], a[68:71], v[84:87]\n"
"ds_read_b64_tr_b16 v[194:195], v228 offset:8704\n"
"buffer_load_dwordx4 a[140:143], v236, s[40:43], s65 offen offset:2048 sc1\n"
"v_mfma_f32_16x16x32_bf16 v[84:87], v[204:207], a[84:87], v[84:87]\n"
"ds_read_b64_tr_b16 v[196:197], v228 offset:12288\n"
"v_cvt_pk_bf16_f32 v48, v48, v49\n"
"v_mfma_f32_16x16x32_bf16 v[88:91], v[200:203], a[72:75], v[88:91]\n"
"ds_read_b64_tr_b16 v[198:199], v228 offset:12800\n"
"v_cvt_pk_bf16_f32 v49, v50, v51\n"
"v_mfma_f32_16x16x32_bf16 v[88:91], v[204:207], a[88:91], v[88:91]\n"
"v_cvt_pk_bf16_f32 v50, v52, v53\n"
"v_cvt_pk_bf16_f32 v51, v54, v55\n"
"v_mfma_f32_16x16x32_bf16 v[92:95], v[200:203], a[76:79], v[92:95]\n"
"ds_read_b64_tr_b16 v[200:201], v229 offset:8192\n"
"s_add_u32 s67, s66, 15360\n"
"v_mfma_f32_16x16x32_bf16 v[92:95], v[204:207], a[92:95], v[92:95]\n"
"ds_read_b64_tr_b16 v[202:203], v229 offset:8704\n"
"s_waitcnt lgkmcnt(10)\n"
"v_mfma_f32_16x16x32_bf16 v[96:99], v[208:211], a[64:67], v[96:99]\n"
"ds_read_b64_tr_b16 v[204:205], v229 offset:12288\n"
"v_mfma_f32_16x16x32_bf16 v[96:99], v[212:215], a[80:83], v[96:99]\n"
"ds_read_b64_tr_b16 v[206:207], v229 offset:12800\n"
"v_mfma_f32_16x16x32_bf16 v[100:103], v[208:211], a[68:71], v[100:103]\n"
"buffer_load_dwordx4 a[156:159], v236, s[40:43], s65 offen offset:2304 sc1\n"
"v_mfma_f32_16x16x32_bf16 v[100:103], v[212:215], a[84:87], v[100:103]\n"
"buffer_store_dwordx4 v[48:51], v232, s[44:47], s67 offen sc1\n"
"v_mfma_f32_16x16x32_bf16 v[104:107], v[208:211], a[72:75], v[104:107]\n"
"v_mfma_f32_16x16x32_bf16 v[104:107], v[212:215], a[88:91], v[104:107]\n"
"v_mfma_f32_16x16x32_bf16 v[108:111], v[208:211], a[76:79], v[108:111]\n"
"s_add_u32 m0, s58, 40960\n"
"s_mov_b32 s60, 3670144\n"
"v_mfma_f32_16x16x32_bf16 v[108:111], v[212:215], a[92:95], v[108:111]\n"
"ds_read_b64_tr_b16 v[208:209], v230 offset:8192\n"
"s_waitcnt lgkmcnt(9)\n"
"v_mfma_f32_16x16x32_bf16 v[112:115], v[216:219], a[64:67], v[112:115]\n"
"buffer_load_dwordx4 v234, s[36:39], s60 offen offset:0 lds\n"
"v_mfma_f32_16x16x32_bf16 v[112:115], v[220:223], a[80:83], v[112:115]\n"
"buffer_load_dwordx4 v235, s[36:39], s60 offen offset:1024 lds\n"
"v_mfma_f32_16x16x32_bf16 v[116:119], v[216:219], a[68:71], v[116:119]\n"
"ds_read_b64_tr_b16 v[210:211], v230 offset:8704\n"
"s_lshl_b32 s60, s10, 14\n"
"v_mfma_f32_16x16x32_bf16 v[116:119], v[220:223], a[84:87], v[116:119]\n"
"ds_read_b64_tr_b16 v[212:213], v230 offset:12288\n"
"s_mov_b32 s36, s80\n"
"v_mfma_f32_16x16x32_bf16 v[120:123], v[216:219], a[72:75], v[120:123]\n"
"ds_read_b64_tr_b16 v[214:215], v230 offset:12800\n"
"buffer_load_dwordx4 a[160:163], v236, s[40:43], s62 offen offset:2560 sc1\n"
"v_mfma_f32_16x16x32_bf16 v[120:123], v[220:223], a[88:91], v[120:123]\n"
"s_and_b32 s37, s81, 0xffff\n"
"s_mov_b32 s38, 2097152\n"
"v_mfma_f32_16x16x32_bf16 v[124:127], v[216:219], a[76:79], v[124:127]\n"
"ds_read_b64_tr_b16 v[216:217], v231 offset:8192\n"
"v_cvt_pk_bf16_f32 v56, v56, v57\n"
"v_mfma_f32_16x16x32_bf16 v[124:127], v[220:223], a[92:95], v[124:127]\n"
"ds_read_b64_tr_b16 v[218:219], v231 offset:8704\n"
"s_waitcnt lgkmcnt(10)\n"
"v_mfma_f32_16x16x32_bf16 v[128:131], v[192:195], a[64:67], v[128:131]\n"
"ds_read_b64_tr_b16 v[220:221], v231 offset:12288\n"
"v_cvt_pk_bf16_f32 v57, v58, v59\n"
"v_mfma_f32_16x16x32_bf16 v[128:131], v[196:199], a[80:83], v[128:131]\n"
"ds_read_b64_tr_b16 v[222:223], v231 offset:12800\n"
"v_cvt_pk_bf16_f32 v58, v60, v61\n"
"v_mfma_f32_16x16x32_bf16 v[132:135], v[192:195], a[68:71], v[132:135]\n"
"v_cvt_pk_bf16_f32 v59, v62, v63\n"
"s_add_u32 s67, s66, 31744\n"
"v_mfma_f32_16x16x32_bf16 v[132:135], v[196:199], a[84:87], v[132:135]\n"
"buffer_store_dwordx4 v[56:59], v232, s[44:47], s67 offen sc1\n"
"v_mfma_f32_16x16x32_bf16 v[136:139], v[192:195], a[72:75], v[136:139]\n"
"s_lshl_b32 s67, s13, 11\n"
"s_add_u32 s67, s67, s15\n"
"v_mfma_f32_16x16x32_bf16 v[136:139], v[196:199], a[88:91], v[136:139]\n"
"s_add_u32 s67, s67, 196608\n"
"buffer_load_dwordx4 a[176:179], v236, s[40:43], s62 offen offset:2816 sc1\n"
"v_mfma_f32_16x16x32_bf16 v[140:143], v[192:195], a[76:79], v[140:143]\n"
"ds_read_b64_tr_b16 v[192:193], v228 offset:16384\n"
"s_lshr_b32 s66, s12, 1\n"
"v_mfma_f32_16x16x32_bf16 v[140:143], v[196:199], a[92:95], v[140:143]\n"
"ds_read_b64_tr_b16 v[194:195], v228 offset:16896\n"
"s_waitcnt lgkmcnt(10)\n"
"v_mfma_f32_16x16x32_bf16 v[144:147], v[200:203], a[64:67], v[144:147]\n"
"ds_read_b64_tr_b16 v[196:197], v228 offset:20480\n"
"s_lshl_b32 s15, s14, 0\n"
"v_mfma_f32_16x16x32_bf16 v[144:147], v[204:207], a[80:83], v[144:147]\n"
"ds_read_b64_tr_b16 v[198:199], v228 offset:20992\n"
"s_lshl_b32 s17, s66, 20\n"
"v_mfma_f32_16x16x32_bf16 v[148:151], v[200:203], a[68:71], v[148:151]\n"
"s_mov_b32 s39, 0x20000\n"
"s_mul_i32 s58, s11, 40960\n"
"v_mfma_f32_16x16x32_bf16 v[148:151], v[204:207], a[84:87], v[148:151]\n"
"v_mfma_f32_16x16x32_bf16 v[152:155], v[200:203], a[72:75], v[152:155]\n"
"v_mfma_f32_16x16x32_bf16 v[152:155], v[204:207], a[88:91], v[152:155]\n"
"v_mfma_f32_16x16x32_bf16 v[156:159], v[200:203], a[76:79], v[156:159]\n"
"ds_read_b64_tr_b16 v[200:201], v229 offset:16384\n"
"v_mfma_f32_16x16x32_bf16 v[156:159], v[204:207], a[92:95], v[156:159]\n"
"ds_read_b64_tr_b16 v[202:203], v229 offset:16896\n"
"s_waitcnt lgkmcnt(10)\n"
"v_mfma_f32_16x16x32_bf16 v[160:163], v[208:211], a[64:67], v[160:163]\n"
"ds_read_b64_tr_b16 v[204:205], v229 offset:20480\n"
"buffer_load_dwordx4 a[164:167], v236, s[40:43], s63 offen offset:2560 sc1\n"
"v_mfma_f32_16x16x32_bf16 v[160:163], v[212:215], a[80:83], v[160:163]\n"
"ds_read_b64_tr_b16 v[206:207], v229 offset:20992\n"
"v_subrev_u32 v240, 0, v245\n"
"v_mfma_f32_16x16x32_bf16 v[164:167], v[208:211], a[68:71], v[164:167]\n"
"v_ashrrev_i32 v240, 31, v240\n"
"v_and_b32 v240, 0xff800000, v240\n"
"v_mfma_f32_16x16x32_bf16 v[164:167], v[212:215], a[84:87], v[164:167]\n"
"v_subrev_u32 v241, 1, v245\n"
"v_ashrrev_i32 v241, 31, v241\n"
"v_mfma_f32_16x16x32_bf16 v[168:171], v[208:211], a[72:75], v[168:171]\n"
"v_and_b32 v241, 0xff800000, v241\n"
"v_subrev_u32 v242, 2, v245\n"
"v_mfma_f32_16x16x32_bf16 v[168:171], v[212:215], a[88:91], v[168:171]\n"
"v_ashrrev_i32 v242, 31, v242\n"
"v_and_b32 v242, 0xff800000, v242\n"
"v_mfma_f32_16x16x32_bf16 v[172:175], v[208:211], a[76:79], v[172:175]\n"
"ds_read_b64_tr_b16 v[208:209], v230 offset:16384\n"
"s_waitcnt vmcnt(22)\n"
"v_subrev_u32 v243, 3, v245\n"
"v_mfma_f32_16x16x32_bf16 v[172:175], v[212:215], a[92:95], v[172:175]\n"
"ds_read_b64_tr_b16 v[210:211], v230 offset:16896\n"
"s_waitcnt lgkmcnt(10)\n"
"v_mfma_f32_16x16x32_bf16 v[176:179], v[216:219], a[64:67], v[176:179]\n"
"ds_read_b64_tr_b16 v[212:213], v230 offset:20480\n"
"buffer_load_dwordx4 a[180:183], v236, s[40:43], s63 offen offset:2816 sc1\n"
"v_mfma_f32_16x16x32_bf16 v[176:179], v[220:223], a[80:83], v[176:179]\n"
"ds_read_b64_tr_b16 v[214:215], v230 offset:20992\n"
"v_ashrrev_i32 v243, 31, v243\n"
"v_mfma_f32_16x16x32_bf16 v[180:183], v[216:219], a[68:71], v[180:183]\n"
"v_and_b32 v243, 0xff800000, v243\n"
"v_mfma_f32_16x16x32_bf16 v[180:183], v[220:223], a[84:87], v[180:183]\n"
"v_mfma_f32_16x16x32_bf16 v[184:187], v[216:219], a[72:75], v[184:187]\n"
"v_mfma_f32_16x16x32_bf16 v[184:187], v[220:223], a[88:91], v[184:187]\n"
"v_mfma_f32_16x16x32_bf16 v[188:191], v[216:219], a[76:79], v[188:191]\n"
"ds_read_b64_tr_b16 v[216:217], v231 offset:16384\n"
"v_mfma_f32_16x16x32_bf16 v[188:191], v[220:223], a[92:95], v[188:191]\n"
"ds_read_b64_tr_b16 v[218:219], v231 offset:16896\n"
"s_waitcnt lgkmcnt(10)\n"
"v_mfma_f32_16x16x32_bf16 v[64:67], v[192:195], a[96:99], v[64:67]\n"
"ds_read_b64_tr_b16 v[220:221], v231 offset:20480\n"
"v_mfma_f32_16x16x32_bf16 v[64:67], v[196:199], a[112:115], v[64:67]\n"
"ds_read_b64_tr_b16 v[222:223], v231 offset:20992\n"
"v_mfma_f32_16x16x32_bf16 v[68:71], v[192:195], a[100:103], v[68:71]\n"
"buffer_load_dwordx4 a[168:171], v236, s[40:43], s64 offen offset:2560 sc1\n"
"v_mfma_f32_16x16x32_bf16 v[68:71], v[196:199], a[116:119], v[68:71]\n"
"v_mfma_f32_16x16x32_bf16 v[72:75], v[192:195], a[104:107], v[72:75]\n"
"v_mfma_f32_16x16x32_bf16 v[72:75], v[196:199], a[120:123], v[72:75]\n"
"v_mfma_f32_16x16x32_bf16 v[76:79], v[192:195], a[108:111], v[76:79]\n"
"ds_read_b64_tr_b16 v[192:193], v228 offset:24576\n"
"v_mfma_f32_16x16x32_bf16 v[76:79], v[196:199], a[124:127], v[76:79]\n"
"ds_read_b64_tr_b16 v[194:195], v228 offset:25088\n"
"s_waitcnt lgkmcnt(10)\n"
"v_mfma_f32_16x16x32_bf16 v[80:83], v[200:203], a[96:99], v[80:83]\n"
"ds_read_b64_tr_b16 v[196:197], v228 offset:28672\n"
"v_mfma_f32_16x16x32_bf16 v[80:83], v[204:207], a[112:115], v[80:83]\n"
"ds_read_b64_tr_b16 v[198:199], v228 offset:29184\n"
"v_mfma_f32_16x16x32_bf16 v[84:87], v[200:203], a[100:103], v[84:87]\n"
"v_mfma_f32_16x16x32_bf16 v[84:87], v[204:207], a[116:119], v[84:87]\n"
"buffer_load_dwordx4 a[184:187], v236, s[40:43], s64 offen offset:2816 sc1\n"
"v_mfma_f32_16x16x32_bf16 v[88:91], v[200:203], a[104:107], v[88:91]\n"
"v_mfma_f32_16x16x32_bf16 v[88:91], v[204:207], a[120:123], v[88:91]\n"
"v_mfma_f32_16x16x32_bf16 v[92:95], v[200:203], a[108:111], v[92:95]\n"
"ds_read_b64_tr_b16 v[200:201], v229 offset:24576\n"
"v_mfma_f32_16x16x32_bf16 v[92:95], v[204:207], a[124:127], v[92:95]\n"
"ds_read_b64_tr_b16 v[202:203], v229 offset:25088\n"
"s_waitcnt lgkmcnt(10)\n"
"v_mfma_f32_16x16x32_bf16 v[96:99], v[208:211], a[96:99], v[96:99]\n"
"ds_read_b64_tr_b16 v[204:205], v229 offset:28672\n"
"v_mfma_f32_16x16x32_bf16 v[96:99], v[212:215], a[112:115], v[96:99]\n"
"ds_read_b64_tr_b16 v[206:207], v229 offset:29184\n"
"v_mfma_f32_16x16x32_bf16 v[100:103], v[208:211], a[100:103], v[100:103]\n"
"v_mfma_f32_16x16x32_bf16 v[100:103], v[212:215], a[116:119], v[100:103]\n"
"v_mfma_f32_16x16x32_bf16 v[104:107], v[208:211], a[104:107], v[104:107]\n"
"v_mfma_f32_16x16x32_bf16 v[104:107], v[212:215], a[120:123], v[104:107]\n"
"buffer_load_dwordx4 a[172:175], v236, s[40:43], s65 offen offset:2560 sc1\n"
"v_mfma_f32_16x16x32_bf16 v[108:111], v[208:211], a[108:111], v[108:111]\n"
"ds_read_b64_tr_b16 v[208:209], v230 offset:24576\n"
"v_mfma_f32_16x16x32_bf16 v[108:111], v[212:215], a[124:127], v[108:111]\n"
"ds_read_b64_tr_b16 v[210:211], v230 offset:25088\n"
"s_waitcnt lgkmcnt(10)\n"
"v_mfma_f32_16x16x32_bf16 v[112:115], v[216:219], a[96:99], v[112:115]\n"
"ds_read_b64_tr_b16 v[212:213], v230 offset:28672\n"
"v_mfma_f32_16x16x32_bf16 v[112:115], v[220:223], a[112:115], v[112:115]\n"
"ds_read_b64_tr_b16 v[214:215], v230 offset:29184\n"
"v_mfma_f32_16x16x32_bf16 v[116:119], v[216:219], a[100:103], v[116:119]\n"
"v_mfma_f32_16x16x32_bf16 v[116:119], v[220:223], a[116:119], v[116:119]\n"
"v_mfma_f32_16x16x32_bf16 v[120:123], v[216:219], a[104:107], v[120:123]\n"
"v_mfma_f32_16x16x32_bf16 v[120:123], v[220:223], a[120:123], v[120:123]\n"
"v_mfma_f32_16x16x32_bf16 v[124:127], v[216:219], a[108:111], v[124:127]\n"
"ds_read_b64_tr_b16 v[216:217], v231 offset:24576\n"
"buffer_load_dwordx4 a[188:191], v236, s[40:43], s65 offen offset:2816 sc1\n"
"v_mfma_f32_16x16x32_bf16 v[124:127], v[220:223], a[124:127], v[124:127]\n"
"ds_read_b64_tr_b16 v[218:219], v231 offset:25088\n"
"s_waitcnt lgkmcnt(10)\n"
"v_mfma_f32_16x16x32_bf16 v[128:131], v[192:195], a[96:99], v[128:131]\n"
"ds_read_b64_tr_b16 v[220:221], v231 offset:28672\n"
"v_mfma_f32_16x16x32_bf16 v[128:131], v[196:199], a[112:115], v[128:131]\n"
"ds_read_b64_tr_b16 v[222:223], v231 offset:29184\n"
"v_mfma_f32_16x16x32_bf16 v[132:135], v[192:195], a[100:103], v[132:135]\n"
"v_mfma_f32_16x16x32_bf16 v[132:135], v[196:199], a[116:119], v[132:135]\n"
"v_mfma_f32_16x16x32_bf16 v[136:139], v[192:195], a[104:107], v[136:139]\n"
"v_mfma_f32_16x16x32_bf16 v[136:139], v[196:199], a[120:123], v[136:139]\n"
"v_mfma_f32_16x16x32_bf16 v[140:143], v[192:195], a[108:111], v[140:143]\n"
"s_waitcnt vmcnt(7)\n"
"s_barrier\n"
"v_mfma_f32_16x16x32_bf16 v[140:143], v[196:199], a[124:127], v[140:143]\n"
"ds_read_b64_tr_b16 v[192:193], v228 offset:32768\n"
"s_waitcnt lgkmcnt(9)\n"
"v_mfma_f32_16x16x32_bf16 v[144:147], v[200:203], a[96:99], v[144:147]\n"
"ds_read_b64_tr_b16 v[194:195], v228 offset:33280\n"
"buffer_load_dwordx4 a[192:195], v236, s[40:43], s62 offen offset:3072 sc1\n"
"v_mfma_f32_16x16x32_bf16 v[144:147], v[204:207], a[112:115], v[144:147]\n"
"ds_read_b64_tr_b16 v[196:197], v228 offset:36864\n"
"v_mfma_f32_16x16x32_bf16 v[148:151], v[200:203], a[100:103], v[148:151]\n"
"ds_read_b64_tr_b16 v[198:199], v228 offset:37376\n"
"v_mfma_f32_16x16x32_bf16 v[148:151], v[204:207], a[116:119], v[148:151]\n"
"v_mfma_f32_16x16x32_bf16 v[152:155], v[200:203], a[104:107], v[152:155]\n"
"v_mfma_f32_16x16x32_bf16 v[152:155], v[204:207], a[120:123], v[152:155]\n"
"v_mfma_f32_16x16x32_bf16 v[156:159], v[200:203], a[108:111], v[156:159]\n"
"ds_read_b64_tr_b16 v[200:201], v229 offset:32768\n"
"v_mfma_f32_16x16x32_bf16 v[156:159], v[204:207], a[124:127], v[156:159]\n"
"v_mov_b32 v250, s6\n"
"s_waitcnt lgkmcnt(9)\n"
"v_mfma_f32_16x16x32_bf16 v[160:163], v[208:211], a[96:99], v[160:163]\n"
"v_mov_b32 v251, s7\n"
"ds_read_b64_tr_b16 v[202:203], v229 offset:33280\n"
"v_mfma_f32_16x16x32_bf16 v[160:163], v[212:215], a[112:115], v[160:163]\n"
"ds_read_b64_tr_b16 v[204:205], v229 offset:36864\n"
"buffer_load_dwordx4 a[208:211], v236, s[40:43], s62 offen offset:3328 sc1\n"
"v_mfma_f32_16x16x32_bf16 v[164:167], v[208:211], a[100:103], v[164:167]\n"
"ds_read_b64_tr_b16 v[206:207], v229 offset:37376\n"
"v_mfma_f32_16x16x32_bf16 v[164:167], v[212:215], a[116:119], v[164:167]\n"
"v_mfma_f32_16x16x32_bf16 v[168:171], v[208:211], a[104:107], v[168:171]\n"
"v_mfma_f32_16x16x32_bf16 v[168:171], v[212:215], a[120:123], v[168:171]\n"
"buffer_store_dwordx2 v[250:251], v237, s[40:43], s67 offen sc1\n"
"s_lshl_b32 s67, s18, 3\n"
"v_mfma_f32_16x16x32_bf16 v[172:175], v[208:211], a[108:111], v[172:175]\n"
"ds_read_b64_tr_b16 v[208:209], v230 offset:32768\n"
"v_and_b32 v237, 0x0fffffff, v237\n"
"v_mfma_f32_16x16x32_bf16 v[172:175], v[212:215], a[124:127], v[172:175]\n"
"ds_read_b64_tr_b16 v[210:211], v230 offset:33280\n"
"s_waitcnt lgkmcnt(10)\n"
"v_mfma_f32_16x16x32_bf16 v[176:179], v[216:219], a[96:99], v[176:179]\n"
"ds_read_b64_tr_b16 v[212:213], v230 offset:36864\n"
"v_mfma_f32_16x16x32_bf16 v[176:179], v[220:223], a[112:115], v[176:179]\n"
"ds_read_b64_tr_b16 v[214:215], v230 offset:37376\n"
"v_mfma_f32_16x16x32_bf16 v[180:183], v[216:219], a[100:103], v[180:183]\n"
"v_mfma_f32_16x16x32_bf16 v[180:183], v[220:223], a[116:119], v[180:183]\n"
"buffer_load_dwordx4 a[196:199], v236, s[40:43], s63 offen offset:3072 sc1\n"
"v_mfma_f32_16x16x32_bf16 v[184:187], v[216:219], a[104:107], v[184:187]\n"
"v_mfma_f32_16x16x32_bf16 v[184:187], v[220:223], a[120:123], v[184:187]\n"
"v_mfma_f32_16x16x32_bf16 v[188:191], v[216:219], a[108:111], v[188:191]\n"
"ds_read_b64_tr_b16 v[216:217], v231 offset:32768\n"
"v_mfma_f32_16x16x32_bf16 v[188:191], v[220:223], a[124:127], v[188:191]\n"
"ds_read_b64_tr_b16 v[218:219], v231 offset:33280\n"
"s_waitcnt lgkmcnt(10)\n"
"v_mfma_f32_16x16x32_bf16 v[64:67], v[192:195], a[128:131], v[64:67]\n"
"ds_read_b64_tr_b16 v[220:221], v231 offset:36864\n"
"v_mfma_f32_16x16x32_bf16 v[64:67], v[196:199], a[144:147], v[64:67]\n"
"ds_read_b64_tr_b16 v[222:223], v231 offset:37376\n"
"v_mfma_f32_16x16x32_bf16 v[68:71], v[192:195], a[132:135], v[68:71]\n"
"v_mfma_f32_16x16x32_bf16 v[68:71], v[196:199], a[148:151], v[68:71]\n"
"v_mfma_f32_16x16x32_bf16 v[72:75], v[192:195], a[136:139], v[72:75]\n"
"buffer_load_dwordx4 a[212:215], v236, s[40:43], s63 offen offset:3328 sc1\n"
"v_mfma_f32_16x16x32_bf16 v[72:75], v[196:199], a[152:155], v[72:75]\n"
"v_mfma_f32_16x16x32_bf16 v[76:79], v[192:195], a[140:143], v[76:79]\n"
"ds_read_b64_tr_b16 v[192:193], v228 offset:40960\n"
"v_mfma_f32_16x16x32_bf16 v[76:79], v[196:199], a[156:159], v[76:79]\n"
"ds_read_b64_tr_b16 v[194:195], v228 offset:41472\n"
"s_waitcnt lgkmcnt(10)\n"
"v_mfma_f32_16x16x32_bf16 v[80:83], v[200:203], a[128:131], v[80:83]\n"
"ds_read_b64_tr_b16 v[196:197], v228 offset:45056\n"
"v_mfma_f32_16x16x32_bf16 v[80:83], v[204:207], a[144:147], v[80:83]\n"
"ds_read_b64_tr_b16 v[198:199], v228 offset:45568\n"
"v_mfma_f32_16x16x32_bf16 v[84:87], v[200:203], a[132:135], v[84:87]\n"
"v_mfma_f32_16x16x32_bf16 v[84:87], v[204:207], a[148:151], v[84:87]\n"
"v_mfma_f32_16x16x32_bf16 v[88:91], v[200:203], a[136:139], v[88:91]\n"
"v_mfma_f32_16x16x32_bf16 v[88:91], v[204:207], a[152:155], v[88:91]\n"
"v_mfma_f32_16x16x32_bf16 v[92:95], v[200:203], a[140:143], v[92:95]\n"
"ds_read_b64_tr_b16 v[200:201], v229 offset:40960\n"
"buffer_load_dwordx4 a[200:203], v236, s[40:43], s64 offen offset:3072 sc1\n"
"v_mfma_f32_16x16x32_bf16 v[92:95], v[204:207], a[156:159], v[92:95]\n"
"ds_read_b64_tr_b16 v[202:203], v229 offset:41472\n"
"s_waitcnt lgkmcnt(10)\n"
"v_mfma_f32_16x16x32_bf16 v[96:99], v[208:211], a[128:131], v[96:99]\n"
"ds_read_b64_tr_b16 v[204:205], v229 offset:45056\n"
"v_mfma_f32_16x16x32_bf16 v[96:99], v[212:215], a[144:147], v[96:99]\n"
"ds_read_b64_tr_b16 v[206:207], v229 offset:45568\n"
"v_mfma_f32_16x16x32_bf16 v[100:103], v[208:211], a[132:135], v[100:103]\n"
"v_mfma_f32_16x16x32_bf16 v[100:103], v[212:215], a[148:151], v[100:103]\n"
"v_mfma_f32_16x16x32_bf16 v[104:107], v[208:211], a[136:139], v[104:107]\n"
"v_mfma_f32_16x16x32_bf16 v[104:107], v[212:215], a[152:155], v[104:107]\n"
"v_mfma_f32_16x16x32_bf16 v[108:111], v[208:211], a[140:143], v[108:111]\n"
"ds_read_b64_tr_b16 v[208:209], v230 offset:40960\n"
"v_mfma_f32_16x16x32_bf16 v[108:111], v[212:215], a[156:159], v[108:111]\n"
"ds_read_b64_tr_b16 v[210:211], v230 offset:41472\n"
"s_waitcnt lgkmcnt(10)\n"
"v_mfma_f32_16x16x32_bf16 v[112:115], v[216:219], a[128:131], v[112:115]\n"
"ds_read_b64_tr_b16 v[212:213], v230 offset:45056\n"
"buffer_load_dwordx4 a[216:219], v236, s[40:43], s64 offen offset:3328 sc1\n"
"v_mfma_f32_16x16x32_bf16 v[112:115], v[220:223], a[144:147], v[112:115]\n"
"ds_read_b64_tr_b16 v[214:215], v230 offset:45568\n"
"v_mfma_f32_16x16x32_bf16 v[116:119], v[216:219], a[132:135], v[116:119]\n"
"v_mfma_f32_16x16x32_bf16 v[116:119], v[220:223], a[148:151], v[116:119]\n"
"v_mfma_f32_16x16x32_bf16 v[120:123], v[216:219], a[136:139], v[120:123]\n"
"v_mfma_f32_16x16x32_bf16 v[120:123], v[220:223], a[152:155], v[120:123]\n"
"v_mfma_f32_16x16x32_bf16 v[124:127], v[216:219], a[140:143], v[124:127]\n"
"ds_read_b64_tr_b16 v[216:217], v231 offset:40960\n"
"v_mfma_f32_16x16x32_bf16 v[124:127], v[220:223], a[156:159], v[124:127]\n"
"ds_read_b64_tr_b16 v[218:219], v231 offset:41472\n"
"s_waitcnt lgkmcnt(10)\n"
"v_mfma_f32_16x16x32_bf16 v[128:131], v[192:195], a[128:131], v[128:131]\n"
"ds_read_b64_tr_b16 v[220:221], v231 offset:45056\n"
"v_mfma_f32_16x16x32_bf16 v[128:131], v[196:199], a[144:147], v[128:131]\n"
"ds_read_b64_tr_b16 v[222:223], v231 offset:45568\n"
"buffer_load_dwordx4 a[204:207], v236, s[40:43], s65 offen offset:3072 sc1\n"
"v_mfma_f32_16x16x32_bf16 v[132:135], v[192:195], a[132:135], v[132:135]\n"
"v_mfma_f32_16x16x32_bf16 v[132:135], v[196:199], a[148:151], v[132:135]\n"
"v_mfma_f32_16x16x32_bf16 v[136:139], v[192:195], a[136:139], v[136:139]\n"
"v_mfma_f32_16x16x32_bf16 v[136:139], v[196:199], a[152:155], v[136:139]\n"
"v_mfma_f32_16x16x32_bf16 v[140:143], v[192:195], a[140:143], v[140:143]\n"
"ds_read_b64_tr_b16 v[192:193], v228 offset:49152\n"
"v_mfma_f32_16x16x32_bf16 v[140:143], v[196:199], a[156:159], v[140:143]\n"
"ds_read_b64_tr_b16 v[194:195], v228 offset:49664\n"
"s_waitcnt lgkmcnt(10)\n"
"v_mfma_f32_16x16x32_bf16 v[144:147], v[200:203], a[128:131], v[144:147]\n"
"ds_read_b64_tr_b16 v[196:197], v228 offset:53248\n"
"v_mfma_f32_16x16x32_bf16 v[144:147], v[204:207], a[144:147], v[144:147]\n"
"ds_read_b64_tr_b16 v[198:199], v228 offset:53760\n"
"v_and_b32 v228, s35, v240\n"
"v_mfma_f32_16x16x32_bf16 v[148:151], v[200:203], a[132:135], v[148:151]\n"
"buffer_load_dwordx4 a[220:223], v236, s[40:43], s65 offen offset:3328 sc1\n"
"v_mfma_f32_16x16x32_bf16 v[148:151], v[204:207], a[148:151], v[148:151]\n"
"v_mfma_f32_16x16x32_bf16 v[152:155], v[200:203], a[136:139], v[152:155]\n"
"v_mfma_f32_16x16x32_bf16 v[152:155], v[204:207], a[152:155], v[152:155]\n"
"v_mfma_f32_16x16x32_bf16 v[156:159], v[200:203], a[140:143], v[156:159]\n"
"ds_read_b64_tr_b16 v[200:201], v229 offset:49152\n"
"v_mfma_f32_16x16x32_bf16 v[156:159], v[204:207], a[156:159], v[156:159]\n"
"ds_read_b64_tr_b16 v[202:203], v229 offset:49664\n"
"s_waitcnt lgkmcnt(10)\n"
"v_mfma_f32_16x16x32_bf16 v[160:163], v[208:211], a[128:131], v[160:163]\n"
"ds_read_b64_tr_b16 v[204:205], v229 offset:53248\n"
"v_mfma_f32_16x16x32_bf16 v[160:163], v[212:215], a[144:147], v[160:163]\n"
"ds_read_b64_tr_b16 v[206:207], v229 offset:53760\n"
"v_and_b32 v229, s35, v241\n"
"v_mfma_f32_16x16x32_bf16 v[164:167], v[208:211], a[132:135], v[164:167]\n"
"v_mfma_f32_16x16x32_bf16 v[164:167], v[212:215], a[148:151], v[164:167]\n"
"v_mfma_f32_16x16x32_bf16 v[168:171], v[208:211], a[136:139], v[168:171]\n"
"buffer_load_dwordx4 a[224:227], v236, s[40:43], s62 offen offset:3584 sc1\n"
"v_mfma_f32_16x16x32_bf16 v[168:171], v[212:215], a[152:155], v[168:171]\n"
"v_mfma_f32_16x16x32_bf16 v[172:175], v[208:211], a[140:143], v[172:175]\n"
"ds_read_b64_tr_b16 v[208:209], v230 offset:49152\n"
"v_mfma_f32_16x16x32_bf16 v[172:175], v[212:215], a[156:159], v[172:175]\n"
"ds_read_b64_tr_b16 v[210:211], v230 offset:49664\n"
"s_waitcnt lgkmcnt(10)\n"
"v_mfma_f32_16x16x32_bf16 v[176:179], v[216:219], a[128:131], v[176:179]\n"
"ds_read_b64_tr_b16 v[212:213], v230 offset:53248\n"
"v_mfma_f32_16x16x32_bf16 v[176:179], v[220:223], a[144:147], v[176:179]\n"
"ds_read_b64_tr_b16 v[214:215], v230 offset:53760\n"
"v_and_b32 v230, s35, v242\n"
"v_mfma_f32_16x16x32_bf16 v[180:183], v[216:219], a[132:135], v[180:183]\n"
"v_accvgpr_write_b32 a144, v244\n"
"v_accvgpr_write_b32 a145, v244\n"
"v_mfma_f32_16x16x32_bf16 v[180:183], v[220:223], a[148:151], v[180:183]\n"
"v_accvgpr_write_b32 a146, v244\n"
"v_accvgpr_write_b32 a147, v244\n"
"v_mfma_f32_16x16x32_bf16 v[184:187], v[216:219], a[136:139], v[184:187]\n"
"v_mfma_f32_16x16x32_bf16 v[184:187], v[220:223], a[152:155], v[184:187]\n"
"buffer_load_dwordx4 a[240:243], v236, s[40:43], s62 offen offset:3840 sc1\n"
"s_lshl_b32 s62, s66, 3\n"
"v_mfma_f32_16x16x32_bf16 v[188:191], v[216:219], a[140:143], v[188:191]\n"
"ds_read_b64_tr_b16 v[216:217], v231 offset:49152\n"
"v_mfma_f32_16x16x32_bf16 v[188:191], v[220:223], a[156:159], v[188:191]\n"
"ds_read_b64_tr_b16 v[218:219], v231 offset:49664\n"
"s_waitcnt vmcnt(11) lgkmcnt(10)\n"
"v_mfma_f32_16x16x32_bf16 v[64:67], v[192:195], a[160:163], v[64:67]\n"
"ds_read_b64_tr_b16 v[220:221], v231 offset:53248\n"
"v_mfma_f32_16x16x32_bf16 v[64:67], v[196:199], a[176:179], v[64:67]\n"
"ds_read_b64_tr_b16 v[222:223], v231 offset:53760\n"
"v_and_b32 v231, s35, v243\n"
"v_mfma_f32_16x16x32_bf16 v[68:71], v[192:195], a[164:167], v[68:71]\n"
"v_mfma_f32_16x16x32_bf16 v[68:71], v[196:199], a[180:183], v[68:71]\n"
"v_mfma_f32_16x16x32_bf16 v[72:75], v[192:195], a[168:171], v[72:75]\n"
"v_mfma_f32_16x16x32_bf16 v[72:75], v[196:199], a[184:187], v[72:75]\n"
"v_mfma_f32_16x16x32_bf16 v[76:79], v[192:195], a[172:175], v[76:79]\n"
"ds_read_b64_tr_b16 v[192:193], v224 offset:8192\n"
"v_mfma_f32_16x16x32_bf16 v[76:79], v[196:199], a[188:191], v[76:79]\n"
"ds_read_b64_tr_b16 v[194:195], v224 offset:8704\n"
"s_waitcnt lgkmcnt(10)\n"
"v_mfma_f32_16x16x32_bf16 v[80:83], v[200:203], a[160:163], v[80:83]\n"
"ds_read_b64_tr_b16 v[196:197], v224 offset:12288\n"
"buffer_load_dwordx4 a[228:231], v236, s[40:43], s63 offen offset:3584 sc1\n"
"v_mfma_f32_16x16x32_bf16 v[80:83], v[204:207], a[176:179], v[80:83]\n"
"ds_read_b64_tr_b16 v[198:199], v224 offset:12800\n"
"v_mfma_f32_16x16x32_bf16 v[84:87], v[200:203], a[164:167], v[84:87]\n"
"v_mfma_f32_16x16x32_bf16 v[84:87], v[204:207], a[180:183], v[84:87]\n"
"v_mfma_f32_16x16x32_bf16 v[88:91], v[200:203], a[168:171], v[88:91]\n"
"v_mfma_f32_16x16x32_bf16 v[88:91], v[204:207], a[184:187], v[88:91]\n"
"v_mfma_f32_16x16x32_bf16 v[92:95], v[200:203], a[172:175], v[92:95]\n"
"ds_read_b64_tr_b16 v[200:201], v225 offset:8192\n"
"v_mfma_f32_16x16x32_bf16 v[92:95], v[204:207], a[188:191], v[92:95]\n"
"ds_read_b64_tr_b16 v[202:203], v225 offset:8704\n"
"s_waitcnt lgkmcnt(10)\n"
"v_mfma_f32_16x16x32_bf16 v[96:99], v[208:211], a[160:163], v[96:99]\n"
"ds_read_b64_tr_b16 v[204:205], v225 offset:12288\n"
"buffer_load_dwordx4 a[244:247], v236, s[40:43], s63 offen offset:3840 sc1\n"
"v_mfma_f32_16x16x32_bf16 v[96:99], v[212:215], a[176:179], v[96:99]\n"
"ds_read_b64_tr_b16 v[206:207], v225 offset:12800\n"
"s_lshl_b32 s63, s18, 2\n"
"v_mfma_f32_16x16x32_bf16 v[100:103], v[208:211], a[164:167], v[100:103]\n"
"v_mfma_f32_16x16x32_bf16 v[100:103], v[212:215], a[180:183], v[100:103]\n"
"v_mfma_f32_16x16x32_bf16 v[104:107], v[208:211], a[168:171], v[104:107]\n"
"v_mfma_f32_16x16x32_bf16 v[104:107], v[212:215], a[184:187], v[104:107]\n"
"v_mfma_f32_16x16x32_bf16 v[108:111], v[208:211], a[172:175], v[108:111]\n"
"ds_read_b64_tr_b16 v[208:209], v226 offset:8192\n"
"v_mfma_f32_16x16x32_bf16 v[108:111], v[212:215], a[188:191], v[108:111]\n"
"ds_read_b64_tr_b16 v[210:211], v226 offset:8704\n"
"s_waitcnt lgkmcnt(10)\n"
"v_mfma_f32_16x16x32_bf16 v[112:115], v[216:219], a[160:163], v[112:115]\n"
"buffer_load_dwordx2 v[250:251], v238, s[40:43], s51 offen sc1\n"
"ds_read_b64_tr_b16 v[212:213], v226 offset:12288\n"
"v_mfma_f32_16x16x32_bf16 v[112:115], v[220:223], a[176:179], v[112:115]\n"
"ds_read_b64_tr_b16 v[214:215], v226 offset:12800\n"
"v_mfma_f32_16x16x32_bf16 v[116:119], v[216:219], a[164:167], v[116:119]\n"
"buffer_load_dwordx4 a[232:235], v236, s[40:43], s64 offen offset:3584 sc1\n"
"v_mfma_f32_16x16x32_bf16 v[116:119], v[220:223], a[180:183], v[116:119]\n"
"v_mfma_f32_16x16x32_bf16 v[120:123], v[216:219], a[168:171], v[120:123]\n"
"v_mfma_f32_16x16x32_bf16 v[120:123], v[220:223], a[184:187], v[120:123]\n"
"v_mfma_f32_16x16x32_bf16 v[124:127], v[216:219], a[172:175], v[124:127]\n"
"ds_read_b64_tr_b16 v[216:217], v227 offset:8192\n"
"v_mfma_f32_16x16x32_bf16 v[124:127], v[220:223], a[188:191], v[124:127]\n"
"ds_read_b64_tr_b16 v[218:219], v227 offset:8704\n"
"s_waitcnt lgkmcnt(10)\n"
"v_mfma_f32_16x16x32_bf16 v[128:131], v[192:195], a[160:163], v[128:131]\n"
"ds_read_b64_tr_b16 v[220:221], v227 offset:12288\n"
"v_mfma_f32_16x16x32_bf16 v[128:131], v[196:199], a[176:179], v[128:131]\n"
"ds_read_b64_tr_b16 v[222:223], v227 offset:12800\n"
"v_mfma_f32_16x16x32_bf16 v[132:135], v[192:195], a[164:167], v[132:135]\n"
"v_mfma_f32_16x16x32_bf16 v[132:135], v[196:199], a[180:183], v[132:135]\n"
"buffer_load_dwordx4 a[248:251], v236, s[40:43], s64 offen offset:3840 sc1\n"
"s_lshl_b32 s64, s18, 3\n"
"v_mfma_f32_16x16x32_bf16 v[136:139], v[192:195], a[168:171], v[136:139]\n"
"s_add_u32 s64, s64, s15\n"
"s_lshl_b32 s15, s16, 0\n"
"v_mfma_f32_16x16x32_bf16 v[136:139], v[196:199], a[184:187], v[136:139]\n"
"s_add_u32 s62, s62, s15\n"
"s_lshl_b32 s15, s16, 0\n"
"v_mfma_f32_16x16x32_bf16 v[140:143], v[192:195], a[172:175], v[140:143]\n"
"s_barrier\n"
"v_mfma_f32_16x16x32_bf16 v[140:143], v[196:199], a[188:191], v[140:143]\n"
"ds_read_b64_tr_b16 v[192:193], v224 offset:16384\n"
"s_waitcnt lgkmcnt(9)\n"
"v_mfma_f32_16x16x32_bf16 v[144:147], v[200:203], a[160:163], v[144:147]\n"
"ds_read_b64_tr_b16 v[194:195], v224 offset:16896\n"
"s_add_u32 s67, s67, s15\n"
"v_mfma_f32_16x16x32_bf16 v[144:147], v[204:207], a[176:179], v[144:147]\n"
"ds_read_b64_tr_b16 v[196:197], v224 offset:20480\n"
"s_and_b32 s16, s14, 3\n"
"v_mfma_f32_16x16x32_bf16 v[148:151], v[200:203], a[164:167], v[148:151]\n"
"ds_read_b64_tr_b16 v[198:199], v224 offset:20992\n"
"s_lshl_b32 s15, s16, 0\n"
"v_mfma_f32_16x16x32_bf16 v[148:151], v[204:207], a[180:183], v[148:151]\n"
"s_add_u32 s63, s63, s15\n"
"s_lshl_b32 s15, s11, 14\n"
"v_mfma_f32_16x16x32_bf16 v[152:155], v[200:203], a[168:171], v[152:155]\n"
"s_add_u32 s17, s17, s15\n"
"s_lshl_b32 s15, s16, 8\n"
"v_mfma_f32_16x16x32_bf16 v[152:155], v[204:207], a[184:187], v[152:155]\n"
"s_add_u32 s17, s17, s15\n"
"s_lshl_b32 s15, s11, 12\n"
"v_mfma_f32_16x16x32_bf16 v[156:159], v[200:203], a[172:175], v[156:159]\n"
"s_add_u32 s60, s60, s15\n"
"s_mul_i32 s15, s11, 1792\n"
"v_mfma_f32_16x16x32_bf16 v[156:159], v[204:207], a[188:191], v[156:159]\n"
"s_add_u32 s59, s59, s15\n"
"s_waitcnt lgkmcnt(8)\n"
"v_mfma_f32_16x16x32_bf16 v[160:163], v[208:211], a[160:163], v[160:163]\n"
"s_and_b32 s18, s14, 0\n"
"s_min_u32 s18, s18, 1\n"
"v_mfma_f32_16x16x32_bf16 v[160:163], v[212:215], a[176:179], v[160:163]\n"
"s_lshl_b32 s15, s18, 0\n"
"ds_read_b64_tr_b16 v[200:201], v225 offset:16384\n"
"v_mfma_f32_16x16x32_bf16 v[164:167], v[208:211], a[164:167], v[164:167]\n"
"ds_read_b64_tr_b16 v[202:203], v225 offset:16896\n"
"buffer_load_dwordx4 a[236:239], v236, s[40:43], s65 offen offset:3584 sc1\n"
"v_mfma_f32_16x16x32_bf16 v[164:167], v[212:215], a[180:183], v[164:167]\n"
"ds_read_b64_tr_b16 v[204:205], v225 offset:20480\n"
"s_xor_b32 s67, s67, 8\n"
"v_mfma_f32_16x16x32_bf16 v[168:171], v[208:211], a[168:171], v[168:171]\n"
"ds_read_b64_tr_b16 v[206:207], v225 offset:20992\n"
"v_mfma_f32_16x16x32_bf16 v[168:171], v[212:215], a[184:187], v[168:171]\n"
"v_mfma_f32_16x16x32_bf16 v[172:175], v[208:211], a[172:175], v[172:175]\n"
"buffer_load_dwordx4 a[252:255], v236, s[40:43], s65 offen offset:3840 sc1\n"
"ds_read_b64_tr_b16 v[208:209], v226 offset:16384\n"
"v_mfma_f32_16x16x32_bf16 v[172:175], v[212:215], a[188:191], v[172:175]\n"
"ds_read_b64_tr_b16 v[210:211], v226 offset:16896\n"
"s_waitcnt lgkmcnt(10)\n"
"v_mfma_f32_16x16x32_bf16 v[176:179], v[216:219], a[160:163], v[176:179]\n"
"ds_read_b64_tr_b16 v[212:213], v226 offset:20480\n"
"v_add_u32 v236, 0x10000, v232\n"
"v_mfma_f32_16x16x32_bf16 v[176:179], v[220:223], a[176:179], v[176:179]\n"
"ds_read_b64_tr_b16 v[214:215], v226 offset:20992\n"
"s_lshr_b32 s65, s62, 1\n"
"v_mfma_f32_16x16x32_bf16 v[180:183], v[216:219], a[164:167], v[180:183]\n"
"v_mfma_f32_16x16x32_bf16 v[180:183], v[220:223], a[180:183], v[180:183]\n"
"v_mfma_f32_16x16x32_bf16 v[184:187], v[216:219], a[168:171], v[184:187]\n"
"v_mfma_f32_16x16x32_bf16 v[184:187], v[220:223], a[184:187], v[184:187]\n"
"v_mfma_f32_16x16x32_bf16 v[188:191], v[216:219], a[172:175], v[188:191]\n"
"ds_read_b64_tr_b16 v[216:217], v227 offset:16384\n"
"v_mfma_f32_16x16x32_bf16 v[188:191], v[220:223], a[188:191], v[188:191]\n"
"s_add_u32 m0, s61, 49152\n"
"s_add_u32 s16, s61, 0\n"
"s_waitcnt vmcnt(4)\n"
"v_cmp_ne_u64 vcc, s[6:7], v[250:251]\n"
"s_waitcnt lgkmcnt(9)\n"
"v_mfma_f32_16x16x32_bf16 v[64:67], v[192:195], a[192:195], v[64:67]\n"
"s_cbranch_vccnz Lpk_%=\n"
"Lpk_ok_%=:\n"
"v_lshrrev_b32 v238, 7, v232\n"
"v_and_b32 v239, 6, v238\n"
"v_mfma_f32_16x16x32_bf16 v[64:67], v[196:199], a[208:211], v[64:67]\n"
"v_lshlrev_b32 v239, 4, v239\n"
"v_xor_b32 v239, v239, v232\n"
"v_and_b32 v239, 0x70, v239\n"
"v_mfma_f32_16x16x32_bf16 v[68:71], v[192:195], a[196:199], v[68:71]\n"
"v_lshl_add_u32 v238, v238, 10, v239\n"
"v_add_u32 v238, s17, v238\n"
"buffer_load_dwordx4 v232, s[44:47], s16 offen offset:0 sc1 lds\n"
"v_mfma_f32_16x16x32_bf16 v[68:71], v[196:199], a[212:215], v[68:71]\n"
"buffer_load_dwordx4 v232, s[44:47], s16 offen offset:1024 sc1 lds\n"
"s_add_u32 m0, s61, 0\n"
"s_add_u32 s16, s61, 8192\n"
"v_mfma_f32_16x16x32_bf16 v[72:75], v[192:195], a[200:203], v[72:75]\n"
"s_lshr_b32 s17, s10, 3\n"
"s_min_u32 s17, s17, 27\n"
"s_lshl_b32 s17, s17, 7\n"
"v_mfma_f32_16x16x32_bf16 v[72:75], v[196:199], a[216:219], v[72:75]\n"
"buffer_load_dwordx4 v232, s[44:47], s16 offen offset:0 sc1 lds\n"
"v_mov_b32 v254, s17\n"
"s_lshl_b32 s17, s11, 1\n"
"v_mfma_f32_16x16x32_bf16 v[76:79], v[192:195], a[204:207], v[76:79]\n"
"s_add_u32 s17, s17, s15\n"
"ds_read_b64_tr_b16 v[218:219], v227 offset:16896\n"
"v_mfma_f32_16x16x32_bf16 v[76:79], v[196:199], a[220:223], v[76:79]\n"
"buffer_load_dwordx4 v232, s[44:47], s16 offen offset:1024 sc1 lds\n"
"s_add_u32 m0, s61, 65536\n"
"s_waitcnt lgkmcnt(6)\n"
"v_mfma_f32_16x16x32_bf16 v[80:83], v[200:203], a[192:195], v[80:83]\n"
"s_add_u32 s16, s61, 16384\n"
"ds_read_b64_tr_b16 v[220:221], v227 offset:20480\n"
"v_mfma_f32_16x16x32_bf16 v[80:83], v[204:207], a[208:211], v[80:83]\n"
"buffer_load_dwordx4 v232, s[44:47], s16 offen offset:0 sc1 lds\n"
"ds_read_b64_tr_b16 v[222:223], v227 offset:20992\n"
"v_mfma_f32_16x16x32_bf16 v[84:87], v[200:203], a[196:199], v[84:87]\n"
"ds_read_b64_tr_b16 v[192:193], v224 offset:24576\n"
"v_add_u32 v239, 7168, v238\n"
"v_mfma_f32_16x16x32_bf16 v[84:87], v[204:207], a[212:215], v[84:87]\n"
"buffer_load_dwordx4 v232, s[44:47], s16 offen offset:1024 sc1 lds\n"
"s_add_u32 m0, s61, 73728\n"
"s_add_u32 s16, s61, 24576\n"
"v_mfma_f32_16x16x32_bf16 v[88:91], v[200:203], a[200:203], v[88:91]\n"
"ds_read_b64_tr_b16 v[194:195], v224 offset:25088\n"
"v_mfma_f32_16x16x32_bf16 v[88:91], v[204:207], a[216:219], v[88:91]\n"
"buffer_load_dwordx4 v232, s[44:47], s16 offen offset:0 sc1 lds\n"
"ds_read_b64_tr_b16 v[196:197], v224 offset:28672\n"
"v_mfma_f32_16x16x32_bf16 v[92:95], v[200:203], a[204:207], v[92:95]\n"
"ds_read_b64_tr_b16 v[198:199], v224 offset:29184\n"
"v_mfma_f32_16x16x32_bf16 v[92:95], v[204:207], a[220:223], v[92:95]\n"
"buffer_load_dwordx4 v232, s[44:47], s16 offen offset:1024 sc1 lds\n"
"s_add_u32 m0, s61, 81920\n"
"s_waitcnt lgkmcnt(8)\n"
"v_mfma_f32_16x16x32_bf16 v[96:99], v[208:211], a[192:195], v[96:99]\n"
"s_add_u32 s16, s61, 32768\n"
"ds_read_b64_tr_b16 v[200:201], v225 offset:24576\n"
"v_mfma_f32_16x16x32_bf16 v[96:99], v[212:215], a[208:211], v[96:99]\n"
"buffer_load_dwordx4 v232, s[44:47], s16 offen offset:0 sc1 lds\n"
"ds_read_b64_tr_b16 v[202:203], v225 offset:25088\n"
"v_mfma_f32_16x16x32_bf16 v[100:103], v[208:211], a[196:199], v[100:103]\n"
"ds_read_b64_tr_b16 v[204:205], v225 offset:28672\n"
"v_mfma_f32_16x16x32_bf16 v[100:103], v[212:215], a[212:215], v[100:103]\n"
"buffer_load_dwordx4 v232, s[44:47], s16 offen offset:1024 sc1 lds\n"
"s_add_u32 m0, s61, 90112\n"
"s_add_u32 s16, s61, 40960\n"
"v_mfma_f32_16x16x32_bf16 v[104:107], v[208:211], a[200:203], v[104:107]\n"
"ds_read_b64_tr_b16 v[206:207], v225 offset:29184\n"
"v_mfma_f32_16x16x32_bf16 v[104:107], v[212:215], a[216:219], v[104:107]\n"
"buffer_load_dwordx4 v232, s[44:47], s16 offen offset:0 sc1 lds\n"
"v_mfma_f32_16x16x32_bf16 v[108:111], v[208:211], a[204:207], v[108:111]\n"
"ds_read_b64_tr_b16 v[208:209], v226 offset:24576\n"
"v_mfma_f32_16x16x32_bf16 v[108:111], v[212:215], a[220:223], v[108:111]\n"
"buffer_load_dwordx4 v232, s[44:47], s16 offen offset:1024 sc1 lds\n"
"s_add_u32 m0, s61, 98304\n"
"s_waitcnt lgkmcnt(9)\n"
"v_mfma_f32_16x16x32_bf16 v[112:115], v[216:219], a[192:195], v[112:115]\n"
"s_add_u32 s16, s61, 49152\n"
"ds_read_b64_tr_b16 v[210:211], v226 offset:25088\n"
"v_mfma_f32_16x16x32_bf16 v[112:115], v[220:223], a[208:211], v[112:115]\n"
"buffer_load_dwordx4 v232, s[44:47], s16 offen offset:0 sc1 lds\n"
"ds_read_b64_tr_b16 v[212:213], v226 offset:28672\n"
"v_mfma_f32_16x16x32_bf16 v[116:119], v[216:219], a[196:199], v[116:119]\n"
"ds_read_b64_tr_b16 v[214:215], v226 offset:29184\n"
"v_mfma_f32_16x16x32_bf16 v[116:119], v[220:223], a[212:215], v[116:119]\n"
"buffer_load_dwordx4 v232, s[44:47], s16 offen offset:1024 sc1 lds\n"
"v_mfma_f32_16x16x32_bf16 v[120:123], v[216:219], a[200:203], v[120:123]\n"
"v_mfma_f32_16x16x32_bf16 v[120:123], v[220:223], a[216:219], v[120:123]\n"
"v_mfma_f32_16x16x32_bf16 v[124:127], v[216:219], a[204:207], v[124:127]\n"
"ds_read_b64_tr_b16 v[216:217], v227 offset:24576\n"
"v_mfma_f32_16x16x32_bf16 v[124:127], v[220:223], a[220:223], v[124:127]\n"
"ds_read_b64_tr_b16 v[218:219], v227 offset:25088\n"
"s_waitcnt lgkmcnt(10)\n"
"v_mfma_f32_16x16x32_bf16 v[128:131], v[192:195], a[192:195], v[128:131]\n"
"ds_read_b64_tr_b16 v[220:221], v227 offset:28672\n"
"v_mfma_f32_16x16x32_bf16 v[128:131], v[196:199], a[208:211], v[128:131]\n"
"ds_read_b64_tr_b16 v[222:223], v227 offset:29184\n"
"v_mfma_f32_16x16x32_bf16 v[132:135], v[192:195], a[196:199], v[132:135]\n"
"v_mfma_f32_16x16x32_bf16 v[132:135], v[196:199], a[212:215], v[132:135]\n"
"v_mfma_f32_16x16x32_bf16 v[136:139], v[192:195], a[200:203], v[136:139]\n"
"v_mfma_f32_16x16x32_bf16 v[136:139], v[196:199], a[216:219], v[136:139]\n"
"buffer_load_dword a150, v252, s[20:23], s60 offen\n"
"v_mov_b32 v252, s35\n"
"v_mfma_f32_16x16x32_bf16 v[140:143], v[192:195], a[204:207], v[140:143]\n"
"buffer_load_dword a150, v253, s[24:27], s59 offen\n"
"ds_read_b64_tr_b16 v[192:193], v224 offset:32768\n"
"v_mfma_f32_16x16x32_bf16 v[140:143], v[196:199], a[220:223], v[140:143]\n"
"buffer_load_dword a150, v253, s[28:31], s59 offen\n"
"s_cmp_lg_u32 s17, 0\n"
"s_cbranch_scc1 Lninv1_%=\n"
"buffer_inv sc1\n"
"s_branch Linv1_%=\n"
"Lninv1_%=:\n"
"global_load_dword a150, v254, s[8:9]\n"
"Linv1_%=:\n"
"s_waitcnt lgkmcnt(9)\n"
"v_mfma_f32_16x16x32_bf16 v[144:147], v[200:203], a[192:195], v[144:147]\n"
"ds_read_b64_tr_b16 v[194:195], v224 offset:33280\n"
"v_mov_b32 v253, s35\n"
"v_mfma_f32_16x16x32_bf16 v[144:147], v[204:207], a[208:211], v[144:147]\n"
"ds_read_b64_tr_b16 v[196:197], v224 offset:36864\n"
"v_mov_b32 v254, s35\n"
"v_mfma_f32_16x16x32_bf16 v[148:151], v[200:203], a[196:199], v[148:151]\n"
"ds_read_b64_tr_b16 v[198:199], v224 offset:37376\n"
"s_lshl_b32 s60, s11, 14\n"
"v_mfma_f32_16x16x32_bf16 v[148:151], v[204:207], a[212:215], v[148:151]\n"
"s_add_u32 s60, s60, 131072\n"
"v_add_u32 v234, s60, v232\n"
"v_mfma_f32_16x16x32_bf16 v[152:155], v[200:203], a[200:203], v[152:155]\n"
"s_lshl_b32 s60, s11, 10\n"
"s_add_u32 s60, s60, 122880\n"
"v_mfma_f32_16x16x32_bf16 v[152:155], v[204:207], a[216:219], v[152:155]\n"
"v_add_u32 v235, s60, v232\n"
"s_mov_b32 s59, 0\n"
"v_mfma_f32_16x16x32_bf16 v[156:159], v[200:203], a[204:207], v[156:159]\n"
"ds_read_b64_tr_b16 v[200:201], v225 offset:32768\n"
"v_mfma_f32_16x16x32_bf16 v[156:159], v[204:207], a[220:223], v[156:159]\n"
"ds_read_b64_tr_b16 v[202:203], v225 offset:33280\n"
"s_waitcnt lgkmcnt(10)\n"
"v_mfma_f32_16x16x32_bf16 v[160:163], v[208:211], a[192:195], v[160:163]\n"
"ds_read_b64_tr_b16 v[204:205], v225 offset:36864\n"
"v_mfma_f32_16x16x32_bf16 v[160:163], v[212:215], a[208:211], v[160:163]\n"
"ds_read_b64_tr_b16 v[206:207], v225 offset:37376\n"
"v_mfma_f32_16x16x32_bf16 v[164:167], v[208:211], a[196:199], v[164:167]\n"
"v_mfma_f32_16x16x32_bf16 v[164:167], v[212:215], a[212:215], v[164:167]\n"
"v_mfma_f32_16x16x32_bf16 v[168:171], v[208:211], a[200:203], v[168:171]\n"
"v_mfma_f32_16x16x32_bf16 v[168:171], v[212:215], a[216:219], v[168:171]\n"
"v_mfma_f32_16x16x32_bf16 v[172:175], v[208:211], a[204:207], v[172:175]\n"
"ds_read_b64_tr_b16 v[208:209], v226 offset:32768\n"
"v_mfma_f32_16x16x32_bf16 v[172:175], v[212:215], a[220:223], v[172:175]\n"
"ds_read_b64_tr_b16 v[210:211], v226 offset:33280\n"
"s_waitcnt lgkmcnt(10)\n"
"v_mfma_f32_16x16x32_bf16 v[176:179], v[216:219], a[192:195], v[176:179]\n"
"ds_read_b64_tr_b16 v[212:213], v226 offset:36864\n"
"v_mfma_f32_16x16x32_bf16 v[176:179], v[220:223], a[208:211], v[176:179]\n"
"ds_read_b64_tr_b16 v[214:215], v226 offset:37376\n"
"v_mfma_f32_16x16x32_bf16 v[180:183], v[216:219], a[196:199], v[180:183]\n"
"v_mfma_f32_16x16x32_bf16 v[180:183], v[220:223], a[212:215], v[180:183]\n"
"v_mfma_f32_16x16x32_bf16 v[184:187], v[216:219], a[200:203], v[184:187]\n"
"v_mfma_f32_16x16x32_bf16 v[184:187], v[220:223], a[216:219], v[184:187]\n"
"v_mfma_f32_16x16x32_bf16 v[188:191], v[216:219], a[204:207], v[188:191]\n"
"ds_read_b64_tr_b16 v[216:217], v227 offset:32768\n"
"v_mfma_f32_16x16x32_bf16 v[188:191], v[220:223], a[220:223], v[188:191]\n"
"ds_read_b64_tr_b16 v[218:219], v227 offset:33280\n"
"s_waitcnt vmcnt(18) lgkmcnt(10)\n"
"v_mfma_f32_16x16x32_bf16 v[64:67], v[192:195], a[224:227], v[64:67]\n"
"ds_read_b64_tr_b16 v[220:221], v227 offset:36864\n"
"v_mfma_f32_16x16x32_bf16 v[64:67], v[196:199], a[240:243], v[64:67]\n"
"ds_read_b64_tr_b16 v[222:223], v227 offset:37376\n"
"v_mfma_f32_16x16x32_bf16 v[68:71], v[192:195], a[228:231], v[68:71]\n"
"v_mfma_f32_16x16x32_bf16 v[68:71], v[196:199], a[244:247], v[68:71]\n"
"v_mfma_f32_16x16x32_bf16 v[72:75], v[192:195], a[232:235], v[72:75]\n"
"v_mfma_f32_16x16x32_bf16 v[72:75], v[196:199], a[248:251], v[72:75]\n"
"v_mfma_f32_16x16x32_bf16 v[76:79], v[192:195], a[236:239], v[76:79]\n"
"ds_read_b64_tr_b16 v[192:193], v224 offset:40960\n"
"v_mfma_f32_16x16x32_bf16 v[76:79], v[196:199], a[252:255], v[76:79]\n"
"ds_read_b64_tr_b16 v[194:195], v224 offset:41472\n"
"s_waitcnt lgkmcnt(10)\n"
"v_mfma_f32_16x16x32_bf16 v[80:83], v[200:203], a[224:227], v[80:83]\n"
"ds_read_b64_tr_b16 v[196:197], v224 offset:45056\n"
"v_cvt_pk_bf16_f32 v0, v64, v65\n"
"v_mfma_f32_16x16x32_bf16 v[80:83], v[204:207], a[240:243], v[80:83]\n"
"ds_read_b64_tr_b16 v[198:199], v224 offset:45568\n"
"v_cvt_pk_bf16_f32 v1, v66, v67\n"
"v_mfma_f32_16x16x32_bf16 v[84:87], v[200:203], a[228:231], v[84:87]\n"
"v_cvt_pk_bf16_f32 v16, v68, v69\n"
"v_cvt_pk_bf16_f32 v17, v70, v71\n"
"v_mfma_f32_16x16x32_bf16 v[84:87], v[204:207], a[244:247], v[84:87]\n"
"v_cvt_pk_bf16_f32 v32, v72, v73\n"
"v_cvt_pk_bf16_f32 v33, v74, v75\n"
"v_mfma_f32_16x16x32_bf16 v[88:91], v[200:203], a[232:235], v[88:91]\n"
"v_cvt_pk_bf16_f32 v48, v76, v77\n"
"v_cvt_pk_bf16_f32 v49, v78, v79\n"
"v_mfma_f32_16x16x32_bf16 v[88:91], v[204:207], a[248:251], v[88:91]\n"
"s_waitcnt vmcnt(4)\n"
"s_barrier\n"
"v_mfma_f32_16x16x32_bf16 v[92:95], v[200:203], a[236:239], v[92:95]\n"
"ds_read_b64_tr_b16 v[200:201], v225 offset:40960\n"
"v_cvt_pk_bf16_f32 v2, v80, v81\n"
"v_mfma_f32_16x16x32_bf16 v[92:95], v[204:207], a[252:255], v[92:95]\n"
"ds_read_b64_tr_b16 v[202:203], v225 offset:41472\n"
"s_waitcnt lgkmcnt(10)\n"
"v_mfma_f32_16x16x32_bf16 v[96:99], v[208:211], a[224:227], v[96:99]\n"
"ds_read_b64_tr_b16 v[204:205], v225 offset:45056\n"
"v_cvt_pk_bf16_f32 v3, v82, v83\n"
"v_mfma_f32_16x16x32_bf16 v[96:99], v[212:215], a[240:243], v[96:99]\n"
"ds_read_b64_tr_b16 v[206:207], v225 offset:45568\n"
"v_cvt_pk_bf16_f32 v18, v84, v85\n"
"v_mfma_f32_16x16x32_bf16 v[100:103], v[208:211], a[228:231], v[100:103]\n"
"ds_read_b128 a[0:3], v232 offset:49152\n"
"v_cvt_pk_bf16_f32 v19, v86, v87\n"
"v_mfma_f32_16x16x32_bf16 v[100:103], v[212:215], a[244:247], v[100:103]\n"
"ds_read_b128 a[4:7], v232 offset:50176\n"
"v_cvt_pk_bf16_f32 v34, v88, v89\n"
"v_mfma_f32_16x16x32_bf16 v[104:107], v[208:211], a[232:235], v[104:107]\n"
"ds_read_b128 a[8:11], v232 offset:51200\n"
"v_cvt_pk_bf16_f32 v35, v90, v91\n"
"v_mfma_f32_16x16x32_bf16 v[104:107], v[212:215], a[248:251], v[104:107]\n"
"ds_read_b128 a[12:15], v232 offset:52224\n"
"v_cvt_pk_bf16_f32 v4, v96, v97\n"
"v_mfma_f32_16x16x32_bf16 v[108:111], v[208:211], a[236:239], v[108:111]\n"
"ds_read_b64_tr_b16 v[208:209], v226 offset:40960\n"
"v_cvt_pk_bf16_f32 v5, v98, v99\n"
"v_mfma_f32_16x16x32_bf16 v[108:111], v[212:215], a[252:255], v[108:111]\n"
"ds_read_b64_tr_b16 v[210:211], v226 offset:41472\n"
"s_waitcnt lgkmcnt(14)\n"
"v_mfma_f32_16x16x32_bf16 v[112:115], v[216:219], a[224:227], v[112:115]\n"
"ds_read_b64_tr_b16 v[212:213], v226 offset:45056\n"
"v_cvt_pk_bf16_f32 v20, v100, v101\n"
"v_mfma_f32_16x16x32_bf16 v[112:115], v[220:223], a[240:243], v[112:115]\n"
"ds_read_b64_tr_b16 v[214:215], v226 offset:45568\n"
"v_cvt_pk_bf16_f32 v21, v102, v103\n"
"v_mfma_f32_16x16x32_bf16 v[116:119], v[216:219], a[228:231], v[116:119]\n"
"ds_read_b128 a[16:19], v232 offset:53248\n"
"v_cvt_pk_bf16_f32 v36, v104, v105\n"
"v_mfma_f32_16x16x32_bf16 v[116:119], v[220:223], a[244:247], v[116:119]\n"
"ds_read_b128 a[20:23], v232 offset:54272\n"
"v_cvt_pk_bf16_f32 v37, v106, v107\n"
"v_mfma_f32_16x16x32_bf16 v[120:123], v[216:219], a[232:235], v[120:123]\n"
"ds_read_b128 a[24:27], v232 offset:55296\n"
"v_cvt_pk_bf16_f32 v50, v92, v93\n"
"v_mfma_f32_16x16x32_bf16 v[120:123], v[220:223], a[248:251], v[120:123]\n"
"ds_read_b128 a[28:31], v232 offset:56320\n"
"v_cvt_pk_bf16_f32 v6, v112, v113\n"
"v_mfma_f32_16x16x32_bf16 v[124:127], v[216:219], a[236:239], v[124:127]\n"
"ds_read_b64_tr_b16 v[216:217], v227 offset:40960\n"
"v_cvt_pk_bf16_f32 v7, v114, v115\n"
"v_mfma_f32_16x16x32_bf16 v[124:127], v[220:223], a[252:255], v[124:127]\n"
"ds_read_b64_tr_b16 v[218:219], v227 offset:41472\n"
"s_waitcnt lgkmcnt(14)\n"
"v_mfma_f32_16x16x32_bf16 v[128:131], v[192:195], a[224:227], v[128:131]\n"
"ds_read_b64_tr_b16 v[220:221], v227 offset:45056\n"
"v_cvt_pk_bf16_f32 v22, v116, v117\n"
"v_mfma_f32_16x16x32_bf16 v[128:131], v[196:199], a[240:243], v[128:131]\n"
"ds_read_b64_tr_b16 v[222:223], v227 offset:45568\n"
"v_cvt_pk_bf16_f32 v23, v118, v119\n"
"v_mfma_f32_16x16x32_bf16 v[132:135], v[192:195], a[228:231], v[132:135]\n"
"ds_read_b128 a[32:35], v232 offset:0\n"
"v_cvt_pk_bf16_f32 v38, v120, v121\n"
"v_mfma_f32_16x16x32_bf16 v[132:135], v[196:199], a[244:247], v[132:135]\n"
"ds_read_b128 a[36:39], v232 offset:1024\n"
"v_cvt_pk_bf16_f32 v39, v122, v123\n"
"v_mfma_f32_16x16x32_bf16 v[136:139], v[192:195], a[232:235], v[136:139]\n"
"ds_read_b128 a[40:43], v232 offset:2048\n"
"v_cvt_pk_bf16_f32 v51, v94, v95\n"
"v_mfma_f32_16x16x32_bf16 v[136:139], v[196:199], a[248:251], v[136:139]\n"
"v_cvt_pk_bf16_f32 v8, v128, v129\n"
"v_cvt_pk_bf16_f32 v9, v130, v131\n"
"v_mfma_f32_16x16x32_bf16 v[140:143], v[192:195], a[236:239], v[140:143]\n"
"ds_read_b128 a[44:47], v232 offset:3072\n"
"v_cvt_pk_bf16_f32 v52, v108, v109\n"
"v_mfma_f32_16x16x32_bf16 v[140:143], v[196:199], a[252:255], v[140:143]\n"
"v_cvt_pk_bf16_f32 v24, v132, v133\n"
"v_mfma_f32_16x16x32_bf16 v[144:147], v[200:203], a[224:227], v[144:147]\n"
"v_cvt_pk_bf16_f32 v25, v134, v135\n"
"ds_read_b128 a[48:51], v232 offset:4096\n"
"v_mfma_f32_16x16x32_bf16 v[144:147], v[204:207], a[240:243], v[144:147]\n"
"v_cvt_pk_bf16_f32 v40, v136, v137\n"
"v_cvt_pk_bf16_f32 v41, v138, v139\n"
"v_mfma_f32_16x16x32_bf16 v[148:151], v[200:203], a[228:231], v[148:151]\n"
"ds_read_b128 a[52:55], v232 offset:5120\n"
"v_cvt_pk_bf16_f32 v53, v110, v111\n"
"v_mfma_f32_16x16x32_bf16 v[148:151], v[204:207], a[244:247], v[148:151]\n"
"ds_read_b128 a[56:59], v232 offset:6144\n"
"v_cvt_pk_bf16_f32 v54, v124, v125\n"
"v_mfma_f32_16x16x32_bf16 v[152:155], v[200:203], a[232:235], v[152:155]\n"
"ds_read_b128 a[60:63], v232 offset:7168\n"
"v_cvt_pk_bf16_f32 v55, v126, v127\n"
"v_mfma_f32_16x16x32_bf16 v[152:155], v[204:207], a[248:251], v[152:155]\n"
"v_cvt_pk_bf16_f32 v10, v144, v145\n"
"v_cvt_pk_bf16_f32 v11, v146, v147\n"
"v_mfma_f32_16x16x32_bf16 v[156:159], v[200:203], a[236:239], v[156:159]\n"
"v_cvt_pk_bf16_f32 v56, v140, v141\n"
"v_cvt_pk_bf16_f32 v57, v142, v143\n"
"v_mfma_f32_16x16x32_bf16 v[156:159], v[204:207], a[252:255], v[156:159]\n"
"v_cvt_pk_bf16_f32 v26, v148, v149\n"
"s_waitcnt lgkmcnt(14)\n"
"v_mfma_f32_16x16x32_bf16 v[160:163], v[208:211], a[224:227], v[160:163]\n"
"v_cvt_pk_bf16_f32 v27, v150, v151\n"
"ds_read_b128 a[64:67], v236 offset:0\n"
"v_mfma_f32_16x16x32_bf16 v[160:163], v[212:215], a[240:243], v[160:163]\n"
"v_cvt_pk_bf16_f32 v42, v152, v153\n"
"v_cvt_pk_bf16_f32 v43, v154, v155\n"
"v_mfma_f32_16x16x32_bf16 v[164:167], v[208:211], a[228:231], v[164:167]\n"
"ds_read_b128 a[68:71], v236 offset:1024\n"
"v_mfma_f32_16x16x32_bf16 v[164:167], v[212:215], a[244:247], v[164:167]\n"
"v_cvt_pk_bf16_f32 v58, v156, v157\n"
"v_cvt_pk_bf16_f32 v59, v158, v159\n"
"v_mfma_f32_16x16x32_bf16 v[168:171], v[208:211], a[232:235], v[168:171]\n"
"ds_read_b128 a[72:75], v236 offset:2048\n"
"v_mfma_f32_16x16x32_bf16 v[168:171], v[212:215], a[248:251], v[168:171]\n"
"v_cvt_pk_bf16_f32 v12, v160, v161\n"
"v_cvt_pk_bf16_f32 v13, v162, v163\n"
"v_mfma_f32_16x16x32_bf16 v[172:175], v[208:211], a[236:239], v[172:175]\n"
"ds_read_b128 a[76:79], v236 offset:3072\n"
"v_mfma_f32_16x16x32_bf16 v[172:175], v[212:215], a[252:255], v[172:175]\n"
"v_cvt_pk_bf16_f32 v28, v164, v165\n"
"s_waitcnt lgkmcnt(12)\n"
"v_mfma_f32_16x16x32_bf16 v[176:179], v[216:219], a[224:227], v[176:179]\n"
"v_cvt_pk_bf16_f32 v29, v166, v167\n"
"ds_read_b128 a[80:83], v236 offset:4096\n"
"v_mfma_f32_16x16x32_bf16 v[176:179], v[220:223], a[240:243], v[176:179]\n"
"v_cvt_pk_bf16_f32 v44, v168, v169\n"
"v_cvt_pk_bf16_f32 v45, v170, v171\n"
"v_mfma_f32_16x16x32_bf16 v[180:183], v[216:219], a[228:231], v[180:183]\n"
"ds_read_b128 a[84:87], v236 offset:5120\n"
"v_mfma_f32_16x16x32_bf16 v[180:183], v[220:223], a[244:247], v[180:183]\n"
"v_cvt_pk_bf16_f32 v60, v172, v173\n"
"v_cvt_pk_bf16_f32 v61, v174, v175\n"
"v_mfma_f32_16x16x32_bf16 v[184:187], v[216:219], a[232:235], v[184:187]\n"
"ds_read_b128 a[88:91], v236 offset:6144\n"
"v_mfma_f32_16x16x32_bf16 v[184:187], v[220:223], a[248:251], v[184:187]\n"
"v_cvt_pk_bf16_f32 v14, v176, v177\n"
"v_cvt_pk_bf16_f32 v15, v178, v179\n"
"v_mfma_f32_16x16x32_bf16 v[188:191], v[216:219], a[236:239], v[188:191]\n"
"ds_read_b128 a[92:95], v236 offset:7168\n"
"v_mfma_f32_16x16x32_bf16 v[188:191], v[220:223], a[252:255], v[188:191]\n"
"v_cvt_pk_bf16_f32 v30, v180, v181\n"
"v_mfma_f32_16x16x32_bf16 v[192:195], a[0:3], v[0:3], v[228:231]\n"
"v_cvt_pk_bf16_f32 v31, v182, v183\n"
"ds_read_b128 a[96:99], v236 offset:8192\n"
"v_mfma_f32_16x16x32_bf16 v[192:195], a[4:7], v[4:7], v[192:195]\n"
"v_cvt_pk_bf16_f32 v46, v184, v185\n"
"v_cvt_pk_bf16_f32 v47, v186, v187\n"
"v_mfma_f32_16x16x32_bf16 v[192:195], a[8:11], v[8:11], v[192:195]\n"
"ds_read_b128 a[100:103], v236 offset:9216\n"
"v_mfma_f32_16x16x32_bf16 v[192:195], a[12:15], v[12:15], v[192:195]\n"
"v_cvt_pk_bf16_f32 v62, v188, v189\n"
"v_mfma_f32_16x16x32_bf16 v[196:199], a[16:19], v[0:3], v[252:255]\n"
"v_cvt_pk_bf16_f32 v63, v190, v191\n"
"ds_read_b128 a[104:107], v236 offset:10240\n"
"v_mfma_f32_16x16x32_bf16 v[196:199], a[20:23], v[4:7], v[196:199]\n"
"ds_read_b128 a[108:111], v236 offset:11264\n"
"v_mfma_f32_16x16x32_bf16 v[196:199], a[24:27], v[8:11], v[196:199]\n"
"ds_read_b128 a[112:115], v236 offset:12288\n"
"v_mfma_f32_16x16x32_bf16 v[196:199], a[28:31], v[12:15], v[196:199]\n"
"v_max3_f32 v245, v192, v193, v194\n"
"ds_read_b128 a[116:119], v236 offset:13312\n"
"v_mfma_f32_16x16x32_bf16 v[200:203], a[0:3], v[16:19], 0\n"
"ds_read_b128 a[120:123], v236 offset:14336\n"
"v_mfma_f32_16x16x32_bf16 v[200:203], a[4:7], v[20:23], v[200:203]\n"
"ds_read_b128 a[124:127], v236 offset:15360\n"
"v_mfma_f32_16x16x32_bf16 v[200:203], a[8:11], v[24:27], v[200:203]\n"
"ds_write_b128 v234, v[0:3] offset:0\n"
"v_mfma_f32_16x16x32_bf16 v[200:203], a[12:15], v[28:31], v[200:203]\n"
"v_max3_f32 v245, v245, v195, v196\n"
"v_max3_f32 v245, v245, v197, v198\n"
"v_mfma_f32_16x16x32_bf16 v[204:207], a[16:19], v[16:19], v[228:231]\n"
"v_max_f32 v245, v245, v199\n"
"v_mov_b32 v246, v245\n"
"v_mfma_f32_16x16x32_bf16 v[204:207], a[20:23], v[20:23], v[204:207]\n"
"s_nop 0\n"
"v_permlane16_swap_b32 v245, v246\n"
"v_max_f32 v245, v245, v246\n"
"v_mov_b32 v246, v245\n"
"v_mfma_f32_16x16x32_bf16 v[204:207], a[24:27], v[24:27], v[204:207]\n"
"s_nop 0\n"
"v_permlane32_swap_b32 v245, v246\n"
"v_max_f32 v245, v245, v246\n"
"v_sub_f32 v192, v192, v245\n"
"v_mfma_f32_16x16x32_bf16 v[204:207], a[28:31], v[28:31], v[204:207]\n"
"v_sub_f32 v193, v193, v245\n"
"v_sub_f32 v194, v194, v245\n"
"v_sub_f32 v195, v195, v245\n"
"v_exp_f32 v192, v192\n"
"v_exp_f32 v193, v193\n"
"v_exp_f32 v194, v194\n"
"v_exp_f32 v195, v195\n"
"v_cvt_pk_bf16_f32 v208, v192, v193\n"
"v_cvt_pk_bf16_f32 v209, v194, v195\n"
"v_mfma_f32_16x16x32_bf16 v[192:195], a[0:3], v[32:35], 0\n"
"v_sub_f32 v196, v196, v245\n"
"v_sub_f32 v197, v197, v245\n"
"v_mfma_f32_16x16x32_bf16 v[192:195], a[4:7], v[36:39], v[192:195]\n"
"v_sub_f32 v198, v198, v245\n"
"v_sub_f32 v199, v199, v245\n"
"v_mfma_f32_16x16x32_bf16 v[192:195], a[8:11], v[40:43], v[192:195]\n"
"v_exp_f32 v196, v196\n"
"v_exp_f32 v197, v197\n"
"v_mfma_f32_16x16x32_bf16 v[192:195], a[12:15], v[44:47], v[192:195]\n"
"v_exp_f32 v198, v198\n"
"v_exp_f32 v199, v199\n"
"v_cvt_pk_bf16_f32 v210, v196, v197\n"
"v_cvt_pk_bf16_f32 v211, v198, v199\n"
"v_mfma_f32_16x16x32_bf16 v[196:199], a[16:19], v[32:35], 0\n"
"v_sub_f32 v248, 0, v245\n"
"v_max3_f32 v245, v200, v201, v202\n"
"v_mfma_f32_16x16x32_bf16 v[196:199], a[20:23], v[36:39], v[196:199]\n"
"v_max3_f32 v245, v245, v203, v204\n"
"v_max3_f32 v245, v245, v205, v206\n"
"v_mfma_f32_16x16x32_bf16 v[196:199], a[24:27], v[40:43], v[196:199]\n"
"v_max_f32 v245, v245, v207\n"
"v_mov_b32 v246, v245\n"
"v_mfma_f32_16x16x32_bf16 v[196:199], a[28:31], v[44:47], v[196:199]\n"
"s_nop 0\n"
"v_permlane16_swap_b32 v245, v246\n"
"s_waitcnt lgkmcnt(14)\n"
"v_mfma_f32_16x16x32_bf16 v[64:67], a[32:35], v[208:211], 0\n"
"v_max_f32 v245, v245, v246\n"
"v_mov_b32 v246, v245\n"
"v_mfma_f32_16x16x32_bf16 v[68:71], a[36:39], v[208:211], 0\n"
"s_nop 0\n"
"v_permlane32_swap_b32 v245, v246\n"
"v_max_f32 v245, v245, v246\n"
"v_mfma_f32_16x16x32_bf16 v[72:75], a[40:43], v[208:211], 0\n"
"v_sub_f32 v200, v200, v245\n"
"v_sub_f32 v201, v201, v245\n"
"v_mfma_f32_16x16x32_bf16 v[76:79], a[44:47], v[208:211], 0\n"
"v_sub_f32 v202, v202, v245\n"
"v_mfma_f32_16x16x32_bf16 v[80:83], a[48:51], v[208:211], 0\n"
"v_sub_f32 v203, v203, v245\n"
"v_sub_f32 v204, v204, v245\n"
"v_mfma_f32_16x16x32_bf16 v[84:87], a[52:55], v[208:211], 0\n"
"v_exp_f32 v200, v200\n"
"v_mfma_f32_16x16x32_bf16 v[88:91], a[56:59], v[208:211], 0\n"
"v_exp_f32 v201, v201\n"
"v_mfma_f32_16x16x32_bf16 v[92:95], a[60:63], v[208:211], 0\n"
"v_exp_f32 v202, v202\n"
"v_exp_f32 v203, v203\n"
"v_mfma_f32_16x16x32_bf16 a[128:131], a[144:147], v[208:211], 0\n"
"v_cvt_pk_bf16_f32 v212, v200, v201\n"
"v_cvt_pk_bf16_f32 v213, v202, v203\n"
"v_mfma_f32_16x16x32_bf16 v[200:203], a[0:3], v[48:51], 0\n"
"v_sub_f32 v205, v205, v245\n"
"v_sub_f32 v206, v206, v245\n"
"v_mfma_f32_16x16x32_bf16 v[200:203], a[4:7], v[52:55], v[200:203]\n"
"v_sub_f32 v207, v207, v245\n"
"v_sub_f32 v249, 0, v245\n"
"v_mfma_f32_16x16x32_bf16 v[200:203], a[8:11], v[56:59], v[200:203]\n"
"v_exp_f32 v204, v204\n"
"v_exp_f32 v205, v205\n"
"v_mfma_f32_16x16x32_bf16 v[200:203], a[12:15], v[60:63], v[200:203]\n"
"v_exp_f32 v206, v206\n"
"v_exp_f32 v207, v207\n"
"v_cvt_pk_bf16_f32 v214, v204, v205\n"
"v_cvt_pk_bf16_f32 v215, v206, v207\n"
"v_mfma_f32_16x16x32_bf16 v[204:207], a[16:19], v[48:51], 0\n"
"v_max3_f32 v245, v192, v193, v194\n"
"v_max3_f32 v245, v245, v195, v196\n"
"v_mfma_f32_16x16x32_bf16 v[204:207], a[20:23], v[52:55], v[204:207]\n"
"v_max3_f32 v245, v245, v197, v198\n"
"v_max_f32 v245, v245, v199\n"
"v_mfma_f32_16x16x32_bf16 v[204:207], a[24:27], v[56:59], v[204:207]\n"
"v_mov_b32 v246, v245\n"
"v_add_f32 v252, s35, v248\n"
"v_mfma_f32_16x16x32_bf16 v[204:207], a[28:31], v[60:63], v[204:207]\n"
"v_permlane16_swap_b32 v245, v246\n"
"v_max_f32 v245, v245, v246\n"
"v_mfma_f32_16x16x32_bf16 v[96:99], a[32:35], v[212:215], 0\n"
"v_mov_b32 v246, v245\n"
"v_add_f32 v253, s35, v248\n"
"v_mfma_f32_16x16x32_bf16 v[100:103], a[36:39], v[212:215], 0\n"
"v_permlane32_swap_b32 v245, v246\n"
"v_max_f32 v245, v245, v246\n"
"v_mfma_f32_16x16x32_bf16 v[104:107], a[40:43], v[212:215], 0\n"
"v_sub_f32 v192, v192, v245\n"
"v_sub_f32 v193, v193, v245\n"
"v_mfma_f32_16x16x32_bf16 v[108:111], a[44:47], v[212:215], 0\n"
"v_sub_f32 v194, v194, v245\n"
"v_sub_f32 v195, v195, v245\n"
"v_mfma_f32_16x16x32_bf16 v[112:115], a[48:51], v[212:215], 0\n"
"v_exp_f32 v192, v192\n"
"v_mfma_f32_16x16x32_bf16 v[116:119], a[52:55], v[212:215], 0\n"
"v_exp_f32 v193, v193\n"
"v_mfma_f32_16x16x32_bf16 v[120:123], a[56:59], v[212:215], 0\n"
"v_exp_f32 v194, v194\n"
"v_mfma_f32_16x16x32_bf16 v[124:127], a[60:63], v[212:215], 0\n"
"v_exp_f32 v195, v195\n"
"v_add_f32 v254, s35, v248\n"
"v_mfma_f32_16x16x32_bf16 a[132:135], a[144:147], v[212:215], 0\n"
"v_add_f32 v255, s35, v248\n"
"v_cvt_pk_bf16_f32 v208, v192, v193\n"
"v_cvt_pk_bf16_f32 v209, v194, v195\n"
"s_waitcnt lgkmcnt(13)\n"
"v_mfma_f32_16x16x32_bf16 v[192:195], a[64:67], v[0:3], v[252:255]\n"
"v_sub_f32 v196, v196, v245\n"
"v_sub_f32 v197, v197, v245\n"
"v_mfma_f32_16x16x32_bf16 v[192:195], a[68:71], v[4:7], v[192:195]\n"
"v_sub_f32 v198, v198, v245\n"
"v_sub_f32 v199, v199, v245\n"
"v_mfma_f32_16x16x32_bf16 v[192:195], a[72:75], v[8:11], v[192:195]\n"
"v_exp_f32 v196, v196\n"
"v_exp_f32 v197, v197\n"
"v_mfma_f32_16x16x32_bf16 v[192:195], a[76:79], v[12:15], v[192:195]\n"
"v_cvt_pk_bf16_f32 v210, v196, v197\n"
"v_exp_f32 v198, v198\n"
"v_exp_f32 v199, v199\n"
"s_nop 0\n"
"v_cvt_pk_bf16_f32 v211, v198, v199\n"
"s_waitcnt lgkmcnt(9)\n"
"v_mfma_f32_16x16x32_bf16 v[196:199], a[80:83], v[0:3], v[252:255]\n"
"v_sub_f32 v250, 0, v245\n"
"v_max3_f32 v245, v200, v201, v202\n"
"v_mfma_f32_16x16x32_bf16 v[196:199], a[84:87], v[4:7], v[196:199]\n"
"v_max3_f32 v245, v245, v203, v204\n"
"v_max3_f32 v245, v245, v205, v206\n"
"v_mfma_f32_16x16x32_bf16 v[196:199], a[88:91], v[8:11], v[196:199]\n"
"v_max_f32 v245, v245, v207\n"
"v_mov_b32 v246, v245\n"
"v_mfma_f32_16x16x32_bf16 v[196:199], a[92:95], v[12:15], v[196:199]\n"
"s_nop 0\n"
"v_permlane16_swap_b32 v245, v246\n"
"v_max_f32 v245, v245, v246\n"
"v_mfma_f32_16x16x32_bf16 v[128:131], a[32:35], v[208:211], 0\n"
"v_mov_b32 v246, v245\n"
"v_add_f32 v252, s35, v249\n"
"v_mfma_f32_16x16x32_bf16 v[132:135], a[36:39], v[208:211], 0\n"
"v_permlane32_swap_b32 v245, v246\n"
"v_max_f32 v245, v245, v246\n"
"v_mfma_f32_16x16x32_bf16 v[136:139], a[40:43], v[208:211], 0\n"
"v_sub_f32 v200, v200, v245\n"
"v_sub_f32 v201, v201, v245\n"
"v_mfma_f32_16x16x32_bf16 v[140:143], a[44:47], v[208:211], 0\n"
"v_sub_f32 v202, v202, v245\n"
"v_sub_f32 v203, v203, v245\n"
"v_mfma_f32_16x16x32_bf16 v[144:147], a[48:51], v[208:211], 0\n"
"v_exp_f32 v200, v200\n"
"v_mfma_f32_16x16x32_bf16 v[148:151], a[52:55], v[208:211], 0\n"
"v_exp_f32 v201, v201\n"
"v_mfma_f32_16x16x32_bf16 v[152:155], a[56:59], v[208:211], 0\n"
"v_exp_f32 v202, v202\n"
"v_mfma_f32_16x16x32_bf16 v[156:159], a[60:63], v[208:211], 0\n"
"v_exp_f32 v203, v203\n"
"v_add_f32 v253, s35, v249\n"
"v_mfma_f32_16x16x32_bf16 a[136:139], a[144:147], v[208:211], 0\n"
"v_add_f32 v254, s35, v249\n"
"v_add_f32 v255, s35, v249\n"
"v_cvt_pk_bf16_f32 v212, v200, v201\n"
"v_cvt_pk_bf16_f32 v213, v202, v203\n"
"v_mfma_f32_16x16x32_bf16 v[200:203], a[64:67], v[16:19], v[252:255]\n"
"v_sub_f32 v204, v204, v245\n"
"v_sub_f32 v205, v205, v245\n"
"v_mfma_f32_16x16x32_bf16 v[200:203], a[68:71], v[20:23], v[200:203]\n"
"v_sub_f32 v206, v206, v245\n"
"v_sub_f32 v207, v207, v245\n"
"v_mfma_f32_16x16x32_bf16 v[200:203], a[72:75], v[24:27], v[200:203]\n"
"v_exp_f32 v204, v204\n"
"v_exp_f32 v205, v205\n"
"v_mfma_f32_16x16x32_bf16 v[200:203], a[76:79], v[28:31], v[200:203]\n"
"v_exp_f32 v206, v206\n"
"v_exp_f32 v207, v207\n"
"v_cvt_pk_bf16_f32 v214, v204, v205\n"
"v_cvt_pk_bf16_f32 v215, v206, v207\n"
"v_mfma_f32_16x16x32_bf16 v[204:207], a[80:83], v[16:19], v[252:255]\n"
"v_exp_f32 v192, v192\n"
"v_mfma_f32_16x16x32_bf16 v[204:207], a[84:87], v[20:23], v[204:207]\n"
"v_exp_f32 v193, v193\n"
"v_mfma_f32_16x16x32_bf16 v[204:207], a[88:91], v[24:27], v[204:207]\n"
"v_exp_f32 v194, v194\n"
"v_mfma_f32_16x16x32_bf16 v[204:207], a[92:95], v[28:31], v[204:207]\n"
"v_exp_f32 v195, v195\n"
"v_mfma_f32_16x16x32_bf16 v[160:163], a[32:35], v[212:215], 0\n"
"v_and_b32 v252, s35, v240\n"
"v_add_f32 v252, v252, v250\n"
"v_mfma_f32_16x16x32_bf16 v[164:167], a[36:39], v[212:215], 0\n"
"v_and_b32 v253, s35, v241\n"
"v_add_f32 v253, v253, v250\n"
"v_mfma_f32_16x16x32_bf16 v[168:171], a[40:43], v[212:215], 0\n"
"v_and_b32 v254, s35, v242\n"
"v_add_f32 v254, v254, v250\n"
"v_mfma_f32_16x16x32_bf16 v[172:175], a[44:47], v[212:215], 0\n"
"v_and_b32 v255, s35, v243\n"
"v_add_f32 v255, v255, v250\n"
"v_mfma_f32_16x16x32_bf16 v[176:179], a[48:51], v[212:215], 0\n"
"v_cvt_pk_bf16_f32 v208, v192, v193\n"
"v_cvt_pk_bf16_f32 v209, v194, v195\n"
"v_mfma_f32_16x16x32_bf16 v[180:183], a[52:55], v[212:215], 0\n"
"v_exp_f32 v196, v196\n"
"v_mfma_f32_16x16x32_bf16 v[184:187], a[56:59], v[212:215], 0\n"
"v_exp_f32 v197, v197\n"
"v_mfma_f32_16x16x32_bf16 v[188:191], a[60:63], v[212:215], 0\n"
"v_exp_f32 v198, v198\n"
"v_mfma_f32_16x16x32_bf16 a[140:143], a[144:147], v[212:215], 0\n"
"v_exp_f32 v199, v199\n"
"v_mfma_f32_16x16x32_bf16 v[192:195], a[64:67], v[32:35], v[252:255]\n"
"v_cvt_pk_bf16_f32 v210, v196, v197\n"
"v_cvt_pk_bf16_f32 v211, v198, v199\n"
"v_mfma_f32_16x16x32_bf16 v[192:195], a[68:71], v[36:39], v[192:195]\n"
"v_sub_f32 v251, 0, v245\n"
"v_mov_b32 v228, v251\n"
"v_mfma_f32_16x16x32_bf16 v[192:195], a[72:75], v[40:43], v[192:195]\n"
"v_add_f32 v252, s35, v250\n"
"v_add_f32 v253, s35, v250\n"
"v_add_f32 v254, s35, v250\n"
"v_mfma_f32_16x16x32_bf16 v[192:195], a[76:79], v[44:47], v[192:195]\n"
"v_add_f32 v255, s35, v250\n"
"v_mov_b32 v229, v251\n"
"s_nop 0\n"
"v_mfma_f32_16x16x32_bf16 v[196:199], a[80:83], v[32:35], v[252:255]\n"
"v_mov_b32 v230, v251\n"
"v_mov_b32 v231, v251\n"
"v_mfma_f32_16x16x32_bf16 v[196:199], a[84:87], v[36:39], v[196:199]\n"
"v_exp_f32 v200, v200\n"
"v_mfma_f32_16x16x32_bf16 v[196:199], a[88:91], v[40:43], v[196:199]\n"
"v_exp_f32 v201, v201\n"
"v_mfma_f32_16x16x32_bf16 v[196:199], a[92:95], v[44:47], v[196:199]\n"
"v_cvt_pk_bf16_f32 v212, v200, v201\n"
"s_waitcnt lgkmcnt(5)\n"
"v_mfma_f32_16x16x32_bf16 v[64:67], a[96:99], v[208:211], v[64:67]\n"
"v_exp_f32 v202, v202\n"
"v_mfma_f32_16x16x32_bf16 v[68:71], a[100:103], v[208:211], v[68:71]\n"
"v_exp_f32 v203, v203\n"
"v_mfma_f32_16x16x32_bf16 v[72:75], a[104:107], v[208:211], v[72:75]\n"
"v_cvt_pk_bf16_f32 v213, v202, v203\n"
"v_and_b32 v252, s35, v240\n"
"v_mfma_f32_16x16x32_bf16 v[76:79], a[108:111], v[208:211], v[76:79]\n"
"v_add_f32 v252, v252, v251\n"
"s_waitcnt lgkmcnt(1)\n"
"v_mfma_f32_16x16x32_bf16 v[80:83], a[112:115], v[208:211], v[80:83]\n"
"v_exp_f32 v204, v204\n"
"v_mfma_f32_16x16x32_bf16 v[84:87], a[116:119], v[208:211], v[84:87]\n"
"v_exp_f32 v205, v205\n"
"v_mfma_f32_16x16x32_bf16 v[88:91], a[120:123], v[208:211], v[88:91]\n"
"v_exp_f32 v206, v206\n"
"v_mfma_f32_16x16x32_bf16 v[92:95], a[124:127], v[208:211], v[92:95]\n"
"v_exp_f32 v207, v207\n"
"v_mfma_f32_16x16x32_bf16 a[128:131], a[144:147], v[208:211], a[128:131]\n"
"v_and_b32 v253, s35, v241\n"
"v_add_f32 v253, v253, v251\n"
"v_mfma_f32_16x16x32_bf16 v[200:203], a[64:67], v[48:51], v[228:231]\n"
"v_and_b32 v254, s35, v242\n"
"v_add_f32 v254, v254, v251\n"
"v_mfma_f32_16x16x32_bf16 v[200:203], a[68:71], v[52:55], v[200:203]\n"
"v_and_b32 v255, s35, v243\n"
"v_add_f32 v255, v255, v251\n"
"v_mfma_f32_16x16x32_bf16 v[200:203], a[72:75], v[56:59], v[200:203]\n"
"v_cvt_pk_bf16_f32 v214, v204, v205\n"
"v_cvt_pk_bf16_f32 v215, v206, v207\n"
"v_mfma_f32_16x16x32_bf16 v[200:203], a[76:79], v[60:63], v[200:203]\n"
"ds_read_b128 a[0:3], v236 offset:16384\n"
"v_mov_b32 v216, v248\n"
"v_mfma_f32_16x16x32_bf16 v[204:207], a[80:83], v[48:51], v[252:255]\n"
"ds_read_b128 a[4:7], v236 offset:17408\n"
"v_mov_b32 v217, v248\n"
"v_mfma_f32_16x16x32_bf16 v[204:207], a[84:87], v[52:55], v[204:207]\n"
"ds_read_b128 a[8:11], v236 offset:18432\n"
"v_mov_b32 v218, v248\n"
"v_mfma_f32_16x16x32_bf16 v[204:207], a[88:91], v[56:59], v[204:207]\n"
"ds_read_b128 a[12:15], v236 offset:19456\n"
"v_mov_b32 v219, v248\n"
"v_mfma_f32_16x16x32_bf16 v[204:207], a[92:95], v[60:63], v[204:207]\n"
"ds_read_b128 a[16:19], v236 offset:20480\n"
"v_mov_b32 v220, v249\n"
"v_mfma_f32_16x16x32_bf16 v[96:99], a[96:99], v[212:215], v[96:99]\n"
"ds_read_b128 a[20:23], v236 offset:21504\n"
"v_mov_b32 v221, v249\n"
"v_mfma_f32_16x16x32_bf16 v[100:103], a[100:103], v[212:215], v[100:103]\n"
"ds_read_b128 a[24:27], v236 offset:22528\n"
"v_mov_b32 v222, v249\n"
"v_mfma_f32_16x16x32_bf16 v[104:107], a[104:107], v[212:215], v[104:107]\n"
"ds_read_b128 a[28:31], v236 offset:23552\n"
"v_mov_b32 v223, v249\n"
"v_mfma_f32_16x16x32_bf16 v[108:111], a[108:111], v[212:215], v[108:111]\n"
"v_exp_f32 v192, v192\n"
"v_mfma_f32_16x16x32_bf16 v[112:115], a[112:115], v[212:215], v[112:115]\n"
"v_exp_f32 v193, v193\n"
"v_mfma_f32_16x16x32_bf16 v[116:119], a[116:119], v[212:215], v[116:119]\n"
"v_exp_f32 v194, v194\n"
"v_mfma_f32_16x16x32_bf16 v[120:123], a[120:123], v[212:215], v[120:123]\n"
"v_exp_f32 v195, v195\n"
"v_mfma_f32_16x16x32_bf16 v[124:127], a[124:127], v[212:215], v[124:127]\n"
"v_cvt_pk_bf16_f32 v208, v192, v193\n"
"v_cvt_pk_bf16_f32 v209, v194, v195\n"
"v_mfma_f32_16x16x32_bf16 a[132:135], a[144:147], v[212:215], a[132:135]\n"
"v_mov_b32 v224, v250\n"
"s_waitcnt lgkmcnt(4)\n"
"v_mfma_f32_16x16x32_bf16 v[192:195], a[0:3], v[0:3], v[216:219]\n"
"v_exp_f32 v196, v196\n"
"v_mfma_f32_16x16x32_bf16 v[192:195], a[4:7], v[4:7], v[192:195]\n"
"v_exp_f32 v197, v197\n"
"v_mfma_f32_16x16x32_bf16 v[192:195], a[8:11], v[8:11], v[192:195]\n"
"v_exp_f32 v198, v198\n"
"v_exp_f32 v199, v199\n"
"v_mfma_f32_16x16x32_bf16 v[192:195], a[12:15], v[12:15], v[192:195]\n"
"v_cvt_pk_bf16_f32 v210, v196, v197\n"
"v_cvt_pk_bf16_f32 v211, v198, v199\n"
"s_waitcnt lgkmcnt(0)\n"
"v_mfma_f32_16x16x32_bf16 v[196:199], a[16:19], v[0:3], v[216:219]\n"
"v_exp_f32 v200, v200\n"
"v_mfma_f32_16x16x32_bf16 v[196:199], a[20:23], v[4:7], v[196:199]\n"
"v_exp_f32 v201, v201\n"
"v_mfma_f32_16x16x32_bf16 v[196:199], a[24:27], v[8:11], v[196:199]\n"
"v_exp_f32 v202, v202\n"
"v_mfma_f32_16x16x32_bf16 v[196:199], a[28:31], v[12:15], v[196:199]\n"
"v_exp_f32 v203, v203\n"
"v_mfma_f32_16x16x32_bf16 v[128:131], a[96:99], v[208:211], v[128:131]\n"
"v_exp_f32 v204, v204\n"
"v_mfma_f32_16x16x32_bf16 v[132:135], a[100:103], v[208:211], v[132:135]\n"
"v_exp_f32 v205, v205\n"
"v_mfma_f32_16x16x32_bf16 v[136:139], a[104:107], v[208:211], v[136:139]\n"
"v_exp_f32 v206, v206\n"
"v_mfma_f32_16x16x32_bf16 v[140:143], a[108:111], v[208:211], v[140:143]\n"
"v_exp_f32 v207, v207\n"
"v_mfma_f32_16x16x32_bf16 v[144:147], a[112:115], v[208:211], v[144:147]\n"
"v_mov_b32 v225, v250\n"
"v_mov_b32 v226, v250\n"
"v_mfma_f32_16x16x32_bf16 v[148:151], a[116:119], v[208:211], v[148:151]\n"
"v_mov_b32 v227, v250\n"
"ds_read_b128 a[32:35], v236 offset:24576\n"
"v_mfma_f32_16x16x32_bf16 v[152:155], a[120:123], v[208:211], v[152:155]\n"
"ds_read_b128 a[36:39], v236 offset:25600\n"
"v_cvt_pk_bf16_f32 v212, v200, v201\n"
"v_mfma_f32_16x16x32_bf16 v[156:159], a[124:127], v[208:211], v[156:159]\n"
"ds_read_b128 a[40:43], v236 offset:26624\n"
"v_cvt_pk_bf16_f32 v213, v202, v203\n"
"v_mfma_f32_16x16x32_bf16 a[136:139], a[144:147], v[208:211], a[136:139]\n"
"ds_read_b128 a[44:47], v236 offset:27648\n"
"v_cvt_pk_bf16_f32 v214, v204, v205\n"
"ds_read_b128 a[48:51], v236 offset:28672\n"
"ds_read_b128 a[52:55], v236 offset:29696\n"
"ds_read_b128 a[56:59], v236 offset:30720\n"
"ds_read_b128 a[60:63], v236 offset:31744\n"
"ds_read_b128 a[64:67], v236 offset:32768\n"
"ds_read_b128 a[68:71], v236 offset:33792\n"
"ds_read_b128 a[72:75], v236 offset:34816\n"
"ds_read_b128 a[76:79], v236 offset:35840\n"
"ds_read_b128 a[80:83], v236 offset:36864\n"
"ds_read_b128 a[84:87], v236 offset:37888\n"
"ds_read_b128 a[88:91], v236 offset:38912\n"
"ds_read_b128 a[92:95], v236 offset:39936\n"
"v_cvt_pk_bf16_f32 v215, v206, v207\n"
"ds_write_b128 v234, v[4:7] offset:1024\n"
"ds_write_b128 v234, v[8:11] offset:2048\n"
"ds_write_b128 v234, v[12:15] offset:3072\n"
"ds_write_b128 v234, v[16:19] offset:4096\n"
"ds_write_b128 v234, v[20:23] offset:5120\n"
"ds_write_b128 v234, v[24:27] offset:6144\n"
"ds_write_b128 v234, v[28:31] offset:7168\n"
"ds_write_b128 v234, v[32:35] offset:8192\n"
"ds_write_b128 v234, v[36:39] offset:9216\n"
"ds_write_b128 v234, v[40:43] offset:10240\n"
"ds_write_b128 v234, v[44:47] offset:11264\n"
"ds_write_b128 v234, v[48:51] offset:12288\n"
"ds_write_b128 v234, v[52:55] offset:13312\n"
"ds_write_b128 v234, v[56:59] offset:14336\n"
"ds_write_b128 v234, v[60:63] offset:15360\n"
"ds_write_b128 v235, v[248:251]\n"
"s_waitcnt vmcnt(0) lgkmcnt(0)\n"
"s_barrier\n"
"Lent_%=:\n"
"s_cmp_eq_u32 s69, 0\n"
"s_cbranch_scc1 Lexit_%=\n"
".p2align 5\n"
"Lloop_%=:\n"
"v_mfma_f32_16x16x32_bf16 v[200:203], a[0:3], v[16:19], v[220:223]\n"
"v_exp_f32 v192, v192\n"
"v_mfma_f32_16x16x32_bf16 v[200:203], a[4:7], v[20:23], v[200:203]\n"
"s_add_u32 s74, s71, 24576\n"
"s_add_u32 s71, s71, s77\n"
"v_mfma_f32_16x16x32_bf16 v[200:203], a[8:11], v[24:27], v[200:203]\n"
"v_exp_f32 v193, v193\n"
"v_mfma_f32_16x16x32_bf16 v[200:203], a[12:15], v[28:31], v[200:203]\n"
"v_cvt_pk_bf16_f32 v208, v192, v193\n"
"s_sub_u32 s69, s69, 1\n"
"v_mfma_f32_16x16x32_bf16 v[204:207], a[16:19], v[16:19], v[220:223]\n"
"v_exp_f32 v194, v194\n"
"v_mfma_f32_16x16x32_bf16 v[204:207], a[20:23], v[20:23], v[204:207]\n"
"s_cmp_eq_u32 s71, s72\n"
"s_cselect_b32 s71, s73, s71\n"
"v_mfma_f32_16x16x32_bf16 v[204:207], a[24:27], v[24:27], v[204:207]\n"
"v_exp_f32 v195, v195\n"
"v_mfma_f32_16x16x32_bf16 v[204:207], a[28:31], v[28:31], v[204:207]\n"
"v_cvt_pk_bf16_f32 v209, v194, v195\n"
"s_add_u32 s75, s71, 8192\n"
"v_mfma_f32_16x16x32_bf16 v[160:163], a[96:99], v[212:215], v[160:163]\n"
"v_exp_f32 v196, v196\n"
"v_mfma_f32_16x16x32_bf16 v[164:167], a[100:103], v[212:215], v[164:167]\n"
"buffer_load_dwordx4 a[96:99], v232, s[44:47], s74 offen offset:0\n"
"s_add_u32 s76, s71, 16384\n"
"v_mfma_f32_16x16x32_bf16 v[168:171], a[104:107], v[212:215], v[168:171]\n"
"v_exp_f32 v197, v197\n"
"v_mfma_f32_16x16x32_bf16 v[172:175], a[108:111], v[212:215], v[172:175]\n"
"v_cvt_pk_bf16_f32 v210, v196, v197\n"
"v_mfma_f32_16x16x32_bf16 v[176:179], a[112:115], v[212:215], v[176:179]\n"
"v_exp_f32 v198, v198\n"
"v_mfma_f32_16x16x32_bf16 v[180:183], a[116:119], v[212:215], v[180:183]\n"
"buffer_load_dwordx4 a[100:103], v232, s[44:47], s74 offen offset:1024\n"
"v_mfma_f32_16x16x32_bf16 v[184:187], a[120:123], v[212:215], v[184:187]\n"
"v_exp_f32 v199, v199\n"
"v_mfma_f32_16x16x32_bf16 v[188:191], a[124:127], v[212:215], v[188:191]\n"
"v_cvt_pk_bf16_f32 v211, v198, v199\n"
"v_mfma_f32_16x16x32_bf16 a[140:143], a[144:147], v[212:215], a[140:143]\n"
"buffer_load_dwordx4 a[104:107], v232, s[44:47], s74 offen offset:2048\n"
"v_mfma_f32_16x16x32_bf16 v[192:195], a[0:3], v[32:35], v[224:227]\n"
"v_exp_f32 v200, v200\n"
"v_mfma_f32_16x16x32_bf16 v[192:195], a[4:7], v[36:39], v[192:195]\n"
"buffer_load_dwordx4 a[108:111], v232, s[44:47], s74 offen offset:3072\n"
"v_mfma_f32_16x16x32_bf16 v[192:195], a[8:11], v[40:43], v[192:195]\n"
"v_exp_f32 v201, v201\n"
"v_mfma_f32_16x16x32_bf16 v[192:195], a[12:15], v[44:47], v[192:195]\n"
"v_cvt_pk_bf16_f32 v212, v200, v201\n"
"v_mfma_f32_16x16x32_bf16 v[196:199], a[16:19], v[32:35], v[224:227]\n"
"v_exp_f32 v202, v202\n"
"v_mfma_f32_16x16x32_bf16 v[196:199], a[20:23], v[36:39], v[196:199]\n"
"buffer_load_dwordx4 a[112:115], v233, s[44:47], s74 offen offset:0\n"
"v_mfma_f32_16x16x32_bf16 v[196:199], a[24:27], v[40:43], v[196:199]\n"
"v_exp_f32 v203, v203\n"
"v_mfma_f32_16x16x32_bf16 v[196:199], a[28:31], v[44:47], v[196:199]\n"
"v_cvt_pk_bf16_f32 v213, v202, v203\n"
"s_waitcnt vmcnt(17)\n"
"v_mfma_f32_16x16x32_bf16 v[64:67], a[32:35], v[208:211], v[64:67]\n"
"v_exp_f32 v204, v204\n"
"v_mfma_f32_16x16x32_bf16 v[68:71], a[36:39], v[208:211], v[68:71]\n"
"buffer_load_dwordx4 a[116:119], v233, s[44:47], s74 offen offset:1024\n"
"v_mfma_f32_16x16x32_bf16 v[72:75], a[40:43], v[208:211], v[72:75]\n"
"v_exp_f32 v205, v205\n"
"v_mfma_f32_16x16x32_bf16 v[76:79], a[44:47], v[208:211], v[76:79]\n"
"v_cvt_pk_bf16_f32 v214, v204, v205\n"
"s_waitcnt vmcnt(14)\n"
"v_mfma_f32_16x16x32_bf16 v[80:83], a[48:51], v[208:211], v[80:83]\n"
"v_exp_f32 v206, v206\n"
"v_mfma_f32_16x16x32_bf16 v[84:87], a[52:55], v[208:211], v[84:87]\n"
"buffer_load_dwordx4 a[120:123], v233, s[44:47], s74 offen offset:2048\n"
"v_mfma_f32_16x16x32_bf16 v[88:91], a[56:59], v[208:211], v[88:91]\n"
"v_exp_f32 v207, v207\n"
"v_mfma_f32_16x16x32_bf16 v[92:95], a[60:63], v[208:211], v[92:95]\n"
"v_cvt_pk_bf16_f32 v215, v206, v207\n"
"v_mfma_f32_16x16x32_bf16 a[128:131], a[144:147], v[208:211], a[128:131]\n"
"buffer_load_dwordx4 a[124:127], v233, s[44:47], s74 offen offset:3072\n"
"v_mfma_f32_16x16x32_bf16 v[200:203], a[0:3], v[48:51], v[228:231]\n"
"v_exp_f32 v192, v192\n"
"v_mfma_f32_16x16x32_bf16 v[200:203], a[4:7], v[52:55], v[200:203]\n"
"buffer_load_dwordx4 a[0:3], v232, s[44:47], s71 offen offset:0\n"
"v_mfma_f32_16x16x32_bf16 v[200:203], a[8:11], v[56:59], v[200:203]\n"
"v_exp_f32 v193, v193\n"
"v_mfma_f32_16x16x32_bf16 v[200:203], a[12:15], v[60:63], v[200:203]\n"
"v_cvt_pk_bf16_f32 v208, v192, v193\n"
"v_mfma_f32_16x16x32_bf16 v[204:207], a[16:19], v[48:51], v[228:231]\n"
"v_exp_f32 v194, v194\n"
"v_mfma_f32_16x16x32_bf16 v[204:207], a[20:23], v[52:55], v[204:207]\n"
"buffer_load_dwordx4 a[4:7], v232, s[44:47], s71 offen offset:1024\n"
"v_mfma_f32_16x16x32_bf16 v[204:207], a[24:27], v[56:59], v[204:207]\n"
"v_exp_f32 v195, v195\n"
"v_mfma_f32_16x16x32_bf16 v[204:207], a[28:31], v[60:63], v[204:207]\n"
"v_cvt_pk_bf16_f32 v209, v194, v195\n"
"v_mfma_f32_16x16x32_bf16 v[96:99], a[32:35], v[212:215], v[96:99]\n"
"v_exp_f32 v196, v196\n"
"v_mfma_f32_16x16x32_bf16 v[100:103], a[36:39], v[212:215], v[100:103]\n"
"buffer_load_dwordx4 a[8:11], v232, s[44:47], s71 offen offset:2048\n"
"v_mfma_f32_16x16x32_bf16 v[104:107], a[40:43], v[212:215], v[104:107]\n"
"v_exp_f32 v197, v197\n"
"v_mfma_f32_16x16x32_bf16 v[108:111], a[44:47], v[212:215], v[108:111]\n"
"v_cvt_pk_bf16_f32 v210, v196, v197\n"
"v_mfma_f32_16x16x32_bf16 v[112:115], a[48:51], v[212:215], v[112:115]\n"
"v_exp_f32 v198, v198\n"
"v_mfma_f32_16x16x32_bf16 v[116:119], a[52:55], v[212:215], v[116:119]\n"
"buffer_load_dwordx4 a[12:15], v232, s[44:47], s71 offen offset:3072\n"
"v_mfma_f32_16x16x32_bf16 v[120:123], a[56:59], v[212:215], v[120:123]\n"
"v_exp_f32 v199, v199\n"
"v_mfma_f32_16x16x32_bf16 v[124:127], a[60:63], v[212:215], v[124:127]\n"
"v_cvt_pk_bf16_f32 v211, v198, v199\n"
"v_mfma_f32_16x16x32_bf16 a[132:135], a[144:147], v[212:215], a[132:135]\n"
"buffer_load_dwordx4 a[16:19], v233, s[44:47], s71 offen offset:0\n"
"s_waitcnt vmcnt(17)\n"
"v_mfma_f32_16x16x32_bf16 v[192:195], a[64:67], v[0:3], v[216:219]\n"
"v_exp_f32 v200, v200\n"
"v_mfma_f32_16x16x32_bf16 v[192:195], a[68:71], v[4:7], v[192:195]\n"
"buffer_load_dwordx4 a[20:23], v233, s[44:47], s71 offen offset:1024\n"
"v_mfma_f32_16x16x32_bf16 v[192:195], a[72:75], v[8:11], v[192:195]\n"
"v_exp_f32 v201, v201\n"
"v_mfma_f32_16x16x32_bf16 v[192:195], a[76:79], v[12:15], v[192:195]\n"
"v_cvt_pk_bf16_f32 v212, v200, v201\n"
"s_waitcnt vmcnt(14)\n"
"v_mfma_f32_16x16x32_bf16 v[196:199], a[80:83], v[0:3], v[216:219]\n"
"v_exp_f32 v202, v202\n"
"v_mfma_f32_16x16x32_bf16 v[196:199], a[84:87], v[4:7], v[196:199]\n"
"buffer_load_dwordx4 a[24:27], v233, s[44:47], s71 offen offset:2048\n"
"v_mfma_f32_16x16x32_bf16 v[196:199], a[88:91], v[8:11], v[196:199]\n"
"v_exp_f32 v203, v203\n"
"v_mfma_f32_16x16x32_bf16 v[196:199], a[92:95], v[12:15], v[196:199]\n"
"v_cvt_pk_bf16_f32 v213, v202, v203\n"
"v_mfma_f32_16x16x32_bf16 v[128:131], a[32:35], v[208:211], v[128:131]\n"
"v_exp_f32 v204, v204\n"
"v_mfma_f32_16x16x32_bf16 v[132:135], a[36:39], v[208:211], v[132:135]\n"
"buffer_load_dwordx4 a[28:31], v233, s[44:47], s71 offen offset:3072\n"
"s_mul_i32 s60, s59, 32896\n"
"v_mfma_f32_16x16x32_bf16 v[136:139], a[40:43], v[208:211], v[136:139]\n"
"v_exp_f32 v205, v205\n"
"v_mfma_f32_16x16x32_bf16 v[140:143], a[44:47], v[208:211], v[140:143]\n"
"v_cvt_pk_bf16_f32 v214, v204, v205\n"
"s_and_b32 s60, s60, 0xffff0080\n"
"v_mfma_f32_16x16x32_bf16 v[144:147], a[48:51], v[208:211], v[144:147]\n"
"v_exp_f32 v206, v206\n"
"v_mfma_f32_16x16x32_bf16 v[148:151], a[52:55], v[208:211], v[148:151]\n"
"s_lshl_b32 s16, s59, 13\n"
"s_and_b32 s16, s16, 0x3ffff\n"
"v_mfma_f32_16x16x32_bf16 v[152:155], a[56:59], v[208:211], v[152:155]\n"
"v_exp_f32 v207, v207\n"
"v_mfma_f32_16x16x32_bf16 v[156:159], a[60:63], v[208:211], v[156:159]\n"
"v_cvt_pk_bf16_f32 v215, v206, v207\n"
"s_add_u32 m0, s16, s61\n"
"v_mfma_f32_16x16x32_bf16 a[136:139], a[144:147], v[208:211], a[136:139]\n"
"buffer_load_dwordx4 v238, s[20:23], s60 offen offset:0 lds\n"
"s_add_u32 s59, s59, 1\n"
"v_mfma_f32_16x16x32_bf16 v[200:203], a[64:67], v[16:19], v[220:223]\n"
"v_exp_f32 v192, v192\n"
"v_mfma_f32_16x16x32_bf16 v[200:203], a[68:71], v[20:23], v[200:203]\n"
"s_cmp_ge_u32 s59, 11\n"
"s_cselect_b32 s59, 0x1018, s59\n"
"v_mfma_f32_16x16x32_bf16 v[200:203], a[72:75], v[24:27], v[200:203]\n"
"v_exp_f32 v193, v193\n"
"v_mfma_f32_16x16x32_bf16 v[200:203], a[76:79], v[28:31], v[200:203]\n"
"v_cvt_pk_bf16_f32 v208, v192, v193\n"
"v_mfma_f32_16x16x32_bf16 v[204:207], a[80:83], v[16:19], v[220:223]\n"
"v_exp_f32 v194, v194\n"
"v_mfma_f32_16x16x32_bf16 v[204:207], a[84:87], v[20:23], v[204:207]\n"
"buffer_load_dwordx4 v239, s[20:23], s60 offen offset:1024 lds\n"
"v_mfma_f32_16x16x32_bf16 v[204:207], a[88:91], v[24:27], v[204:207]\n"
"v_exp_f32 v195, v195\n"
"v_mfma_f32_16x16x32_bf16 v[204:207], a[92:95], v[28:31], v[204:207]\n"
"v_cvt_pk_bf16_f32 v209, v194, v195\n"
"v_mfma_f32_16x16x32_bf16 v[160:163], a[32:35], v[212:215], v[160:163]\n"
"v_exp_f32 v196, v196\n"
"v_mfma_f32_16x16x32_bf16 v[164:167], a[36:39], v[212:215], v[164:167]\n"
"buffer_load_dwordx4 a[32:35], v232, s[44:47], s75 offen offset:0\n"
"v_mfma_f32_16x16x32_bf16 v[168:171], a[40:43], v[212:215], v[168:171]\n"
"v_exp_f32 v197, v197\n"
"v_mfma_f32_16x16x32_bf16 v[172:175], a[44:47], v[212:215], v[172:175]\n"
"v_cvt_pk_bf16_f32 v210, v196, v197\n"
"v_mfma_f32_16x16x32_bf16 v[176:179], a[48:51], v[212:215], v[176:179]\n"
"v_exp_f32 v198, v198\n"
"v_mfma_f32_16x16x32_bf16 v[180:183], a[52:55], v[212:215], v[180:183]\n"
"buffer_load_dwordx4 a[36:39], v232, s[44:47], s75 offen offset:1024\n"
"v_mfma_f32_16x16x32_bf16 v[184:187], a[56:59], v[212:215], v[184:187]\n"
"v_exp_f32 v199, v199\n"
"v_mfma_f32_16x16x32_bf16 v[188:191], a[60:63], v[212:215], v[188:191]\n"
"v_cvt_pk_bf16_f32 v211, v198, v199\n"
"v_mfma_f32_16x16x32_bf16 a[140:143], a[144:147], v[212:215], a[140:143]\n"
"buffer_load_dwordx4 a[40:43], v232, s[44:47], s75 offen offset:2048\n"
"v_mfma_f32_16x16x32_bf16 v[192:195], a[64:67], v[32:35], v[224:227]\n"
"v_exp_f32 v200, v200\n"
"v_mfma_f32_16x16x32_bf16 v[192:195], a[68:71], v[36:39], v[192:195]\n"
"buffer_load_dwordx4 a[44:47], v232, s[44:47], s75 offen offset:3072\n"
"v_mfma_f32_16x16x32_bf16 v[192:195], a[72:75], v[40:43], v[192:195]\n"
"v_exp_f32 v201, v201\n"
"v_mfma_f32_16x16x32_bf16 v[192:195], a[76:79], v[44:47], v[192:195]\n"
"v_cvt_pk_bf16_f32 v212, v200, v201\n"
"v_mfma_f32_16x16x32_bf16 v[196:199], a[80:83], v[32:35], v[224:227]\n"
"v_exp_f32 v202, v202\n"
"v_mfma_f32_16x16x32_bf16 v[196:199], a[84:87], v[36:39], v[196:199]\n"
"buffer_load_dwordx4 a[48:51], v233, s[44:47], s75 offen offset:0\n"
"v_mfma_f32_16x16x32_bf16 v[196:199], a[88:91], v[40:43], v[196:199]\n"
"v_exp_f32 v203, v203\n"
"v_mfma_f32_16x16x32_bf16 v[196:199], a[92:95], v[44:47], v[196:199]\n"
"v_cvt_pk_bf16_f32 v213, v202, v203\n"
"s_waitcnt vmcnt(19)\n"
"v_mfma_f32_16x16x32_bf16 v[64:67], a[96:99], v[208:211], v[64:67]\n"
"v_exp_f32 v204, v204\n"
"v_mfma_f32_16x16x32_bf16 v[68:71], a[100:103], v[208:211], v[68:71]\n"
"buffer_load_dwordx4 a[52:55], v233, s[44:47], s75 offen offset:1024\n"
"v_mfma_f32_16x16x32_bf16 v[72:75], a[104:107], v[208:211], v[72:75]\n"
"v_exp_f32 v205, v205\n"
"v_mfma_f32_16x16x32_bf16 v[76:79], a[108:111], v[208:211], v[76:79]\n"
"v_cvt_pk_bf16_f32 v214, v204, v205\n"
"s_waitcnt vmcnt(16)\n"
"v_mfma_f32_16x16x32_bf16 v[80:83], a[112:115], v[208:211], v[80:83]\n"
"v_exp_f32 v206, v206\n"
"v_mfma_f32_16x16x32_bf16 v[84:87], a[116:119], v[208:211], v[84:87]\n"
"buffer_load_dwordx4 a[56:59], v233, s[44:47], s75 offen offset:2048\n"
"v_mfma_f32_16x16x32_bf16 v[88:91], a[120:123], v[208:211], v[88:91]\n"
"v_exp_f32 v207, v207\n"
"v_mfma_f32_16x16x32_bf16 v[92:95], a[124:127], v[208:211], v[92:95]\n"
"v_cvt_pk_bf16_f32 v215, v206, v207\n"
"v_mfma_f32_16x16x32_bf16 a[128:131], a[144:147], v[208:211], a[128:131]\n"
"buffer_load_dwordx4 a[60:63], v233, s[44:47], s75 offen offset:3072\n"
"v_mfma_f32_16x16x32_bf16 v[200:203], a[64:67], v[48:51], v[228:231]\n"
"v_exp_f32 v192, v192\n"
"v_mfma_f32_16x16x32_bf16 v[200:203], a[68:71], v[52:55], v[200:203]\n"
"buffer_load_dwordx4 a[64:67], v232, s[44:47], s76 offen offset:0\n"
"v_mfma_f32_16x16x32_bf16 v[200:203], a[72:75], v[56:59], v[200:203]\n"
"v_exp_f32 v193, v193\n"
"v_mfma_f32_16x16x32_bf16 v[200:203], a[76:79], v[60:63], v[200:203]\n"
"v_cvt_pk_bf16_f32 v208, v192, v193\n"
"v_mfma_f32_16x16x32_bf16 v[204:207], a[80:83], v[48:51], v[228:231]\n"
"v_exp_f32 v194, v194\n"
"v_mfma_f32_16x16x32_bf16 v[204:207], a[84:87], v[52:55], v[204:207]\n"
"buffer_load_dwordx4 a[68:71], v232, s[44:47], s76 offen offset:1024\n"
"v_mfma_f32_16x16x32_bf16 v[204:207], a[88:91], v[56:59], v[204:207]\n"
"v_exp_f32 v195, v195\n"
"v_mfma_f32_16x16x32_bf16 v[204:207], a[92:95], v[60:63], v[204:207]\n"
"v_cvt_pk_bf16_f32 v209, v194, v195\n"
"v_mfma_f32_16x16x32_bf16 v[96:99], a[96:99], v[212:215], v[96:99]\n"
"v_exp_f32 v196, v196\n"
"v_mfma_f32_16x16x32_bf16 v[100:103], a[100:103], v[212:215], v[100:103]\n"
"buffer_load_dwordx4 a[72:75], v232, s[44:47], s76 offen offset:2048\n"
"v_mfma_f32_16x16x32_bf16 v[104:107], a[104:107], v[212:215], v[104:107]\n"
"v_exp_f32 v197, v197\n"
"v_mfma_f32_16x16x32_bf16 v[108:111], a[108:111], v[212:215], v[108:111]\n"
"v_cvt_pk_bf16_f32 v210, v196, v197\n"
"v_mfma_f32_16x16x32_bf16 v[112:115], a[112:115], v[212:215], v[112:115]\n"
"v_exp_f32 v198, v198\n"
"v_mfma_f32_16x16x32_bf16 v[116:119], a[116:119], v[212:215], v[116:119]\n"
"buffer_load_dwordx4 a[76:79], v232, s[44:47], s76 offen offset:3072\n"
"v_mfma_f32_16x16x32_bf16 v[120:123], a[120:123], v[212:215], v[120:123]\n"
"v_exp_f32 v199, v199\n"
"v_mfma_f32_16x16x32_bf16 v[124:127], a[124:127], v[212:215], v[124:127]\n"
"v_cvt_pk_bf16_f32 v211, v198, v199\n"
"v_mfma_f32_16x16x32_bf16 a[132:135], a[144:147], v[212:215], a[132:135]\n"
"buffer_load_dwordx4 a[80:83], v233, s[44:47], s76 offen offset:0\n"
"s_waitcnt vmcnt(19)\n"
"v_mfma_f32_16x16x32_bf16 v[192:195], a[0:3], v[0:3], v[216:219]\n"
"v_exp_f32 v200, v200\n"
"v_mfma_f32_16x16x32_bf16 v[192:195], a[4:7], v[4:7], v[192:195]\n"
"buffer_load_dwordx4 a[84:87], v233, s[44:47], s76 offen offset:1024\n"
"v_mfma_f32_16x16x32_bf16 v[192:195], a[8:11], v[8:11], v[192:195]\n"
"v_exp_f32 v201, v201\n"
"v_mfma_f32_16x16x32_bf16 v[192:195], a[12:15], v[12:15], v[192:195]\n"
"v_cvt_pk_bf16_f32 v212, v200, v201\n"
"s_waitcnt vmcnt(16)\n"
"v_mfma_f32_16x16x32_bf16 v[196:199], a[16:19], v[0:3], v[216:219]\n"
"v_exp_f32 v202, v202\n"
"v_mfma_f32_16x16x32_bf16 v[196:199], a[20:23], v[4:7], v[196:199]\n"
"buffer_load_dwordx4 a[88:91], v233, s[44:47], s76 offen offset:2048\n"
"s_cmp_lg_u32 s69, 0\n"
"v_mfma_f32_16x16x32_bf16 v[196:199], a[24:27], v[8:11], v[196:199]\n"
"v_exp_f32 v203, v203\n"
"v_mfma_f32_16x16x32_bf16 v[196:199], a[28:31], v[12:15], v[196:199]\n"
"v_cvt_pk_bf16_f32 v213, v202, v203\n"
"v_mfma_f32_16x16x32_bf16 v[128:131], a[96:99], v[208:211], v[128:131]\n"
"v_exp_f32 v204, v204\n"
"v_mfma_f32_16x16x32_bf16 v[132:135], a[100:103], v[208:211], v[132:135]\n"
"buffer_load_dwordx4 a[92:95], v233, s[44:47], s76 offen offset:3072\n"
"v_mfma_f32_16x16x32_bf16 v[136:139], a[104:107], v[208:211], v[136:139]\n"
"v_exp_f32 v205, v205\n"
"v_mfma_f32_16x16x32_bf16 v[140:143], a[108:111], v[208:211], v[140:143]\n"
"v_cvt_pk_bf16_f32 v214, v204, v205\n"
"v_mfma_f32_16x16x32_bf16 v[144:147], a[112:115], v[208:211], v[144:147]\n"
"v_exp_f32 v206, v206\n"
"v_mfma_f32_16x16x32_bf16 v[148:151], a[116:119], v[208:211], v[148:151]\n"
"v_mfma_f32_16x16x32_bf16 v[152:155], a[120:123], v[208:211], v[152:155]\n"
"v_exp_f32 v207, v207\n"
"v_mfma_f32_16x16x32_bf16 v[156:159], a[124:127], v[208:211], v[156:159]\n"
"v_cvt_pk_bf16_f32 v215, v206, v207\n"
"v_mfma_f32_16x16x32_bf16 a[136:139], a[144:147], v[208:211], a[136:139]\n"
"s_cbranch_scc1 Lloop_%=\n"
"Lexit_%=:\n"
"s_cmp_eq_u32 s34, 0\n"
"s_cbranch_scc1 Lnodiag_%=\n"
"v_mfma_f32_16x16x32_bf16 v[200:203], a[0:3], v[16:19], v[220:223]\n"
"v_add_f32 v252, v240, v220\n"
"v_add_f32 v253, v241, v220\n"
"v_mfma_f32_16x16x32_bf16 v[200:203], a[4:7], v[20:23], v[200:203]\n"
"v_add_f32 v254, v242, v220\n"
"v_add_f32 v255, v243, v220\n"
"v_mfma_f32_16x16x32_bf16 v[200:203], a[8:11], v[24:27], v[200:203]\n"
"s_add_u32 s74, s71, 24576\n"
"v_add_f32 v192, v192, v240\n"
"v_mfma_f32_16x16x32_bf16 v[200:203], a[12:15], v[28:31], v[200:203]\n"
"v_add_f32 v193, v193, v241\n"
"v_add_f32 v194, v194, v242\n"
"v_mfma_f32_16x16x32_bf16 v[204:207], a[16:19], v[16:19], v[252:255]\n"
"v_add_f32 v195, v195, v243\n"
"s_mov_b32 s71, s48\n"
"v_mfma_f32_16x16x32_bf16 v[204:207], a[20:23], v[20:23], v[204:207]\n"
"v_exp_f32 v192, v192\n"
"v_mfma_f32_16x16x32_bf16 v[204:207], a[24:27], v[24:27], v[204:207]\n"
"v_exp_f32 v193, v193\n"
"v_mfma_f32_16x16x32_bf16 v[204:207], a[28:31], v[28:31], v[204:207]\n"
"v_exp_f32 v194, v194\n"
"v_mfma_f32_16x16x32_bf16 v[160:163], a[96:99], v[212:215], v[160:163]\n"
"v_exp_f32 v195, v195\n"
"v_mfma_f32_16x16x32_bf16 v[164:167], a[100:103], v[212:215], v[164:167]\n"
"buffer_load_dwordx4 a[96:99], v232, s[44:47], s74 offen offset:0\n"
"v_cvt_pk_bf16_f32 v208, v192, v193\n"
"v_mfma_f32_16x16x32_bf16 v[168:171], a[104:107], v[212:215], v[168:171]\n"
"v_cvt_pk_bf16_f32 v209, v194, v195\n"
"v_mov_b32 v210, 0\n"
"v_mfma_f32_16x16x32_bf16 v[172:175], a[108:111], v[212:215], v[172:175]\n"
"buffer_load_dwordx4 a[100:103], v232, s[44:47], s74 offen offset:1024\n"
"v_mov_b32 v211, 0\n"
"v_mfma_f32_16x16x32_bf16 v[176:179], a[112:115], v[212:215], v[176:179]\n"
"v_exp_f32 v200, v200\n"
"v_mfma_f32_16x16x32_bf16 v[180:183], a[116:119], v[212:215], v[180:183]\n"
"v_exp_f32 v201, v201\n"
"v_mfma_f32_16x16x32_bf16 v[184:187], a[120:123], v[212:215], v[184:187]\n"
"buffer_load_dwordx4 a[104:107], v232, s[44:47], s74 offen offset:2048\n"
"v_add_f32 v252, v240, v224\n"
"v_mfma_f32_16x16x32_bf16 v[188:191], a[124:127], v[212:215], v[188:191]\n"
"v_exp_f32 v202, v202\n"
"v_mfma_f32_16x16x32_bf16 a[140:143], a[144:147], v[212:215], a[140:143]\n"
"buffer_load_dwordx4 a[108:111], v232, s[44:47], s74 offen offset:3072\n"
"v_cvt_pk_bf16_f32 v212, v200, v201\n"
"v_mfma_f32_16x16x32_bf16 v[192:195], a[0:3], v[32:35], v[224:227]\n"
"v_exp_f32 v203, v203\n"
"v_mfma_f32_16x16x32_bf16 v[192:195], a[4:7], v[36:39], v[192:195]\n"
"v_cvt_pk_bf16_f32 v213, v202, v203\n"
"v_add_f32 v253, v241, v224\n"
"v_mfma_f32_16x16x32_bf16 v[192:195], a[8:11], v[40:43], v[192:195]\n"
"buffer_load_dwordx4 a[112:115], v233, s[44:47], s74 offen offset:0\n"
"v_add_f32 v254, v242, v224\n"
"v_mfma_f32_16x16x32_bf16 v[192:195], a[12:15], v[44:47], v[192:195]\n"
"v_exp_f32 v204, v204\n"
"v_mfma_f32_16x16x32_bf16 v[196:199], a[16:19], v[32:35], v[224:227]\n"
"buffer_load_dwordx4 a[116:119], v233, s[44:47], s74 offen offset:1024\n"
"v_add_f32 v255, v243, v224\n"
"v_mfma_f32_16x16x32_bf16 v[196:199], a[20:23], v[36:39], v[196:199]\n"
"v_exp_f32 v205, v205\n"
"v_mfma_f32_16x16x32_bf16 v[196:199], a[24:27], v[40:43], v[196:199]\n"
"v_exp_f32 v206, v206\n"
"v_mfma_f32_16x16x32_bf16 v[196:199], a[28:31], v[44:47], v[196:199]\n"
"buffer_load_dwordx4 a[120:123], v233, s[44:47], s74 offen offset:2048\n"
"s_waitcnt vmcnt(19)\n"
"v_mfma_f32_16x16x32_bf16 v[64:67], a[32:35], v[208:211], v[64:67]\n"
"v_exp_f32 v207, v207\n"
"v_mfma_f32_16x16x32_bf16 v[68:71], a[36:39], v[208:211], v[68:71]\n"
"buffer_load_dwordx4 a[124:127], v233, s[44:47], s74 offen offset:3072\n"
"v_cvt_pk_bf16_f32 v214, v204, v205\n"
"v_mfma_f32_16x16x32_bf16 v[72:75], a[40:43], v[208:211], v[72:75]\n"
"v_cvt_pk_bf16_f32 v215, v206, v207\n"
"s_add_u32 s75, s71, 8192\n"
"v_mfma_f32_16x16x32_bf16 v[76:79], a[44:47], v[208:211], v[76:79]\n"
"s_add_u32 s76, s71, 16384\n"
"s_waitcnt vmcnt(16)\n"
"v_mfma_f32_16x16x32_bf16 v[80:83], a[48:51], v[208:211], v[80:83]\n"
"v_exp_f32 v192, v192\n"
"v_mfma_f32_16x16x32_bf16 v[84:87], a[52:55], v[208:211], v[84:87]\n"
"v_exp_f32 v193, v193\n"
"v_mfma_f32_16x16x32_bf16 v[88:91], a[56:59], v[208:211], v[88:91]\n"
"v_exp_f32 v194, v194\n"
"v_mfma_f32_16x16x32_bf16 v[92:95], a[60:63], v[208:211], v[92:95]\n"
"v_exp_f32 v195, v195\n"
"v_mfma_f32_16x16x32_bf16 a[128:131], a[144:147], v[208:211], a[128:131]\n"
"v_cvt_pk_bf16_f32 v208, v192, v193\n"
"v_cvt_pk_bf16_f32 v209, v194, v195\n"
"v_mfma_f32_16x16x32_bf16 v[200:203], a[0:3], v[48:51], v[228:231]\n"
"v_exp_f32 v196, v196\n"
"v_mfma_f32_16x16x32_bf16 v[200:203], a[4:7], v[52:55], v[200:203]\n"
"buffer_load_dwordx4 a[0:3], v232, s[44:47], s71 offen offset:0\n"
"s_mov_b32 s34, 0\n"
"v_mfma_f32_16x16x32_bf16 v[200:203], a[8:11], v[56:59], v[200:203]\n"
"v_exp_f32 v197, v197\n"
"v_mfma_f32_16x16x32_bf16 v[200:203], a[12:15], v[60:63], v[200:203]\n"
"buffer_load_dwordx4 a[4:7], v232, s[44:47], s71 offen offset:1024\n"
"v_cvt_pk_bf16_f32 v210, v196, v197\n"
"v_mfma_f32_16x16x32_bf16 v[204:207], a[16:19], v[48:51], v[228:231]\n"
"v_exp_f32 v198, v198\n"
"v_mfma_f32_16x16x32_bf16 v[204:207], a[20:23], v[52:55], v[204:207]\n"
"buffer_load_dwordx4 a[8:11], v232, s[44:47], s71 offen offset:2048\n"
"s_mov_b32 s69, s49\n"
"v_mfma_f32_16x16x32_bf16 v[204:207], a[24:27], v[56:59], v[204:207]\n"
"v_exp_f32 v199, v199\n"
"v_mfma_f32_16x16x32_bf16 v[204:207], a[28:31], v[60:63], v[204:207]\n"
"buffer_load_dwordx4 a[12:15], v232, s[44:47], s71 offen offset:3072\n"
"v_cvt_pk_bf16_f32 v211, v198, v199\n"
"v_mfma_f32_16x16x32_bf16 v[96:99], a[32:35], v[212:215], v[96:99]\n"
"v_exp_f32 v200, v200\n"
"v_mfma_f32_16x16x32_bf16 v[100:103], a[36:39], v[212:215], v[100:103]\n"
"buffer_load_dwordx4 a[16:19], v233, s[44:47], s71 offen offset:0\n"
"s_mov_b32 s72, -1\n"
"v_mfma_f32_16x16x32_bf16 v[104:107], a[40:43], v[212:215], v[104:107]\n"
"v_exp_f32 v201, v201\n"
"v_mfma_f32_16x16x32_bf16 v[108:111], a[44:47], v[212:215], v[108:111]\n"
"buffer_load_dwordx4 a[20:23], v233, s[44:47], s71 offen offset:1024\n"
"v_mfma_f32_16x16x32_bf16 v[112:115], a[48:51], v[212:215], v[112:115]\n"
"v_exp_f32 v202, v202\n"
"v_mfma_f32_16x16x32_bf16 v[116:119], a[52:55], v[212:215], v[116:119]\n"
"buffer_load_dwordx4 a[24:27], v233, s[44:47], s71 offen offset:2048\n"
"v_mfma_f32_16x16x32_bf16 v[120:123], a[56:59], v[212:215], v[120:123]\n"
"v_exp_f32 v203, v203\n"
"v_mfma_f32_16x16x32_bf16 v[124:127], a[60:63], v[212:215], v[124:127]\n"
"buffer_load_dwordx4 a[28:31], v233, s[44:47], s71 offen offset:3072\n"
"v_mfma_f32_16x16x32_bf16 a[132:135], a[144:147], v[212:215], a[132:135]\n"
"v_cvt_pk_bf16_f32 v212, v200, v201\n"
"v_cvt_pk_bf16_f32 v213, v202, v203\n"
"s_waitcnt vmcnt(20)\n"
"v_mfma_f32_16x16x32_bf16 v[192:195], a[64:67], v[32:35], v[252:255]\n"
"v_exp_f32 v204, v204\n"
"v_mfma_f32_16x16x32_bf16 v[192:195], a[68:71], v[36:39], v[192:195]\n"
"v_exp_f32 v205, v205\n"
"v_mfma_f32_16x16x32_bf16 v[192:195], a[72:75], v[40:43], v[192:195]\n"
"v_exp_f32 v206, v206\n"
"v_mfma_f32_16x16x32_bf16 v[192:195], a[76:79], v[44:47], v[192:195]\n"
"v_exp_f32 v207, v207\n"
"v_mfma_f32_16x16x32_bf16 v[128:131], a[32:35], v[208:211], v[128:131]\n"
"v_add_f32 v252, v240, v228\n"
"v_add_f32 v253, v241, v228\n"
"v_mfma_f32_16x16x32_bf16 v[132:135], a[36:39], v[208:211], v[132:135]\n"
"v_add_f32 v254, v242, v228\n"
"v_add_f32 v255, v243, v228\n"
"v_mfma_f32_16x16x32_bf16 v[136:139], a[40:43], v[208:211], v[136:139]\n"
"v_cvt_pk_bf16_f32 v214, v204, v205\n"
"v_cvt_pk_bf16_f32 v215, v206, v207\n"
"v_mfma_f32_16x16x32_bf16 v[140:143], a[44:47], v[208:211], v[140:143]\n"
"v_exp_f32 v192, v192\n"
"v_mfma_f32_16x16x32_bf16 v[144:147], a[48:51], v[208:211], v[144:147]\n"
"v_exp_f32 v193, v193\n"
"v_mfma_f32_16x16x32_bf16 v[148:151], a[52:55], v[208:211], v[148:151]\n"
"v_exp_f32 v194, v194\n"
"v_mfma_f32_16x16x32_bf16 v[152:155], a[56:59], v[208:211], v[152:155]\n"
"v_exp_f32 v195, v195\n"
"v_mfma_f32_16x16x32_bf16 v[156:159], a[60:63], v[208:211], v[156:159]\n"
"v_mfma_f32_16x16x32_bf16 a[136:139], a[144:147], v[208:211], a[136:139]\n"
"v_cvt_pk_bf16_f32 v208, v192, v193\n"
"v_cvt_pk_bf16_f32 v209, v194, v195\n"
"v_mfma_f32_16x16x32_bf16 v[200:203], a[64:67], v[48:51], v[228:231]\n"
"v_mov_b32 v210, 0\n"
"v_mov_b32 v211, 0\n"
"v_mfma_f32_16x16x32_bf16 v[200:203], a[68:71], v[52:55], v[200:203]\n"
"v_mfma_f32_16x16x32_bf16 v[200:203], a[72:75], v[56:59], v[200:203]\n"
"v_mfma_f32_16x16x32_bf16 v[200:203], a[76:79], v[60:63], v[200:203]\n"
"s_waitcnt vmcnt(16)\n"
"v_mfma_f32_16x16x32_bf16 v[204:207], a[80:83], v[48:51], v[252:255]\n"
"v_mfma_f32_16x16x32_bf16 v[204:207], a[84:87], v[52:55], v[204:207]\n"
"v_mfma_f32_16x16x32_bf16 v[204:207], a[88:91], v[56:59], v[204:207]\n"
"v_mfma_f32_16x16x32_bf16 v[204:207], a[92:95], v[60:63], v[204:207]\n"
"v_mfma_f32_16x16x32_bf16 v[160:163], a[32:35], v[212:215], v[160:163]\n"
"v_mfma_f32_16x16x32_bf16 v[164:167], a[36:39], v[212:215], v[164:167]\n"
"buffer_load_dwordx4 a[32:35], v232, s[44:47], s75 offen offset:0\n"
"v_mfma_f32_16x16x32_bf16 v[168:171], a[40:43], v[212:215], v[168:171]\n"
"buffer_load_dwordx4 a[36:39], v232, s[44:47], s75 offen offset:1024\n"
"v_mfma_f32_16x16x32_bf16 v[172:175], a[44:47], v[212:215], v[172:175]\n"
"v_exp_f32 v200, v200\n"
"v_mfma_f32_16x16x32_bf16 v[176:179], a[48:51], v[212:215], v[176:179]\n"
"buffer_load_dwordx4 a[40:43], v232, s[44:47], s75 offen offset:2048\n"
"v_mfma_f32_16x16x32_bf16 v[180:183], a[52:55], v[212:215], v[180:183]\n"
"buffer_load_dwordx4 a[44:47], v232, s[44:47], s75 offen offset:3072\n"
"v_mfma_f32_16x16x32_bf16 v[184:187], a[56:59], v[212:215], v[184:187]\n"
"v_exp_f32 v201, v201\n"
"v_mfma_f32_16x16x32_bf16 v[188:191], a[60:63], v[212:215], v[188:191]\n"
"buffer_load_dwordx4 a[48:51], v233, s[44:47], s75 offen offset:0\n"
"v_mfma_f32_16x16x32_bf16 a[140:143], a[144:147], v[212:215], a[140:143]\n"
"buffer_load_dwordx4 a[52:55], v233, s[44:47], s75 offen offset:1024\n"
"s_waitcnt vmcnt(10)\n"
"v_mfma_f32_16x16x32_bf16 v[192:195], a[0:3], v[0:3], v[216:219]\n"
"v_exp_f32 v202, v202\n"
"v_mfma_f32_16x16x32_bf16 v[192:195], a[4:7], v[4:7], v[192:195]\n"
"buffer_load_dwordx4 a[56:59], v233, s[44:47], s75 offen offset:2048\n"
"v_cvt_pk_bf16_f32 v212, v200, v201\n"
"v_mfma_f32_16x16x32_bf16 v[192:195], a[8:11], v[8:11], v[192:195]\n"
"buffer_load_dwordx4 a[60:63], v233, s[44:47], s75 offen offset:3072\n"
"v_mfma_f32_16x16x32_bf16 v[192:195], a[12:15], v[12:15], v[192:195]\n"
"s_waitcnt vmcnt(8)\n"
"v_mfma_f32_16x16x32_bf16 v[196:199], a[16:19], v[0:3], v[216:219]\n"
"buffer_load_dwordx4 a[64:67], v232, s[44:47], s76 offen offset:0\n"
"v_mfma_f32_16x16x32_bf16 v[196:199], a[20:23], v[4:7], v[196:199]\n"
"buffer_load_dwordx4 a[68:71], v232, s[44:47], s76 offen offset:1024\n"
"v_mfma_f32_16x16x32_bf16 v[196:199], a[24:27], v[8:11], v[196:199]\n"
"v_exp_f32 v203, v203\n"
"v_mfma_f32_16x16x32_bf16 v[196:199], a[28:31], v[12:15], v[196:199]\n"
"buffer_load_dwordx4 a[72:75], v232, s[44:47], s76 offen offset:2048\n"
"v_cvt_pk_bf16_f32 v213, v202, v203\n"
"v_mfma_f32_16x16x32_bf16 v[128:131], a[96:99], v[208:211], v[128:131]\n"
"buffer_load_dwordx4 a[76:79], v232, s[44:47], s76 offen offset:3072\n"
"v_mfma_f32_16x16x32_bf16 v[132:135], a[100:103], v[208:211], v[132:135]\n"
"v_exp_f32 v204, v204\n"
"v_mfma_f32_16x16x32_bf16 v[136:139], a[104:107], v[208:211], v[136:139]\n"
"buffer_load_dwordx4 a[80:83], v233, s[44:47], s76 offen offset:0\n"
"s_cmp_eq_u32 s70, 1\n"
"v_mfma_f32_16x16x32_bf16 v[140:143], a[108:111], v[208:211], v[140:143]\n"
"buffer_load_dwordx4 a[84:87], v233, s[44:47], s76 offen offset:1024\n"
"v_mfma_f32_16x16x32_bf16 v[144:147], a[112:115], v[208:211], v[144:147]\n"
"v_exp_f32 v205, v205\n"
"v_mfma_f32_16x16x32_bf16 v[148:151], a[116:119], v[208:211], v[148:151]\n"
"buffer_load_dwordx4 a[88:91], v233, s[44:47], s76 offen offset:2048\n"
"v_cvt_pk_bf16_f32 v214, v204, v205\n"
"v_mfma_f32_16x16x32_bf16 v[152:155], a[120:123], v[208:211], v[152:155]\n"
"buffer_load_dwordx4 a[92:95], v233, s[44:47], s76 offen offset:3072\n"
"v_mfma_f32_16x16x32_bf16 v[156:159], a[124:127], v[208:211], v[156:159]\n"
"v_exp_f32 v206, v206\n"
"v_mfma_f32_16x16x32_bf16 a[136:139], a[144:147], v[208:211], a[136:139]\n"
"v_exp_f32 v207, v207\n"
"s_nop 0\n"
"v_cvt_pk_bf16_f32 v215, v206, v207\n"
"s_cbranch_scc1 Lloop_%=\n"
"Ldrain_%=:\n"
"s_nop 0\n"
"v_mfma_f32_16x16x32_bf16 v[160:163], a[96:99], v[212:215], v[160:163]\n"
"v_mfma_f32_16x16x32_bf16 v[164:167], a[100:103], v[212:215], v[164:167]\n"
"v_mfma_f32_16x16x32_bf16 v[168:171], a[104:107], v[212:215], v[168:171]\n"
"v_mfma_f32_16x16x32_bf16 v[172:175], a[108:111], v[212:215], v[172:175]\n"
"v_mfma_f32_16x16x32_bf16 v[176:179], a[112:115], v[212:215], v[176:179]\n"
"v_mfma_f32_16x16x32_bf16 v[180:183], a[116:119], v[212:215], v[180:183]\n"
"v_mfma_f32_16x16x32_bf16 v[184:187], a[120:123], v[212:215], v[184:187]\n"
"v_mfma_f32_16x16x32_bf16 v[188:191], a[124:127], v[212:215], v[188:191]\n"
"v_mfma_f32_16x16x32_bf16 a[140:143], a[144:147], v[212:215], a[140:143]\n"
"v_subrev_u32 v234, 0x8000, v234\n"
"v_subrev_u32 v235, 0x800, v235\n"
"s_lshl_b32 s78, s68, 19\n"
"s_lshl_b32 s15, s13, 12\n"
"s_add_u32 s78, s78, s15\n"
"s_add_u32 s78, s78, 37748736\n"
"s_add_u32 s79, s78, 131072\n"
"s_add_u32 s80, s78, 262144\n"
"s_add_u32 s81, s78, 393216\n"
"v_accvgpr_read_b32 v244, a128\n"
"v_accvgpr_read_b32 v246, a132\n"
"v_accvgpr_read_b32 v248, a136\n"
"v_accvgpr_read_b32 v250, a140\n"
"v_add_f32 v245, v244, v246\n"
"v_add_f32 v247, v248, v250\n"
"v_add_f32 v245, v245, v247\n"
"v_cmp_ngt_f32 vcc, 0x5f800000, v245\n"
"s_nop 0\n"
"s_cbranch_vccnz Lslow_%=\n"
"Lmid_%=:\n"
"ds_read_b128 v[0:3], v234 offset:8192\n"
"ds_read_b128 v[4:7], v234 offset:9216\n"
"ds_read_b128 v[8:11], v234 offset:10240\n"
"ds_read_b128 v[12:15], v234 offset:11264\n"
"ds_read_b128 v[16:19], v234 offset:12288\n"
"ds_read_b128 v[20:23], v234 offset:13312\n"
"ds_read_b128 v[24:27], v234 offset:14336\n"
"ds_read_b128 v[28:31], v234 offset:15360\n"
"ds_read_b128 v[32:35], v234 offset:0\n"
"ds_read_b128 v[36:39], v234 offset:1024\n"
"ds_read_b128 v[40:43], v234 offset:2048\n"
"ds_read_b128 v[44:47], v234 offset:3072\n"
"ds_read_b128 v[48:51], v234 offset:4096\n"
"ds_read_b128 v[52:55], v234 offset:5120\n"
"ds_read_b128 v[56:59], v234 offset:6144\n"
"ds_read_b128 v[60:63], v234 offset:7168\n"
"ds_read_b128 v[200:203], v235\n"
"v_rcp_f32 v244, v244\n"
"v_rcp_f32 v246, v246\n"
"v_rcp_f32 v248, v248\n"
"v_rcp_f32 v250, v250\n"
"v_mov_b32 v212, 0\n"
"v_mov_b32 v213, 0\n"
"v_mov_b32 v214, 0\n"
"v_mov_b32 v215, 0\n"
"s_nop 1\n"
"v_mfma_f32_32x32x16_bf16 a[128:143], v[212:215], v[212:215], 0\n"
"v_pk_mul_f32 v[64:65], v[64:65], v[244:245] op_sel_hi:[1,0]\n"
"v_pk_mul_f32 v[66:67], v[66:67], v[244:245] op_sel_hi:[1,0]\n"
"v_pk_mul_f32 v[68:69], v[68:69], v[244:245] op_sel_hi:[1,0]\n"
"v_pk_mul_f32 v[70:71], v[70:71], v[244:245] op_sel_hi:[1,0]\n"
"v_cvt_pk_bf16_f32 v64, v64, v65\n"
"v_cvt_pk_bf16_f32 v65, v66, v67\n"
"v_cvt_pk_bf16_f32 v66, v68, v69\n"
"v_cvt_pk_bf16_f32 v67, v70, v71\n"
"buffer_store_dwordx4 v[64:67], v232, s[40:43], s78 offen offset:0 sc1\n"
"v_pk_mul_f32 v[72:73], v[72:73], v[244:245] op_sel_hi:[1,0]\n"
"v_pk_mul_f32 v[74:75], v[74:75], v[244:245] op_sel_hi:[1,0]\n"
"v_pk_mul_f32 v[76:77], v[76:77], v[244:245] op_sel_hi:[1,0]\n"
"v_pk_mul_f32 v[78:79], v[78:79], v[244:245] op_sel_hi:[1,0]\n"
"v_cvt_pk_bf16_f32 v72, v72, v73\n"
"v_cvt_pk_bf16_f32 v73, v74, v75\n"
"v_cvt_pk_bf16_f32 v74, v76, v77\n"
"v_cvt_pk_bf16_f32 v75, v78, v79\n"
"buffer_store_dwordx4 v[72:75], v232, s[40:43], s78 offen offset:1024 sc1\n"
"v_pk_mul_f32 v[80:81], v[80:81], v[244:245] op_sel_hi:[1,0]\n"
"v_pk_mul_f32 v[82:83], v[82:83], v[244:245] op_sel_hi:[1,0]\n"
"v_pk_mul_f32 v[84:85], v[84:85], v[244:245] op_sel_hi:[1,0]\n"
"v_pk_mul_f32 v[86:87], v[86:87], v[244:245] op_sel_hi:[1,0]\n"
"v_cvt_pk_bf16_f32 v80, v80, v81\n"
"v_cvt_pk_bf16_f32 v81, v82, v83\n"
"v_cvt_pk_bf16_f32 v82, v84, v85\n"
"v_cvt_pk_bf16_f32 v83, v86, v87\n"
"buffer_store_dwordx4 v[80:83], v232, s[40:43], s78 offen offset:2048 sc1\n"
"v_pk_mul_f32 v[88:89], v[88:89], v[244:245] op_sel_hi:[1,0]\n"
"v_pk_mul_f32 v[90:91], v[90:91], v[244:245] op_sel_hi:[1,0]\n"
"v_pk_mul_f32 v[92:93], v[92:93], v[244:245] op_sel_hi:[1,0]\n"
"v_pk_mul_f32 v[94:95], v[94:95], v[244:245] op_sel_hi:[1,0]\n"
"v_cvt_pk_bf16_f32 v88, v88, v89\n"
"v_cvt_pk_bf16_f32 v89, v90, v91\n"
"v_cvt_pk_bf16_f32 v90, v92, v93\n"
"v_cvt_pk_bf16_f32 v91, v94, v95\n"
"buffer_store_dwordx4 v[88:91], v232, s[40:43], s78 offen offset:3072 sc1\n"
"v_pk_mul_f32 v[96:97], v[96:97], v[246:247] op_sel_hi:[1,0]\n"
"v_pk_mul_f32 v[98:99], v[98:99], v[246:247] op_sel_hi:[1,0]\n"
"v_pk_mul_f32 v[100:101], v[100:101], v[246:247] op_sel_hi:[1,0]\n"
"v_pk_mul_f32 v[102:103], v[102:103], v[246:247] op_sel_hi:[1,0]\n"
"v_cvt_pk_bf16_f32 v96, v96, v97\n"
"v_cvt_pk_bf16_f32 v97, v98, v99\n"
"v_cvt_pk_bf16_f32 v98, v100, v101\n"
"v_cvt_pk_bf16_f32 v99, v102, v103\n"
"buffer_store_dwordx4 v[96:99], v232, s[40:43], s79 offen offset:0 sc1\n"
"v_pk_mul_f32 v[104:105], v[104:105], v[246:247] op_sel_hi:[1,0]\n"
"v_pk_mul_f32 v[106:107], v[106:107], v[246:247] op_sel_hi:[1,0]\n"
"v_pk_mul_f32 v[108:109], v[108:109], v[246:247] op_sel_hi:[1,0]\n"
"v_pk_mul_f32 v[110:111], v[110:111], v[246:247] op_sel_hi:[1,0]\n"
"v_cvt_pk_bf16_f32 v104, v104, v105\n"
"v_cvt_pk_bf16_f32 v105, v106, v107\n"
"v_cvt_pk_bf16_f32 v106, v108, v109\n"
"v_cvt_pk_bf16_f32 v107, v110, v111\n"
"buffer_store_dwordx4 v[104:107], v232, s[40:43], s79 offen offset:1024 sc1\n"
"v_pk_mul_f32 v[112:113], v[112:113], v[246:247] op_sel_hi:[1,0]\n"
"v_pk_mul_f32 v[114:115], v[114:115], v[246:247] op_sel_hi:[1,0]\n"
"v_pk_mul_f32 v[116:117], v[116:117], v[246:247] op_sel_hi:[1,0]\n"
"v_pk_mul_f32 v[118:119], v[118:119], v[246:247] op_sel_hi:[1,0]\n"
"v_cvt_pk_bf16_f32 v112, v112, v113\n"
"v_cvt_pk_bf16_f32 v113, v114, v115\n"
"v_cvt_pk_bf16_f32 v114, v116, v117\n"
"v_cvt_pk_bf16_f32 v115, v118, v119\n"
"buffer_store_dwordx4 v[112:115], v232, s[40:43], s79 offen offset:2048 sc1\n"
"v_pk_mul_f32 v[120:121], v[120:121], v[246:247] op_sel_hi:[1,0]\n"
"v_pk_mul_f32 v[122:123], v[122:123], v[246:247] op_sel_hi:[1,0]\n"
"v_pk_mul_f32 v[124:125], v[124:125], v[246:247] op_sel_hi:[1,0]\n"
"v_pk_mul_f32 v[126:127], v[126:127], v[246:247] op_sel_hi:[1,0]\n"
"v_cvt_pk_bf16_f32 v120, v120, v121\n"
"v_cvt_pk_bf16_f32 v121, v122, v123\n"
"v_cvt_pk_bf16_f32 v122, v124, v125\n"
"v_cvt_pk_bf16_f32 v123, v126, v127\n"
"buffer_store_dwordx4 v[120:123], v232, s[40:43], s79 offen offset:3072 sc1\n"
"v_mfma_f32_32x32x16_bf16 v[64:79], v[212:215], v[212:215], 0\n"
"v_mfma_f32_32x32x16_bf16 v[80:95], v[212:215], v[212:215], 0\n"
"v_pk_mul_f32 v[128:129], v[128:129], v[248:249] op_sel_hi:[1,0]\n"
"v_pk_mul_f32 v[130:131], v[130:131], v[248:249] op_sel_hi:[1,0]\n"
"v_pk_mul_f32 v[132:133], v[132:133], v[248:249] op_sel_hi:[1,0]\n"
"v_pk_mul_f32 v[134:135], v[134:135], v[248:249] op_sel_hi:[1,0]\n"
"v_cvt_pk_bf16_f32 v128, v128, v129\n"
"v_cvt_pk_bf16_f32 v129, v130, v131\n"
"v_cvt_pk_bf16_f32 v130, v132, v133\n"
"v_cvt_pk_bf16_f32 v131, v134, v135\n"
"buffer_store_dwordx4 v[128:131], v232, s[40:43], s80 offen offset:0 sc1\n"
"v_pk_mul_f32 v[136:137], v[136:137], v[248:249] op_sel_hi:[1,0]\n"
"v_pk_mul_f32 v[138:139], v[138:139], v[248:249] op_sel_hi:[1,0]\n"
"v_pk_mul_f32 v[140:141], v[140:141], v[248:249] op_sel_hi:[1,0]\n"
"v_pk_mul_f32 v[142:143], v[142:143], v[248:249] op_sel_hi:[1,0]\n"
"v_cvt_pk_bf16_f32 v136, v136, v137\n"
"v_cvt_pk_bf16_f32 v137, v138, v139\n"
"v_cvt_pk_bf16_f32 v138, v140, v141\n"
"v_cvt_pk_bf16_f32 v139, v142, v143\n"
"buffer_store_dwordx4 v[136:139], v232, s[40:43], s80 offen offset:1024 sc1\n"
"v_pk_mul_f32 v[144:145], v[144:145], v[248:249] op_sel_hi:[1,0]\n"
"v_pk_mul_f32 v[146:147], v[146:147], v[248:249] op_sel_hi:[1,0]\n"
"v_pk_mul_f32 v[148:149], v[148:149], v[248:249] op_sel_hi:[1,0]\n"
"v_pk_mul_f32 v[150:151], v[150:151], v[248:249] op_sel_hi:[1,0]\n"
"v_cvt_pk_bf16_f32 v144, v144, v145\n"
"v_cvt_pk_bf16_f32 v145, v146, v147\n"
"v_cvt_pk_bf16_f32 v146, v148, v149\n"
"v_cvt_pk_bf16_f32 v147, v150, v151\n"
"buffer_store_dwordx4 v[144:147], v232, s[40:43], s80 offen offset:2048 sc1\n"
"v_pk_mul_f32 v[152:153], v[152:153], v[248:249] op_sel_hi:[1,0]\n"
"v_pk_mul_f32 v[154:155], v[154:155], v[248:249] op_sel_hi:[1,0]\n"
"v_pk_mul_f32 v[156:157], v[156:157], v[248:249] op_sel_hi:[1,0]\n"
"v_pk_mul_f32 v[158:159], v[158:159], v[248:249] op_sel_hi:[1,0]\n"
"v_cvt_pk_bf16_f32 v152, v152, v153\n"
"v_cvt_pk_bf16_f32 v153, v154, v155\n"
"v_cvt_pk_bf16_f32 v154, v156, v157\n"
"v_cvt_pk_bf16_f32 v155, v158, v159\n"
"buffer_store_dwordx4 v[152:155], v232, s[40:43], s80 offen offset:3072 sc1\n"
"v_mfma_f32_32x32x16_bf16 v[96:111], v[212:215], v[212:215], 0\n"
"v_mfma_f32_32x32x16_bf16 v[112:127], v[212:215], v[212:215], 0\n"
"v_pk_mul_f32 v[160:161], v[160:161], v[250:251] op_sel_hi:[1,0]\n"
"v_pk_mul_f32 v[162:163], v[162:163], v[250:251] op_sel_hi:[1,0]\n"
"v_pk_mul_f32 v[164:165], v[164:165], v[250:251] op_sel_hi:[1,0]\n"
"v_pk_mul_f32 v[166:167], v[166:167], v[250:251] op_sel_hi:[1,0]\n"
"v_cvt_pk_bf16_f32 v160, v160, v161\n"
"v_cvt_pk_bf16_f32 v161, v162, v163\n"
"v_cvt_pk_bf16_f32 v162, v164, v165\n"
"v_cvt_pk_bf16_f32 v163, v166, v167\n"
"buffer_store_dwordx4 v[160:163], v232, s[40:43], s81 offen offset:0 sc1\n"
"v_pk_mul_f32 v[168:169], v[168:169], v[250:251] op_sel_hi:[1,0]\n"
"v_pk_mul_f32 v[170:171], v[170:171], v[250:251] op_sel_hi:[1,0]\n"
"v_pk_mul_f32 v[172:173], v[172:173], v[250:251] op_sel_hi:[1,0]\n"
"v_pk_mul_f32 v[174:175], v[174:175], v[250:251] op_sel_hi:[1,0]\n"
"v_cvt_pk_bf16_f32 v168, v168, v169\n"
"v_cvt_pk_bf16_f32 v169, v170, v171\n"
"v_cvt_pk_bf16_f32 v170, v172, v173\n"
"v_cvt_pk_bf16_f32 v171, v174, v175\n"
"buffer_store_dwordx4 v[168:171], v232, s[40:43], s81 offen offset:1024 sc1\n"
"v_pk_mul_f32 v[176:177], v[176:177], v[250:251] op_sel_hi:[1,0]\n"
"v_pk_mul_f32 v[178:179], v[178:179], v[250:251] op_sel_hi:[1,0]\n"
"v_pk_mul_f32 v[180:181], v[180:181], v[250:251] op_sel_hi:[1,0]\n"
"v_pk_mul_f32 v[182:183], v[182:183], v[250:251] op_sel_hi:[1,0]\n"
"v_cvt_pk_bf16_f32 v176, v176, v177\n"
"v_cvt_pk_bf16_f32 v177, v178, v179\n"
"v_cvt_pk_bf16_f32 v178, v180, v181\n"
"v_cvt_pk_bf16_f32 v179, v182, v183\n"
"buffer_store_dwordx4 v[176:179], v232, s[40:43], s81 offen offset:2048 sc1\n"
"v_pk_mul_f32 v[184:185], v[184:185], v[250:251] op_sel_hi:[1,0]\n"
"v_pk_mul_f32 v[186:187], v[186:187], v[250:251] op_sel_hi:[1,0]\n"
"v_pk_mul_f32 v[188:189], v[188:189], v[250:251] op_sel_hi:[1,0]\n"
"v_pk_mul_f32 v[190:191], v[190:191], v[250:251] op_sel_hi:[1,0]\n"
"v_cvt_pk_bf16_f32 v184, v184, v185\n"
"v_cvt_pk_bf16_f32 v185, v186, v187\n"
"v_cvt_pk_bf16_f32 v186, v188, v189\n"
"v_cvt_pk_bf16_f32 v187, v190, v191\n"
"buffer_store_dwordx4 v[184:187], v232, s[40:43], s81 offen offset:3072 sc1\n"
"v_mfma_f32_32x32x16_bf16 v[128:143], v[212:215], v[212:215], 0\n"
"v_mfma_f32_32x32x16_bf16 v[144:159], v[212:215], v[212:215], 0\n"
"s_waitcnt lgkmcnt(0)\n"
"v_mov_b32 v216, v202\n"
"v_mov_b32 v217, v202\n"
"v_mov_b32 v218, v202\n"
"v_mov_b32 v219, v202\n"
"v_mov_b32 v220, v203\n"
"v_mov_b32 v221, v203\n"
"v_mov_b32 v222, v203\n"
"v_mov_b32 v223, v203\n"
"v_mov_b32 v224, v200\n"
"v_mov_b32 v225, v200\n"
"v_mov_b32 v226, v200\n"
"v_mov_b32 v227, v200\n"
"v_mov_b32 v228, v201\n"
"v_mov_b32 v229, v201\n"
"v_mov_b32 v230, v201\n"
"v_mov_b32 v231, v201\n"
"v_mfma_f32_32x32x16_bf16 v[160:175], v[212:215], v[212:215], 0\n"
"v_mfma_f32_32x32x16_bf16 v[176:191], v[212:215], v[212:215], 0\n"
"s_waitcnt vmcnt(36)\n"
"v_mfma_f32_16x16x32_bf16 v[192:195], a[0:3], v[0:3], v[216:219]\n"
"v_mfma_f32_16x16x32_bf16 v[192:195], a[4:7], v[4:7], v[192:195]\n"
"v_mfma_f32_16x16x32_bf16 v[192:195], a[8:11], v[8:11], v[192:195]\n"
"v_mfma_f32_16x16x32_bf16 v[192:195], a[12:15], v[12:15], v[192:195]\n"
"s_waitcnt vmcnt(32)\n"
"v_mfma_f32_16x16x32_bf16 v[196:199], a[16:19], v[0:3], v[216:219]\n"
"v_mfma_f32_16x16x32_bf16 v[196:199], a[20:23], v[4:7], v[196:199]\n"
"v_mfma_f32_16x16x32_bf16 v[196:199], a[24:27], v[8:11], v[196:199]\n"
"v_mfma_f32_16x16x32_bf16 v[196:199], a[28:31], v[12:15], v[196:199]\n"
"s_mov_b32 s70, 1\n"
"s_mov_b32 s77, -32768\n"
"s_mov_b32 s72, -1\n"
"s_sub_u32 s68, 31, s68\n"
"s_sub_u32 s69, s68, 17\n"
"s_cbranch_scc0 Lent_%=\n"
"s_and_b32 s16, s11, 1\n"
"s_lshr_b32 s17, s11, 1\n"
"s_lshl_b32 s18, s16, 14\n"
"s_mul_i32 s15, s17, 40960\n"
"s_add_u32 s18, s18, s15\n"
"s_add_u32 s18, s18, 90112\n"
"v_add_u32 v234, s18, v232\n"
"s_xor_b32 s17, s17, 1\n"
"s_lshl_b32 s18, s16, 14\n"
"s_mul_i32 s15, s17, 40960\n"
"s_add_u32 s18, s18, s15\n"
"s_add_u32 s18, s18, 90112\n"
"v_add_u32 v235, s18, v232\n"
"s_lshl_b32 s78, s68, 19\n"
"s_lshl_b32 s15, s17, 18\n"
"s_add_u32 s78, s78, s15\n"
"s_lshl_b32 s15, s13, 12\n"
"s_add_u32 s78, s78, s15\n"
"s_add_u32 s78, s78, 37748736\n"
"s_add_u32 s79, s78, 131072\n"
"s_lshl_b32 s18, s11, 10\n"
"s_add_u32 s18, s18, 126976\n"
"v_add_u32 v252, s18, v232\n"
"v_xor_b32 v253, 0x800, v252\n"
"s_lshl_b32 s76, s13, 13\n"
"s_lshl_b32 s15, s14, 10\n"
"s_add_u32 s76, s76, s15\n"
"s_lshl_b32 s15, s11, 8\n"
"s_add_u32 s76, s76, s15\n"
"s_add_u32 s76, s76, 12582912\n"
"v_mov_b32 v254, s6\n"
"v_mov_b32 v255, s7\n"
"s_waitcnt vmcnt(0)\n"
"buffer_store_dwordx2 v[254:255], v237, s[40:43], s76 offen sc1\n"
"ds_write_b128 v234, v[64:67] offset:0\n"
"ds_write_b128 v234, v[68:71] offset:1024\n"
"ds_write_b128 v234, v[72:75] offset:2048\n"
"ds_write_b128 v234, v[76:79] offset:3072\n"
"ds_write_b128 v234, v[80:83] offset:4096\n"
"ds_write_b128 v234, v[84:87] offset:5120\n"
"ds_write_b128 v234, v[88:91] offset:6144\n"
"ds_write_b128 v234, v[92:95] offset:7168\n"
"ds_write_b128 v234, v[96:99] offset:8192\n"
"ds_write_b128 v234, v[100:103] offset:9216\n"
"ds_write_b128 v234, v[104:107] offset:10240\n"
"ds_write_b128 v234, v[108:111] offset:11264\n"
"ds_write_b128 v234, v[112:115] offset:12288\n"
"ds_write_b128 v234, v[116:119] offset:13312\n"
"ds_write_b128 v234, v[120:123] offset:14336\n"
"ds_write_b128 v234, v[124:127] offset:15360\n"
"v_accvgpr_read_b32 v244, a128\n"
"v_accvgpr_read_b32 v245, a132\n"
"ds_write_b64 v252, v[244:245]\n"
"s_branch Ljoin_%=\n"
"Lb0_%=:\n"
"buffer_load_dwordx4 a[0:3], v232, s[44:47], s71 offen offset:0\n"
"buffer_load_dwordx4 a[4:7], v232, s[44:47], s71 offen offset:1024\n"
"buffer_load_dwordx4 a[8:11], v232, s[44:47], s71 offen offset:2048\n"
"buffer_load_dwordx4 a[12:15], v232, s[44:47], s71 offen offset:3072\n"
"buffer_load_dwordx4 a[16:19], v233, s[44:47], s71 offen offset:0\n"
"buffer_load_dwordx4 a[20:23], v233, s[44:47], s71 offen offset:1024\n"
"buffer_load_dwordx4 a[24:27], v233, s[44:47], s71 offen offset:2048\n"
"buffer_load_dwordx4 a[28:31], v233, s[44:47], s71 offen offset:3072\n"
"buffer_load_dwordx4 a[32:35], v232, s[44:47], s75 offen offset:0\n"
"buffer_load_dwordx4 a[36:39], v232, s[44:47], s75 offen offset:1024\n"
"buffer_load_dwordx4 a[40:43], v232, s[44:47], s75 offen offset:2048\n"
"buffer_load_dwordx4 a[44:47], v232, s[44:47], s75 offen offset:3072\n"
"buffer_load_dwordx4 a[48:51], v233, s[44:47], s75 offen offset:0\n"
"buffer_load_dwordx4 a[52:55], v233, s[44:47], s75 offen offset:1024\n"
"buffer_load_dwordx4 a[56:59], v233, s[44:47], s75 offen offset:2048\n"
"buffer_load_dwordx4 a[60:63], v233, s[44:47], s75 offen offset:3072\n"
"buffer_load_dwordx4 a[64:67], v232, s[44:47], s76 offen offset:0\n"
"buffer_load_dwordx4 a[68:71], v232, s[44:47], s76 offen offset:1024\n"
"buffer_load_dwordx4 a[72:75], v232, s[44:47], s76 offen offset:2048\n"
"buffer_load_dwordx4 a[76:79], v232, s[44:47], s76 offen offset:3072\n"
"buffer_load_dwordx4 a[80:83], v233, s[44:47], s76 offen offset:0\n"
"buffer_load_dwordx4 a[84:87], v233, s[44:47], s76 offen offset:1024\n"
"buffer_load_dwordx4 a[88:91], v233, s[44:47], s76 offen offset:2048\n"
"buffer_load_dwordx4 a[92:95], v233, s[44:47], s76 offen offset:3072\n"
"s_branch Ldrain_%=\n"
"Lnodiag_%=:\n"
"s_cmp_eq_u32 s70, 0\n"
"s_cbranch_scc1 Lb0_%=\n"
"v_mfma_f32_16x16x32_bf16 v[200:203], a[0:3], v[16:19], v[220:223]\n"
"v_exp_f32 v192, v192\n"
"v_mfma_f32_16x16x32_bf16 v[200:203], a[4:7], v[20:23], v[200:203]\n"
"s_add_u32 s74, s71, 24576\n"
"s_and_b32 s16, s11, 1\n"
"v_mfma_f32_16x16x32_bf16 v[200:203], a[8:11], v[24:27], v[200:203]\n"
"v_exp_f32 v193, v193\n"
"v_mfma_f32_16x16x32_bf16 v[200:203], a[12:15], v[28:31], v[200:203]\n"
"v_cvt_pk_bf16_f32 v208, v192, v193\n"
"s_lshr_b32 s17, s11, 1\n"
"v_mfma_f32_16x16x32_bf16 v[204:207], a[16:19], v[16:19], v[220:223]\n"
"v_exp_f32 v194, v194\n"
"v_mfma_f32_16x16x32_bf16 v[204:207], a[20:23], v[20:23], v[204:207]\n"
"s_lshl_b32 s18, s16, 14\n"
"s_mul_i32 s15, s17, 40960\n"
"v_mfma_f32_16x16x32_bf16 v[204:207], a[24:27], v[24:27], v[204:207]\n"
"v_exp_f32 v195, v195\n"
"v_mfma_f32_16x16x32_bf16 v[204:207], a[28:31], v[28:31], v[204:207]\n"
"v_cvt_pk_bf16_f32 v209, v194, v195\n"
"s_add_u32 s18, s18, s15\n"
"v_mfma_f32_16x16x32_bf16 v[160:163], a[96:99], v[212:215], v[160:163]\n"
"v_exp_f32 v196, v196\n"
"v_mfma_f32_16x16x32_bf16 v[164:167], a[100:103], v[212:215], v[164:167]\n"
"buffer_load_dwordx4 a[96:99], v232, s[44:47], s74 offen offset:0\n"
"s_add_u32 s18, s18, 90112\n"
"v_mfma_f32_16x16x32_bf16 v[168:171], a[104:107], v[212:215], v[168:171]\n"
"v_exp_f32 v197, v197\n"
"v_mfma_f32_16x16x32_bf16 v[172:175], a[108:111], v[212:215], v[172:175]\n"
"v_cvt_pk_bf16_f32 v210, v196, v197\n"
"v_add_u32 v234, s18, v232\n"
"v_mfma_f32_16x16x32_bf16 v[176:179], a[112:115], v[212:215], v[176:179]\n"
"v_exp_f32 v198, v198\n"
"v_mfma_f32_16x16x32_bf16 v[180:183], a[116:119], v[212:215], v[180:183]\n"
"buffer_load_dwordx4 a[100:103], v232, s[44:47], s74 offen offset:1024\n"
"s_xor_b32 s17, s17, 1\n"
"v_mfma_f32_16x16x32_bf16 v[184:187], a[120:123], v[212:215], v[184:187]\n"
"v_exp_f32 v199, v199\n"
"v_mfma_f32_16x16x32_bf16 v[188:191], a[124:127], v[212:215], v[188:191]\n"
"v_cvt_pk_bf16_f32 v211, v198, v199\n"
"s_lshl_b32 s18, s16, 14\n"
"v_mfma_f32_16x16x32_bf16 a[140:143], a[144:147], v[212:215], a[140:143]\n"
"buffer_load_dwordx4 a[104:107], v232, s[44:47], s74 offen offset:2048\n"
"s_mul_i32 s15, s17, 40960\n"
"v_mfma_f32_16x16x32_bf16 v[192:195], a[0:3], v[32:35], v[224:227]\n"
"v_exp_f32 v200, v200\n"
"v_mfma_f32_16x16x32_bf16 v[192:195], a[4:7], v[36:39], v[192:195]\n"
"buffer_load_dwordx4 a[108:111], v232, s[44:47], s74 offen offset:3072\n"
"s_add_u32 s18, s18, s15\n"
"v_mfma_f32_16x16x32_bf16 v[192:195], a[8:11], v[40:43], v[192:195]\n"
"v_exp_f32 v201, v201\n"
"v_mfma_f32_16x16x32_bf16 v[192:195], a[12:15], v[44:47], v[192:195]\n"
"v_cvt_pk_bf16_f32 v212, v200, v201\n"
"s_add_u32 s18, s18, 90112\n"
"v_mfma_f32_16x16x32_bf16 v[196:199], a[16:19], v[32:35], v[224:227]\n"
"v_exp_f32 v202, v202\n"
"v_mfma_f32_16x16x32_bf16 v[196:199], a[20:23], v[36:39], v[196:199]\n"
"buffer_load_dwordx4 a[112:115], v233, s[44:47], s74 offen offset:0\n"
"v_add_u32 v235, s18, v232\n"
"v_mfma_f32_16x16x32_bf16 v[196:199], a[24:27], v[40:43], v[196:199]\n"
"v_exp_f32 v203, v203\n"
"v_mfma_f32_16x16x32_bf16 v[196:199], a[28:31], v[44:47], v[196:199]\n"
"v_cvt_pk_bf16_f32 v213, v202, v203\n"
"s_lshl_b32 s78, s68, 19\n"
"s_waitcnt vmcnt(17)\n"
"v_mfma_f32_16x16x32_bf16 v[64:67], a[32:35], v[208:211], v[64:67]\n"
"v_exp_f32 v204, v204\n"
"v_mfma_f32_16x16x32_bf16 v[68:71], a[36:39], v[208:211], v[68:71]\n"
"buffer_load_dwordx4 a[116:119], v233, s[44:47], s74 offen offset:1024\n"
"s_lshl_b32 s15, s17, 18\n"
"v_mfma_f32_16x16x32_bf16 v[72:75], a[40:43], v[208:211], v[72:75]\n"
"v_exp_f32 v205, v205\n"
"v_mfma_f32_16x16x32_bf16 v[76:79], a[44:47], v[208:211], v[76:79]\n"
"v_cvt_pk_bf16_f32 v214, v204, v205\n"
"s_add_u32 s78, s78, s15\n"
"s_waitcnt vmcnt(14)\n"
"v_mfma_f32_16x16x32_bf16 v[80:83], a[48:51], v[208:211], v[80:83]\n"
"v_exp_f32 v206, v206\n"
"v_mfma_f32_16x16x32_bf16 v[84:87], a[52:55], v[208:211], v[84:87]\n"
"buffer_load_dwordx4 a[120:123], v233, s[44:47], s74 offen offset:2048\n"
"s_lshr_b32 s96, s10, 3\n"
"v_mfma_f32_16x16x32_bf16 v[88:91], a[56:59], v[208:211], v[88:91]\n"
"v_exp_f32 v207, v207\n"
"v_mfma_f32_16x16x32_bf16 v[92:95], a[60:63], v[208:211], v[92:95]\n"
"v_cvt_pk_bf16_f32 v215, v206, v207\n"
"s_lshl_b32 s96, s96, 2\n"
"v_mfma_f32_16x16x32_bf16 a[128:131], a[144:147], v[208:211], a[128:131]\n"
"buffer_load_dwordx4 a[124:127], v233, s[44:47], s74 offen offset:3072\n"
"s_add_u32 s96, s96, s11\n"
"v_mfma_f32_16x16x32_bf16 v[200:203], a[0:3], v[48:51], v[228:231]\n"
"v_exp_f32 v192, v192\n"
"v_mfma_f32_16x16x32_bf16 v[200:203], a[4:7], v[52:55], v[200:203]\n"
"s_lshl_b32 s96, s96, 7\n"
"s_add_u32 s96, s96, 1024\n"
"v_mfma_f32_16x16x32_bf16 v[200:203], a[8:11], v[56:59], v[200:203]\n"
"v_exp_f32 v193, v193\n"
"v_mfma_f32_16x16x32_bf16 v[200:203], a[12:15], v[60:63], v[200:203]\n"
"v_cvt_pk_bf16_f32 v208, v192, v193\n"
"v_lshl_add_u32 v243, v232, 10, s96\n"
"v_mfma_f32_16x16x32_bf16 v[204:207], a[16:19], v[48:51], v[228:231]\n"
"v_exp_f32 v194, v194\n"
"v_mfma_f32_16x16x32_bf16 v[204:207], a[20:23], v[52:55], v[204:207]\n"
"v_min_u32 v243, Lslow_%=-Lct2_%=, v243\n"
"s_lshl_b32 s15, s13, 12\n"
"v_mfma_f32_16x16x32_bf16 v[204:207], a[24:27], v[56:59], v[204:207]\n"
"v_exp_f32 v195, v195\n"
"v_mfma_f32_16x16x32_bf16 v[204:207], a[28:31], v[60:63], v[204:207]\n"
"v_cvt_pk_bf16_f32 v209, v194, v195\n"
"s_add_u32 s78, s78, s15\n"
"v_mfma_f32_16x16x32_bf16 v[96:99], a[32:35], v[212:215], v[96:99]\n"
"v_exp_f32 v196, v196\n"
"v_mfma_f32_16x16x32_bf16 v[100:103], a[36:39], v[212:215], v[100:103]\n"
"s_getpc_b64 s[96:97]\n"
"Lct2_%=:\n"
"s_mov_b64 exec, 1\n"
"global_load_dword a151, v243, s[96:97]\n"
"s_mov_b64 exec, -1\n"
"v_mfma_f32_16x16x32_bf16 v[104:107], a[40:43], v[212:215], v[104:107]\n"
"v_exp_f32 v197, v197\n"
"v_mfma_f32_16x16x32_bf16 v[108:111], a[44:47], v[212:215], v[108:111]\n"
"v_cvt_pk_bf16_f32 v210, v196, v197\n"
"s_add_u32 s78, s78, 37748736\n"
"v_mfma_f32_16x16x32_bf16 v[112:115], a[48:51], v[212:215], v[112:115]\n"
"v_exp_f32 v198, v198\n"
"v_mfma_f32_16x16x32_bf16 v[116:119], a[52:55], v[212:215], v[116:119]\n"
"s_add_u32 s79, s78, 131072\n"
"s_lshl_b32 s18, s11, 10\n"
"v_mfma_f32_16x16x32_bf16 v[120:123], a[56:59], v[212:215], v[120:123]\n"
"v_exp_f32 v199, v199\n"
"v_mfma_f32_16x16x32_bf16 v[124:127], a[60:63], v[212:215], v[124:127]\n"
"v_cvt_pk_bf16_f32 v211, v198, v199\n"
"s_add_u32 s18, s18, 126976\n"
"v_mfma_f32_16x16x32_bf16 a[132:135], a[144:147], v[212:215], a[132:135]\n"
"v_add_u32 v252, s18, v232\n"
"v_xor_b32 v253, 0x800, v252\n"
"s_waitcnt vmcnt(13)\n"
"v_mfma_f32_16x16x32_bf16 v[192:195], a[64:67], v[0:3], v[216:219]\n"
"v_exp_f32 v200, v200\n"
"v_mfma_f32_16x16x32_bf16 v[192:195], a[68:71], v[4:7], v[192:195]\n"
"s_lshl_b32 s76, s13, 13\n"
"s_lshl_b32 s15, s14, 10\n"
"v_mfma_f32_16x16x32_bf16 v[192:195], a[72:75], v[8:11], v[192:195]\n"
"v_exp_f32 v201, v201\n"
"v_mfma_f32_16x16x32_bf16 v[192:195], a[76:79], v[12:15], v[192:195]\n"
"v_cvt_pk_bf16_f32 v212, v200, v201\n"
"s_add_u32 s76, s76, s15\n"
"s_waitcnt vmcnt(9)\n"
"v_mfma_f32_16x16x32_bf16 v[196:199], a[80:83], v[0:3], v[216:219]\n"
"v_exp_f32 v202, v202\n"
"v_mfma_f32_16x16x32_bf16 v[196:199], a[84:87], v[4:7], v[196:199]\n"
"s_lshl_b32 s15, s11, 8\n"
"s_add_u32 s76, s76, s15\n"
"v_mfma_f32_16x16x32_bf16 v[196:199], a[88:91], v[8:11], v[196:199]\n"
"v_exp_f32 v203, v203\n"
"v_mfma_f32_16x16x32_bf16 v[196:199], a[92:95], v[12:15], v[196:199]\n"
"v_cvt_pk_bf16_f32 v213, v202, v203\n"
"s_add_u32 s76, s76, 12582912\n"
"v_mfma_f32_16x16x32_bf16 v[128:131], a[32:35], v[208:211], v[128:131]\n"
"v_exp_f32 v204, v204\n"
"v_mfma_f32_16x16x32_bf16 v[132:135], a[36:39], v[208:211], v[132:135]\n"
"v_mov_b32 v254, s6\n"
"v_mov_b32 v255, s7\n"
"v_mfma_f32_16x16x32_bf16 v[136:139], a[40:43], v[208:211], v[136:139]\n"
"v_exp_f32 v205, v205\n"
"v_mfma_f32_16x16x32_bf16 v[140:143], a[44:47], v[208:211], v[140:143]\n"
"v_cvt_pk_bf16_f32 v214, v204, v205\n"
"buffer_store_dwordx2 v[254:255], v237, s[40:43], s76 offen sc1\n"
"v_mfma_f32_16x16x32_bf16 v[144:147], a[48:51], v[208:211], v[144:147]\n"
"v_exp_f32 v206, v206\n"
"v_mfma_f32_16x16x32_bf16 v[148:151], a[52:55], v[208:211], v[148:151]\n"
"v_mfma_f32_16x16x32_bf16 v[152:155], a[56:59], v[208:211], v[152:155]\n"
"v_exp_f32 v207, v207\n"
"v_mfma_f32_16x16x32_bf16 v[156:159], a[60:63], v[208:211], v[156:159]\n"
"v_cvt_pk_bf16_f32 v215, v206, v207\n"
"v_mfma_f32_16x16x32_bf16 a[136:139], a[144:147], v[208:211], a[136:139]\n"
"v_mfma_f32_16x16x32_bf16 v[200:203], a[64:67], v[16:19], v[220:223]\n"
"v_exp_f32 v192, v192\n"
"v_mfma_f32_16x16x32_bf16 v[200:203], a[68:71], v[20:23], v[200:203]\n"
"v_mfma_f32_16x16x32_bf16 v[200:203], a[72:75], v[24:27], v[200:203]\n"
"v_exp_f32 v193, v193\n"
"v_mfma_f32_16x16x32_bf16 v[200:203], a[76:79], v[28:31], v[200:203]\n"
"v_cvt_pk_bf16_f32 v208, v192, v193\n"
"v_mfma_f32_16x16x32_bf16 v[204:207], a[80:83], v[16:19], v[220:223]\n"
"v_exp_f32 v194, v194\n"
"v_mfma_f32_16x16x32_bf16 v[204:207], a[84:87], v[20:23], v[204:207]\n"
"v_mfma_f32_16x16x32_bf16 v[204:207], a[88:91], v[24:27], v[204:207]\n"
"v_exp_f32 v195, v195\n"
"v_mfma_f32_16x16x32_bf16 v[204:207], a[92:95], v[28:31], v[204:207]\n"
"v_cvt_pk_bf16_f32 v209, v194, v195\n"
"v_mfma_f32_16x16x32_bf16 v[160:163], a[32:35], v[212:215], v[160:163]\n"
"v_exp_f32 v196, v196\n"
"v_mfma_f32_16x16x32_bf16 v[164:167], a[36:39], v[212:215], v[164:167]\n"
"v_mfma_f32_16x16x32_bf16 v[168:171], a[40:43], v[212:215], v[168:171]\n"
"v_exp_f32 v197, v197\n"
"v_mfma_f32_16x16x32_bf16 v[172:175], a[44:47], v[212:215], v[172:175]\n"
"v_cvt_pk_bf16_f32 v210, v196, v197\n"
"v_mfma_f32_16x16x32_bf16 v[176:179], a[48:51], v[212:215], v[176:179]\n"
"v_exp_f32 v198, v198\n"
"v_mfma_f32_16x16x32_bf16 v[180:183], a[52:55], v[212:215], v[180:183]\n"
"v_mfma_f32_16x16x32_bf16 v[184:187], a[56:59], v[212:215], v[184:187]\n"
"v_exp_f32 v199, v199\n"
"v_mfma_f32_16x16x32_bf16 v[188:191], a[60:63], v[212:215], v[188:191]\n"
"v_cvt_pk_bf16_f32 v211, v198, v199\n"
"v_mfma_f32_16x16x32_bf16 a[140:143], a[144:147], v[212:215], a[140:143]\n"
"v_mfma_f32_16x16x32_bf16 v[192:195], a[64:67], v[32:35], v[224:227]\n"
"v_exp_f32 v200, v200\n"
"v_mfma_f32_16x16x32_bf16 v[192:195], a[68:71], v[36:39], v[192:195]\n"
"v_mfma_f32_16x16x32_bf16 v[192:195], a[72:75], v[40:43], v[192:195]\n"
"v_exp_f32 v201, v201\n"
"v_mfma_f32_16x16x32_bf16 v[192:195], a[76:79], v[44:47], v[192:195]\n"
"v_cvt_pk_bf16_f32 v212, v200, v201\n"
"v_mfma_f32_16x16x32_bf16 v[196:199], a[80:83], v[32:35], v[224:227]\n"
"v_exp_f32 v202, v202\n"
"v_mfma_f32_16x16x32_bf16 v[196:199], a[84:87], v[36:39], v[196:199]\n"
"v_mfma_f32_16x16x32_bf16 v[196:199], a[88:91], v[40:43], v[196:199]\n"
"v_exp_f32 v203, v203\n"
"v_mfma_f32_16x16x32_bf16 v[196:199], a[92:95], v[44:47], v[196:199]\n"
"v_cvt_pk_bf16_f32 v213, v202, v203\n"
"s_waitcnt vmcnt(6)\n"
"v_mfma_f32_16x16x32_bf16 v[64:67], a[96:99], v[208:211], v[64:67]\n"
"v_exp_f32 v204, v204\n"
"v_mfma_f32_16x16x32_bf16 v[68:71], a[100:103], v[208:211], v[68:71]\n"
"v_mfma_f32_16x16x32_bf16 v[72:75], a[104:107], v[208:211], v[72:75]\n"
"v_exp_f32 v205, v205\n"
"v_mfma_f32_16x16x32_bf16 v[76:79], a[108:111], v[208:211], v[76:79]\n"
"v_cvt_pk_bf16_f32 v214, v204, v205\n"
"s_waitcnt vmcnt(2)\n"
"v_mfma_f32_16x16x32_bf16 v[80:83], a[112:115], v[208:211], v[80:83]\n"
"v_exp_f32 v206, v206\n"
"v_mfma_f32_16x16x32_bf16 v[84:87], a[116:119], v[208:211], v[84:87]\n"
"ds_write_b128 v234, v[64:67] offset:0\n"
"v_mfma_f32_16x16x32_bf16 v[88:91], a[120:123], v[208:211], v[88:91]\n"
"v_exp_f32 v207, v207\n"
"v_mfma_f32_16x16x32_bf16 v[92:95], a[124:127], v[208:211], v[92:95]\n"
"v_cvt_pk_bf16_f32 v215, v206, v207\n"
"ds_write_b128 v234, v[68:71] offset:1024\n"
"v_mfma_f32_16x16x32_bf16 a[128:131], a[144:147], v[208:211], a[128:131]\n"
"ds_write_b128 v234, v[72:75] offset:2048\n"
"v_mfma_f32_16x16x32_bf16 v[200:203], a[64:67], v[48:51], v[228:231]\n"
"v_exp_f32 v192, v192\n"
"v_mfma_f32_16x16x32_bf16 v[200:203], a[68:71], v[52:55], v[200:203]\n"
"v_exp_f32 v193, v193\n"
"v_mfma_f32_16x16x32_bf16 v[200:203], a[72:75], v[56:59], v[200:203]\n"
"v_cvt_pk_bf16_f32 v208, v192, v193\n"
"ds_write_b128 v234, v[76:79] offset:3072\n"
"v_mfma_f32_16x16x32_bf16 v[200:203], a[76:79], v[60:63], v[200:203]\n"
"v_exp_f32 v194, v194\n"
"v_mfma_f32_16x16x32_bf16 v[204:207], a[80:83], v[48:51], v[228:231]\n"
"v_exp_f32 v195, v195\n"
"v_mfma_f32_16x16x32_bf16 v[204:207], a[84:87], v[52:55], v[204:207]\n"
"v_cvt_pk_bf16_f32 v209, v194, v195\n"
"ds_write_b128 v234, v[80:83] offset:4096\n"
"v_mfma_f32_16x16x32_bf16 v[204:207], a[88:91], v[56:59], v[204:207]\n"
"v_exp_f32 v196, v196\n"
"v_mfma_f32_16x16x32_bf16 v[204:207], a[92:95], v[60:63], v[204:207]\n"
"v_exp_f32 v197, v197\n"
"v_mfma_f32_16x16x32_bf16 v[96:99], a[96:99], v[212:215], v[96:99]\n"
"v_cvt_pk_bf16_f32 v210, v196, v197\n"
"ds_write_b128 v234, v[84:87] offset:5120\n"
"v_mfma_f32_16x16x32_bf16 v[100:103], a[100:103], v[212:215], v[100:103]\n"
"v_exp_f32 v198, v198\n"
"v_mfma_f32_16x16x32_bf16 v[104:107], a[104:107], v[212:215], v[104:107]\n"
"v_exp_f32 v199, v199\n"
"v_mfma_f32_16x16x32_bf16 v[108:111], a[108:111], v[212:215], v[108:111]\n"
"v_cvt_pk_bf16_f32 v211, v198, v199\n"
"ds_write_b128 v234, v[88:91] offset:6144\n"
"v_mfma_f32_16x16x32_bf16 v[112:115], a[112:115], v[212:215], v[112:115]\n"
"v_exp_f32 v200, v200\n"
"v_mfma_f32_16x16x32_bf16 v[116:119], a[116:119], v[212:215], v[116:119]\n"
"v_exp_f32 v201, v201\n"
"v_mfma_f32_16x16x32_bf16 v[120:123], a[120:123], v[212:215], v[120:123]\n"
"ds_write_b128 v234, v[92:95] offset:7168\n"
"v_accvgpr_read_b32 v244, a128\n"
"v_mfma_f32_16x16x32_bf16 v[124:127], a[124:127], v[212:215], v[124:127]\n"
"v_exp_f32 v202, v202\n"
"v_mfma_f32_16x16x32_bf16 a[132:135], a[144:147], v[212:215], a[132:135]\n"
"v_exp_f32 v203, v203\n"
"v_cvt_pk_bf16_f32 v212, v200, v201\n"
"v_mfma_f32_16x16x32_bf16 v[128:131], a[96:99], v[208:211], v[128:131]\n"
"v_cvt_pk_bf16_f32 v213, v202, v203\n"
"ds_write_b128 v234, v[96:99] offset:8192\n"
"v_mfma_f32_16x16x32_bf16 v[132:135], a[100:103], v[208:211], v[132:135]\n"
"v_exp_f32 v204, v204\n"
"v_mfma_f32_16x16x32_bf16 v[136:139], a[104:107], v[208:211], v[136:139]\n"
"v_exp_f32 v205, v205\n"
"v_mfma_f32_16x16x32_bf16 v[140:143], a[108:111], v[208:211], v[140:143]\n"
"v_cvt_pk_bf16_f32 v214, v204, v205\n"
"ds_write_b128 v234, v[100:103] offset:9216\n"
"v_mfma_f32_16x16x32_bf16 v[144:147], a[112:115], v[208:211], v[144:147]\n"
"v_exp_f32 v206, v206\n"
"v_mfma_f32_16x16x32_bf16 v[148:151], a[116:119], v[208:211], v[148:151]\n"
"v_exp_f32 v207, v207\n"
"v_mfma_f32_16x16x32_bf16 v[152:155], a[120:123], v[208:211], v[152:155]\n"
"v_cvt_pk_bf16_f32 v215, v206, v207\n"
"ds_write_b128 v234, v[104:107] offset:10240\n"
"v_mfma_f32_16x16x32_bf16 v[156:159], a[124:127], v[208:211], v[156:159]\n"
"ds_write_b128 v234, v[108:111] offset:11264\n"
"v_accvgpr_read_b32 v245, a132\n"
"v_mfma_f32_16x16x32_bf16 a[136:139], a[144:147], v[208:211], a[136:139]\n"
"ds_write_b128 v234, v[112:115] offset:12288\n"
"v_mfma_f32_16x16x32_bf16 v[160:163], a[96:99], v[212:215], v[160:163]\n"
"ds_write_b128 v234, v[116:119] offset:13312\n"
"v_mfma_f32_16x16x32_bf16 v[164:167], a[100:103], v[212:215], v[164:167]\n"
"ds_write_b128 v234, v[120:123] offset:14336\n"
"v_mfma_f32_16x16x32_bf16 v[168:171], a[104:107], v[212:215], v[168:171]\n"
"ds_write_b128 v234, v[124:127] offset:15360\n"
"v_mfma_f32_16x16x32_bf16 v[172:175], a[108:111], v[212:215], v[172:175]\n"
"ds_write_b64 v252, v[244:245]\n"
"v_mfma_f32_16x16x32_bf16 v[176:179], a[112:115], v[212:215], v[176:179]\n"
"v_mfma_f32_16x16x32_bf16 v[180:183], a[116:119], v[212:215], v[180:183]\n"
"v_mfma_f32_16x16x32_bf16 v[184:187], a[120:123], v[212:215], v[184:187]\n"
"v_mfma_f32_16x16x32_bf16 v[188:191], a[124:127], v[212:215], v[188:191]\n"
"v_mfma_f32_16x16x32_bf16 a[140:143], a[144:147], v[212:215], a[140:143]\n"
"Ljoin_%=:\n"
"s_waitcnt lgkmcnt(0)\n"
"s_barrier\n"
"ds_read_b64 v[244:245], v253\n"
"ds_read_b128 v[64:67], v235 offset:0\n"
"ds_read_b128 v[68:71], v235 offset:1024\n"
"ds_read_b128 v[72:75], v235 offset:2048\n"
"ds_read_b128 v[76:79], v235 offset:3072\n"
"ds_read_b128 v[80:83], v235 offset:4096\n"
"ds_read_b128 v[84:87], v235 offset:5120\n"
"ds_read_b128 v[88:91], v235 offset:6144\n"
"ds_read_b128 v[92:95], v235 offset:7168\n"
"ds_read_b128 v[96:99], v235 offset:8192\n"
"ds_read_b128 v[100:103], v235 offset:9216\n"
"ds_read_b128 v[104:107], v235 offset:10240\n"
"ds_read_b128 v[108:111], v235 offset:11264\n"
"ds_read_b128 v[112:115], v235 offset:12288\n"
"ds_read_b128 v[116:119], v235 offset:13312\n"
"ds_read_b128 v[120:123], v235 offset:14336\n"
"ds_read_b128 v[124:127], v235 offset:15360\n"
"v_accvgpr_read_b32 v248, a136\n"
"v_accvgpr_read_b32 v250, a140\n"
"s_waitcnt lgkmcnt(14)\n"
"v_add_f32 v248, v248, v244\n"
"v_add_f32 v250, v250, v245\n"
"v_add_f32 v245, v248, v250\n"
"v_cmp_ngt_f32 vcc, 0x5f800000, v245\n"
"s_nop 0\n"
"s_cbranch_vccnz Lslow_%=\n"
"Lfin_%=:\n"
"v_rcp_f32 v248, v248\n"
"v_rcp_f32 v250, v250\n"
"v_pk_add_f32 v[128:129], v[128:129], v[64:65]\n"
"v_pk_add_f32 v[130:131], v[130:131], v[66:67]\n"
"v_pk_add_f32 v[132:133], v[132:133], v[68:69]\n"
"v_pk_add_f32 v[134:135], v[134:135], v[70:71]\n"
"v_pk_mul_f32 v[128:129], v[128:129], v[248:249] op_sel_hi:[1,0]\n"
"v_pk_mul_f32 v[130:131], v[130:131], v[248:249] op_sel_hi:[1,0]\n"
"v_pk_mul_f32 v[132:133], v[132:133], v[248:249] op_sel_hi:[1,0]\n"
"v_pk_mul_f32 v[134:135], v[134:135], v[248:249] op_sel_hi:[1,0]\n"
"v_cvt_pk_bf16_f32 v128, v128, v129\n"
"v_cvt_pk_bf16_f32 v129, v130, v131\n"
"v_cvt_pk_bf16_f32 v130, v132, v133\n"
"v_cvt_pk_bf16_f32 v131, v134, v135\n"
"buffer_store_dwordx4 v[128:131], v232, s[40:43], s78 offen offset:0 sc1\n"
"s_waitcnt lgkmcnt(13)\n"
"v_pk_add_f32 v[136:137], v[136:137], v[72:73]\n"
"v_pk_add_f32 v[138:139], v[138:139], v[74:75]\n"
"s_waitcnt lgkmcnt(12)\n"
"v_pk_add_f32 v[140:141], v[140:141], v[76:77]\n"
"v_pk_add_f32 v[142:143], v[142:143], v[78:79]\n"
"v_pk_mul_f32 v[136:137], v[136:137], v[248:249] op_sel_hi:[1,0]\n"
"v_pk_mul_f32 v[138:139], v[138:139], v[248:249] op_sel_hi:[1,0]\n"
"v_pk_mul_f32 v[140:141], v[140:141], v[248:249] op_sel_hi:[1,0]\n"
"v_pk_mul_f32 v[142:143], v[142:143], v[248:249] op_sel_hi:[1,0]\n"
"v_cvt_pk_bf16_f32 v136, v136, v137\n"
"v_cvt_pk_bf16_f32 v137, v138, v139\n"
"v_cvt_pk_bf16_f32 v138, v140, v141\n"
"v_cvt_pk_bf16_f32 v139, v142, v143\n"
"buffer_store_dwordx4 v[136:139], v232, s[40:43], s78 offen offset:1024 sc1\n"
"s_waitcnt lgkmcnt(11)\n"
"v_pk_add_f32 v[144:145], v[144:145], v[80:81]\n"
"v_pk_add_f32 v[146:147], v[146:147], v[82:83]\n"
"s_waitcnt lgkmcnt(10)\n"
"v_pk_add_f32 v[148:149], v[148:149], v[84:85]\n"
"v_pk_add_f32 v[150:151], v[150:151], v[86:87]\n"
"v_pk_mul_f32 v[144:145], v[144:145], v[248:249] op_sel_hi:[1,0]\n"
"v_pk_mul_f32 v[146:147], v[146:147], v[248:249] op_sel_hi:[1,0]\n"
"v_pk_mul_f32 v[148:149], v[148:149], v[248:249] op_sel_hi:[1,0]\n"
"v_pk_mul_f32 v[150:151], v[150:151], v[248:249] op_sel_hi:[1,0]\n"
"v_cvt_pk_bf16_f32 v144, v144, v145\n"
"v_cvt_pk_bf16_f32 v145, v146, v147\n"
"v_cvt_pk_bf16_f32 v146, v148, v149\n"
"v_cvt_pk_bf16_f32 v147, v150, v151\n"
"buffer_store_dwordx4 v[144:147], v232, s[40:43], s78 offen offset:2048 sc1\n"
"s_waitcnt lgkmcnt(9)\n"
"v_pk_add_f32 v[152:153], v[152:153], v[88:89]\n"
"v_pk_add_f32 v[154:155], v[154:155], v[90:91]\n"
"s_waitcnt lgkmcnt(8)\n"
"v_pk_add_f32 v[156:157], v[156:157], v[92:93]\n"
"v_pk_add_f32 v[158:159], v[158:159], v[94:95]\n"
"v_pk_mul_f32 v[152:153], v[152:153], v[248:249] op_sel_hi:[1,0]\n"
"v_pk_mul_f32 v[154:155], v[154:155], v[248:249] op_sel_hi:[1,0]\n"
"v_pk_mul_f32 v[156:157], v[156:157], v[248:249] op_sel_hi:[1,0]\n"
"v_pk_mul_f32 v[158:159], v[158:159], v[248:249] op_sel_hi:[1,0]\n"
"v_cvt_pk_bf16_f32 v152, v152, v153\n"
"v_cvt_pk_bf16_f32 v153, v154, v155\n"
"v_cvt_pk_bf16_f32 v154, v156, v157\n"
"v_cvt_pk_bf16_f32 v155, v158, v159\n"
"buffer_store_dwordx4 v[152:155], v232, s[40:43], s78 offen offset:3072 sc1\n"
"s_waitcnt lgkmcnt(7)\n"
"v_pk_add_f32 v[160:161], v[160:161], v[96:97]\n"
"v_pk_add_f32 v[162:163], v[162:163], v[98:99]\n"
"s_waitcnt lgkmcnt(6)\n"
"v_pk_add_f32 v[164:165], v[164:165], v[100:101]\n"
"v_pk_add_f32 v[166:167], v[166:167], v[102:103]\n"
"v_pk_mul_f32 v[160:161], v[160:161], v[250:251] op_sel_hi:[1,0]\n"
"v_pk_mul_f32 v[162:163], v[162:163], v[250:251] op_sel_hi:[1,0]\n"
"v_pk_mul_f32 v[164:165], v[164:165], v[250:251] op_sel_hi:[1,0]\n"
"v_pk_mul_f32 v[166:167], v[166:167], v[250:251] op_sel_hi:[1,0]\n"
"v_cvt_pk_bf16_f32 v160, v160, v161\n"
"v_cvt_pk_bf16_f32 v161, v162, v163\n"
"v_cvt_pk_bf16_f32 v162, v164, v165\n"
"v_cvt_pk_bf16_f32 v163, v166, v167\n"
"buffer_store_dwordx4 v[160:163], v232, s[40:43], s79 offen offset:0 sc1\n"
"s_waitcnt lgkmcnt(5)\n"
"v_pk_add_f32 v[168:169], v[168:169], v[104:105]\n"
"v_pk_add_f32 v[170:171], v[170:171], v[106:107]\n"
"s_waitcnt lgkmcnt(4)\n"
"v_pk_add_f32 v[172:173], v[172:173], v[108:109]\n"
"v_pk_add_f32 v[174:175], v[174:175], v[110:111]\n"
"v_pk_mul_f32 v[168:169], v[168:169], v[250:251] op_sel_hi:[1,0]\n"
"v_pk_mul_f32 v[170:171], v[170:171], v[250:251] op_sel_hi:[1,0]\n"
"v_pk_mul_f32 v[172:173], v[172:173], v[250:251] op_sel_hi:[1,0]\n"
"v_pk_mul_f32 v[174:175], v[174:175], v[250:251] op_sel_hi:[1,0]\n"
"v_cvt_pk_bf16_f32 v168, v168, v169\n"
"v_cvt_pk_bf16_f32 v169, v170, v171\n"
"v_cvt_pk_bf16_f32 v170, v172, v173\n"
"v_cvt_pk_bf16_f32 v171, v174, v175\n"
"buffer_store_dwordx4 v[168:171], v232, s[40:43], s79 offen offset:1024 sc1\n"
"s_waitcnt lgkmcnt(3)\n"
"v_pk_add_f32 v[176:177], v[176:177], v[112:113]\n"
"v_pk_add_f32 v[178:179], v[178:179], v[114:115]\n"
"s_waitcnt lgkmcnt(2)\n"
"v_pk_add_f32 v[180:181], v[180:181], v[116:117]\n"
"v_pk_add_f32 v[182:183], v[182:183], v[118:119]\n"
"v_pk_mul_f32 v[176:177], v[176:177], v[250:251] op_sel_hi:[1,0]\n"
"v_pk_mul_f32 v[178:179], v[178:179], v[250:251] op_sel_hi:[1,0]\n"
"v_pk_mul_f32 v[180:181], v[180:181], v[250:251] op_sel_hi:[1,0]\n"
"v_pk_mul_f32 v[182:183], v[182:183], v[250:251] op_sel_hi:[1,0]\n"
"v_cvt_pk_bf16_f32 v176, v176, v177\n"
"v_cvt_pk_bf16_f32 v177, v178, v179\n"
"v_cvt_pk_bf16_f32 v178, v180, v181\n"
"v_cvt_pk_bf16_f32 v179, v182, v183\n"
"buffer_store_dwordx4 v[176:179], v232, s[40:43], s79 offen offset:2048 sc1\n"
"s_waitcnt lgkmcnt(1)\n"
"v_pk_add_f32 v[184:185], v[184:185], v[120:121]\n"
"v_pk_add_f32 v[186:187], v[186:187], v[122:123]\n"
"s_waitcnt lgkmcnt(0)\n"
"v_pk_add_f32 v[188:189], v[188:189], v[124:125]\n"
"v_pk_add_f32 v[190:191], v[190:191], v[126:127]\n"
"v_pk_mul_f32 v[184:185], v[184:185], v[250:251] op_sel_hi:[1,0]\n"
"v_pk_mul_f32 v[186:187], v[186:187], v[250:251] op_sel_hi:[1,0]\n"
"v_pk_mul_f32 v[188:189], v[188:189], v[250:251] op_sel_hi:[1,0]\n"
"v_pk_mul_f32 v[190:191], v[190:191], v[250:251] op_sel_hi:[1,0]\n"
"v_cvt_pk_bf16_f32 v184, v184, v185\n"
"v_cvt_pk_bf16_f32 v185, v186, v187\n"
"v_cvt_pk_bf16_f32 v186, v188, v189\n"
"v_cvt_pk_bf16_f32 v187, v190, v191\n"
"buffer_store_dwordx4 v[184:187], v232, s[40:43], s79 offen offset:3072 sc1\n"
"v_lshrrev_b32 v233, 4, v232\n"
"s_barrier\n"
"v_mov_b32 v220, s6\n"
"v_mov_b32 v222, s6\n"
"v_mov_b32 v221, s7\n"
"v_mov_b32 v223, s7\n"
"s_lshl_b32 s34, s13, 13\n"
"s_lshl_b32 s15, s14, 10\n"
"s_add_u32 s34, s34, s15\n"
"s_lshl_b32 s15, s11, 8\n"
"s_add_u32 s34, s34, s15\n"
"s_add_u32 s34, s34, 12845056\n"
"s_sub_u32 s16, 15, s67\n"
"s_min_u32 s16, s16, s67\n"
"s_and_b32 s18, s67, 8\n"
"s_lshl_b32 s17, s11, 2\n"
"s_lshl_b32 s15, s18, 1\n"
"s_add_u32 s17, s17, s15\n"
"s_lshr_b32 s18, 0x02133322, s17\n"
"s_and_b32 s18, s18, 3\n"
"s_lshr_b32 s17, s67, 3\n"
"s_lshl_b32 s35, s66, 16\n"
"s_lshl_b32 s15, s16, 10\n"
"s_add_u32 s35, s35, s15\n"
"s_lshl_b32 s15, s18, 8\n"
"s_add_u32 s35, s35, s15\n"
"s_lshl_b32 s15, s17, 18\n"
"s_add_u32 s35, s35, s15\n"
"s_add_u32 s35, s35, 12582912\n"
"v_lshlrev_b32 v236, 9, v232\n"
"v_and_b32 v236, 24576, v236\n"
"v_lshrrev_b32 v234, 2, v233\n"
"v_lshlrev_b32 v234, 7, v234\n"
"v_and_b32 v235, 3, v233\n"
"v_lshl_add_u32 v234, v235, 3, v234\n"
"v_bfe_u32 v235, v233, 3, 2\n"
"v_lshl_add_u32 v64, v235, 5, v234\n"
"v_xor_b32 v65, 32, v64\n"
"v_xor_b32 v66, 64, v64\n"
"v_xor_b32 v67, 96, v64\n"
"v_add_u32 v68, 0x10000, v64\n"
"v_add_u32 v72, 0x20000, v64\n"
"v_add_u32 v69, 0x10000, v65\n"
"v_add_u32 v73, 0x20000, v65\n"
"v_add_u32 v70, 0x10000, v66\n"
"v_add_u32 v74, 0x20000, v66\n"
"v_add_u32 v71, 0x10000, v67\n"
"v_add_u32 v75, 0x20000, v67\n"
"s_lshl_b32 s17, s67, 20\n"
"s_lshl_b32 s15, s11, 18\n"
"s_add_u32 s17, s17, s15\n"
"s_lshl_b32 s15, s66, 15\n"
"s_add_u32 s17, s17, s15\n"
"s_add_u32 s17, s17, 37748736\n"
"v_add_u32 v76, s17, v232\n"
"v_add_u32 v77, 0x20000, v76\n"
"ds_read_b64_tr_b16 v[160:161], v64 offset:0\n"
"ds_read_b64_tr_b16 v[162:163], v64 offset:2048\n"
"ds_read_b64_tr_b16 v[164:165], v64 offset:4096\n"
"ds_read_b64_tr_b16 v[166:167], v64 offset:6144\n"
"ds_read_b64_tr_b16 v[168:169], v65 offset:0\n"
"ds_read_b64_tr_b16 v[170:171], v65 offset:2048\n"
"ds_read_b64_tr_b16 v[172:173], v65 offset:4096\n"
"ds_read_b64_tr_b16 v[174:175], v65 offset:6144\n"
"ds_read_b64_tr_b16 v[176:177], v66 offset:0\n"
"ds_read_b64_tr_b16 v[178:179], v66 offset:2048\n"
"ds_read_b64_tr_b16 v[180:181], v66 offset:4096\n"
"ds_read_b64_tr_b16 v[182:183], v66 offset:6144\n"
"ds_read_b64_tr_b16 v[184:185], v67 offset:0\n"
"ds_read_b64_tr_b16 v[186:187], v67 offset:2048\n"
"ds_read_b64_tr_b16 v[188:189], v67 offset:4096\n"
"ds_read_b64_tr_b16 v[190:191], v67 offset:6144\n"
"ds_read_b64_tr_b16 v[192:193], v64 offset:8192\n"
"ds_read_b64_tr_b16 v[194:195], v64 offset:10240\n"
"ds_read_b64_tr_b16 v[196:197], v64 offset:12288\n"
"ds_read_b64_tr_b16 v[198:199], v64 offset:14336\n"
"ds_read_b64_tr_b16 v[200:201], v65 offset:8192\n"
"ds_read_b64_tr_b16 v[202:203], v65 offset:10240\n"
"ds_read_b64_tr_b16 v[204:205], v65 offset:12288\n"
"ds_read_b64_tr_b16 v[206:207], v65 offset:14336\n"
"s_mov_b32 s48, s8\n"
"s_and_b32 s49, s9, 0xffff\n"
"s_mov_b32 s50, 3584\n"
"s_mov_b32 s51, 0x20000\n"
"s_add_u32 m0, s61, 90112\n"
"s_mov_b32 s60, 327808\n"
"buffer_load_dwordx4 v238, s[20:23], s60 offen offset:0 lds\n"
"buffer_load_dwordx4 v239, s[20:23], s60 offen offset:1024 lds\n"
"s_add_u32 m0, s61, 98304\n"
"s_mov_b32 s60, 393216\n"
"buffer_load_dwordx4 v238, s[20:23], s60 offen offset:0 lds\n"
"buffer_load_dwordx4 v239, s[20:23], s60 offen offset:1024 lds\n"
"s_waitcnt vmcnt(4)\n"
"buffer_store_dwordx2 v[220:221], v237, s[40:43], s34 offen sc1\n"
"s_add_u32 m0, s61, 106496\n"
"s_mov_b32 s60, 393344\n"
"buffer_load_dwordx4 v238, s[20:23], s60 offen offset:0 lds\n"
"buffer_load_dwordx4 v239, s[20:23], s60 offen offset:1024 lds\n"
"s_add_u32 m0, s61, 114688\n"
"s_mov_b32 s60, 458752\n"
"buffer_load_dwordx4 v238, s[20:23], s60 offen offset:0 lds\n"
"buffer_load_dwordx4 v239, s[20:23], s60 offen offset:1024 lds\n"
"s_mov_b64 exec, 0xf\n"
"v_mov_b64 v[222:223], 0\n"
"buffer_atomic_add_x2 v[222:223], v236, s[40:43], s35 offen sc0 sc1\n"
"s_mov_b64 exec, -1\n"
"s_add_u32 m0, s61, 122880\n"
"s_mov_b32 s60, 458880\n"
"buffer_load_dwordx4 v238, s[20:23], s60 offen offset:0 lds\n"
"buffer_load_dwordx4 v239, s[20:23], s60 offen offset:1024 lds\n"
"s_add_u32 m0, s61, 131072\n"
"s_mov_b32 s60, 524288\n"
"buffer_load_dwordx4 v238, s[20:23], s60 offen offset:0 lds\n"
"buffer_load_dwordx4 v239, s[20:23], s60 offen offset:1024 lds\n"
"s_add_u32 m0, s61, 139264\n"
"s_mov_b32 s60, 524416\n"
"buffer_load_dwordx4 v238, s[20:23], s60 offen offset:0 lds\n"
"buffer_load_dwordx4 v239, s[20:23], s60 offen offset:1024 lds\n"
"s_add_u32 m0, s61, 147456\n"
"s_mov_b32 s60, 589824\n"
"buffer_load_dwordx4 v238, s[20:23], s60 offen offset:0 lds\n"
"buffer_load_dwordx4 v239, s[20:23], s60 offen offset:1024 lds\n"
"s_add_u32 m0, s61, 155648\n"
"s_mov_b32 s60, 589952\n"
"buffer_load_dwordx4 v238, s[20:23], s60 offen offset:0 lds\n"
"buffer_load_dwordx4 v239, s[20:23], s60 offen offset:1024 lds\n"
"s_waitcnt vmcnt(10)\n"
"v_cmp_ne_u64 vcc, s[6:7], v[222:223]\n"
"s_nop 0\n"
"s_cbranch_vccnz Lpc_%=\n"
"Lpc_ok_%=:\n"
"s_mov_b32 s59, 0\n"
"buffer_load_dwordx4 a[0:3], v76, s[40:43], s59 offen offset:0 sc1\n"
"buffer_load_dwordx4 a[4:7], v76, s[40:43], s59 offen offset:1024 sc1\n"
"buffer_load_dwordx4 a[8:11], v77, s[40:43], s59 offen offset:0 sc1\n"
"buffer_load_dwordx4 a[12:15], v77, s[40:43], s59 offen offset:1024 sc1\n"
"buffer_load_dwordx4 a[16:19], v76, s[40:43], s59 offen offset:2048 sc1\n"
"buffer_load_dwordx4 a[20:23], v76, s[40:43], s59 offen offset:3072 sc1\n"
"buffer_load_dwordx4 a[24:27], v77, s[40:43], s59 offen offset:2048 sc1\n"
"buffer_load_dwordx4 a[28:31], v77, s[40:43], s59 offen offset:3072 sc1\n"
"s_mov_b32 s59, 4096\n"
"buffer_load_dwordx4 a[32:35], v76, s[40:43], s59 offen offset:0 sc1\n"
"buffer_load_dwordx4 a[36:39], v76, s[40:43], s59 offen offset:1024 sc1\n"
"buffer_load_dwordx4 a[40:43], v77, s[40:43], s59 offen offset:0 sc1\n"
"s_waitcnt vmcnt(7) lgkmcnt(14)\n"
"v_mfma_f32_16x16x32_bf16 v[0:3], v[160:163], a[0:3], 0\n"
"s_add_u32 s35, s35, 32768\n"
"v_lshrrev_b32 v234, 3, v233\n"
"v_mfma_f32_16x16x32_bf16 v[0:3], v[164:167], a[4:7], v[0:3]\n"
"v_and_b32 v235, 0x70, v238\n"
"v_lshl_add_u32 v228, v234, 10, v235\n"
"v_mfma_f32_16x16x32_bf16 v[4:7], v[160:163], a[8:11], 0\n"
"ds_read_b64_tr_b16 v[160:161], v66 offset:8192\n"
"s_lshl_b32 s16, s63, 7\n"
"v_mfma_f32_16x16x32_bf16 v[4:7], v[164:167], a[12:15], v[4:7]\n"
"ds_read_b64_tr_b16 v[162:163], v66 offset:10240\n"
"v_mfma_f32_16x16x32_bf16 v[8:11], v[168:171], a[0:3], 0\n"
"ds_read_b64_tr_b16 v[164:165], v66 offset:12288\n"
"v_add_u32 v228, s16, v228\n"
"v_mfma_f32_16x16x32_bf16 v[8:11], v[172:175], a[4:7], v[8:11]\n"
"buffer_load_dwordx4 a[44:47], v77, s[40:43], s59 offen offset:1024 sc1\n"
"ds_read_b64_tr_b16 v[166:167], v66 offset:14336\n"
"v_mfma_f32_16x16x32_bf16 v[12:15], v[168:171], a[8:11], 0\n"
"ds_read_b64_tr_b16 v[168:169], v67 offset:8192\n"
"s_mul_i32 s78, s64, 96\n"
"v_mfma_f32_16x16x32_bf16 v[12:15], v[172:175], a[12:15], v[12:15]\n"
"ds_read_b64_tr_b16 v[170:171], v67 offset:10240\n"
"s_waitcnt lgkmcnt(14)\n"
"v_mfma_f32_16x16x32_bf16 v[16:19], v[176:179], a[0:3], 0\n"
"ds_read_b64_tr_b16 v[172:173], v67 offset:12288\n"
"s_add_u32 s78, s78, 2048\n"
"v_mfma_f32_16x16x32_bf16 v[16:19], v[180:183], a[4:7], v[16:19]\n"
"ds_read_b64_tr_b16 v[174:175], v67 offset:14336\n"
"v_mul_u32_u24 v234, 3584, v234\n"
"v_mfma_f32_16x16x32_bf16 v[20:23], v[176:179], a[8:11], 0\n"
"ds_read_b64_tr_b16 v[176:177], v64 offset:16384\n"
"s_mul_i32 s16, s11, 57344\n"
"v_mfma_f32_16x16x32_bf16 v[20:23], v[180:183], a[12:15], v[20:23]\n"
"ds_read_b64_tr_b16 v[178:179], v64 offset:18432\n"
"v_mfma_f32_16x16x32_bf16 v[24:27], v[184:187], a[0:3], 0\n"
"buffer_load_dwordx4 a[48:51], v76, s[40:43], s59 offen offset:2048 sc1\n"
"ds_read_b64_tr_b16 v[180:181], v64 offset:20480\n"
"v_mfma_f32_16x16x32_bf16 v[24:27], v[188:191], a[4:7], v[24:27]\n"
"ds_read_b64_tr_b16 v[182:183], v64 offset:22528\n"
"v_add_u32 v234, s16, v234\n"
"v_mfma_f32_16x16x32_bf16 v[28:31], v[184:187], a[8:11], 0\n"
"ds_read_b64_tr_b16 v[184:185], v65 offset:16384\n"
"s_lshl_b32 s16, s64, 7\n"
"v_mfma_f32_16x16x32_bf16 v[28:31], v[188:191], a[12:15], v[28:31]\n"
"ds_read_b64_tr_b16 v[186:187], v65 offset:18432\n"
"s_waitcnt lgkmcnt(14)\n"
"v_mfma_f32_16x16x32_bf16 v[32:35], v[192:195], a[0:3], 0\n"
"ds_read_b64_tr_b16 v[188:189], v65 offset:20480\n"
"v_add3_u32 v78, v234, v235, s16\n"
"v_mfma_f32_16x16x32_bf16 v[32:35], v[196:199], a[4:7], v[32:35]\n"
"ds_read_b64_tr_b16 v[190:191], v65 offset:22528\n"
"v_add_u32 v80, 32, v235\n"
"v_mfma_f32_16x16x32_bf16 v[36:39], v[192:195], a[8:11], 0\n"
"ds_read_b64_tr_b16 v[192:193], v66 offset:16384\n"
"v_and_b32 v80, 128, v80\n"
"v_mfma_f32_16x16x32_bf16 v[36:39], v[196:199], a[12:15], v[36:39]\n"
"buffer_load_dwordx4 a[52:55], v76, s[40:43], s59 offen offset:3072 sc1\n"
"v_mfma_f32_16x16x32_bf16 v[40:43], v[200:203], a[0:3], 0\n"
"ds_read_b64_tr_b16 v[194:195], v66 offset:18432\n"
"v_lshl_add_u32 v80, v80, 20, v235\n"
"v_mfma_f32_16x16x32_bf16 v[40:43], v[204:207], a[4:7], v[40:43]\n"
"ds_read_b64_tr_b16 v[196:197], v66 offset:20480\n"
"v_add3_u32 v80, v80, v234, s78\n"
"v_mfma_f32_16x16x32_bf16 v[44:47], v[200:203], a[8:11], 0\n"
"ds_read_b64_tr_b16 v[198:199], v66 offset:22528\n"
"v_lshrrev_b32 v234, 4, v233\n"
"v_mfma_f32_16x16x32_bf16 v[44:47], v[204:207], a[12:15], v[44:47]\n"
"ds_read_b64_tr_b16 v[200:201], v67 offset:16384\n"
"s_waitcnt lgkmcnt(14)\n"
"v_mfma_f32_16x16x32_bf16 v[48:51], v[160:163], a[0:3], 0\n"
"ds_read_b64_tr_b16 v[202:203], v67 offset:18432\n"
"v_bfe_u32 v235, v233, 3, 1\n"
"v_mfma_f32_16x16x32_bf16 v[48:51], v[164:167], a[4:7], v[48:51]\n"
"ds_read_b64_tr_b16 v[204:205], v67 offset:20480\n"
"v_and_b32 v85, 7, v233\n"
"v_mfma_f32_16x16x32_bf16 v[52:55], v[160:163], a[8:11], 0\n"
"buffer_load_dwordx4 a[56:59], v77, s[40:43], s59 offen offset:2048 sc1\n"
"ds_read_b64_tr_b16 v[206:207], v67 offset:22528\n"
"v_mfma_f32_16x16x32_bf16 v[52:55], v[164:167], a[12:15], v[52:55]\n"
"ds_read_b64_tr_b16 v[160:161], v64 offset:24576\n"
"s_waitcnt lgkmcnt(14)\n"
"v_mfma_f32_16x16x32_bf16 v[56:59], v[168:171], a[0:3], 0\n"
"ds_read_b64_tr_b16 v[162:163], v64 offset:26624\n"
"v_lshlrev_b32 v86, 3, v234\n"
"v_mfma_f32_16x16x32_bf16 v[56:59], v[172:175], a[4:7], v[56:59]\n"
"ds_read_b64_tr_b16 v[164:165], v64 offset:28672\n"
"v_lshl_add_u32 v84, v235, 6, v86\n"
"v_mfma_f32_16x16x32_bf16 v[60:63], v[168:171], a[8:11], 0\n"
"ds_read_b64_tr_b16 v[166:167], v64 offset:30720\n"
"v_lshl_add_u32 v83, v85, 10, v84\n"
"v_mfma_f32_16x16x32_bf16 v[60:63], v[172:175], a[12:15], v[60:63]\n"
"ds_read_b64_tr_b16 v[168:169], v65 offset:24576\n"
"s_waitcnt vmcnt(7) lgkmcnt(14)\n"
"v_mfma_f32_16x16x32_bf16 v[0:3], v[176:179], a[16:19], v[0:3]\n"
"ds_read_b64_tr_b16 v[170:171], v65 offset:26624\n"
"v_and_b32 v88, 8, v86\n"
"v_mfma_f32_16x16x32_bf16 v[0:3], v[180:183], a[20:23], v[0:3]\n"
"buffer_load_dwordx4 a[60:63], v77, s[40:43], s59 offen offset:3072 sc1\n"
"s_mov_b32 s59, 8192\n"
"v_mfma_f32_16x16x32_bf16 v[4:7], v[176:179], a[24:27], v[4:7]\n"
"ds_read_b64_tr_b16 v[172:173], v65 offset:28672\n"
"v_mad_u32_u24 v83, v88, 3, v83\n"
"v_mfma_f32_16x16x32_bf16 v[4:7], v[180:183], a[28:31], v[4:7]\n"
"ds_read_b64_tr_b16 v[174:175], v65 offset:30720\n"
"s_waitcnt lgkmcnt(14)\n"
"v_mfma_f32_16x16x32_bf16 v[8:11], v[184:187], a[16:19], v[8:11]\n"
"ds_read_b64_tr_b16 v[176:177], v66 offset:24576\n"
"s_lshl_b32 s68, s65, 17\n"
"v_mfma_f32_16x16x32_bf16 v[8:11], v[188:191], a[20:23], v[8:11]\n"
"ds_read_b64_tr_b16 v[178:179], v66 offset:26624\n"
"s_lshl_b32 s15, s64, 13\n"
"v_mfma_f32_16x16x32_bf16 v[12:15], v[184:187], a[24:27], v[12:15]\n"
"ds_read_b64_tr_b16 v[180:181], v66 offset:28672\n"
"s_add_u32 s68, s68, s15\n"
"v_mfma_f32_16x16x32_bf16 v[12:15], v[188:191], a[28:31], v[12:15]\n"
"ds_read_b64_tr_b16 v[182:183], v66 offset:30720\n"
"s_waitcnt lgkmcnt(14)\n"
"v_mfma_f32_16x16x32_bf16 v[16:19], v[192:195], a[16:19], v[16:19]\n"
"buffer_load_dwordx4 a[64:67], v76, s[40:43], s59 offen offset:0 sc1\n"
"ds_read_b64_tr_b16 v[184:185], v67 offset:24576\n"
"v_mfma_f32_16x16x32_bf16 v[16:19], v[196:199], a[20:23], v[16:19]\n"
"ds_read_b64_tr_b16 v[186:187], v67 offset:26624\n"
"s_lshl_b32 s15, s11, 8\n"
"v_mfma_f32_16x16x32_bf16 v[20:23], v[192:195], a[24:27], v[20:23]\n"
"ds_read_b64_tr_b16 v[188:189], v67 offset:28672\n"
"s_add_u32 s68, s68, s15\n"
"v_mfma_f32_16x16x32_bf16 v[20:23], v[196:199], a[28:31], v[20:23]\n"
"ds_read_b64_tr_b16 v[190:191], v67 offset:30720\n"
"s_waitcnt lgkmcnt(14)\n"
"v_mfma_f32_16x16x32_bf16 v[24:27], v[200:203], a[16:19], v[24:27]\n"
"ds_read_b64_tr_b16 v[192:193], v64 offset:32768\n"
"v_add_u32 v229, 7168, v228\n"
"v_mfma_f32_16x16x32_bf16 v[24:27], v[204:207], a[20:23], v[24:27]\n"
"ds_read_b64_tr_b16 v[194:195], v64 offset:34816\n"
"v_add_u32 v230, 14336, v228\n"
"v_mfma_f32_16x16x32_bf16 v[28:31], v[200:203], a[24:27], v[28:31]\n"
"ds_read_b64_tr_b16 v[196:197], v64 offset:36864\n"
"v_add_u32 v231, 21504, v228\n"
"v_mfma_f32_16x16x32_bf16 v[28:31], v[204:207], a[28:31], v[28:31]\n"
"buffer_load_dwordx4 a[68:71], v76, s[40:43], s59 offen offset:1024 sc1\n"
"s_waitcnt lgkmcnt(14)\n"
"v_mfma_f32_16x16x32_bf16 v[32:35], v[160:163], a[16:19], v[32:35]\n"
"ds_read_b64_tr_b16 v[198:199], v64 offset:38912\n"
"v_add_u32 v224, s58, v64\n"
"v_mfma_f32_16x16x32_bf16 v[32:35], v[164:167], a[20:23], v[32:35]\n"
"ds_read_b64_tr_b16 v[200:201], v65 offset:32768\n"
"v_add_u32 v225, s58, v65\n"
"v_mfma_f32_16x16x32_bf16 v[36:39], v[160:163], a[24:27], v[36:39]\n"
"ds_read_b64_tr_b16 v[202:203], v65 offset:34816\n"
"v_add_u32 v226, s58, v66\n"
"v_mfma_f32_16x16x32_bf16 v[36:39], v[164:167], a[28:31], v[36:39]\n"
"ds_read_b64_tr_b16 v[204:205], v65 offset:36864\n"
"s_waitcnt lgkmcnt(14)\n"
"v_mfma_f32_16x16x32_bf16 v[40:43], v[168:171], a[16:19], v[40:43]\n"
"ds_read_b64_tr_b16 v[206:207], v65 offset:38912\n"
"v_add_u32 v227, s58, v67\n"
"v_mfma_f32_16x16x32_bf16 v[40:43], v[172:175], a[20:23], v[40:43]\n"
"ds_read_b64_tr_b16 v[160:161], v66 offset:32768\n"
"v_add_u32 v79, 27648, v78\n"
"v_mfma_f32_16x16x32_bf16 v[44:47], v[168:171], a[24:27], v[44:47]\n"
"buffer_load_dwordx4 a[72:75], v77, s[40:43], s59 offen offset:0 sc1\n"
"ds_read_b64_tr_b16 v[162:163], v66 offset:34816\n"
"v_mfma_f32_16x16x32_bf16 v[44:47], v[172:175], a[28:31], v[44:47]\n"
"ds_read_b64_tr_b16 v[164:165], v66 offset:36864\n"
"s_waitcnt lgkmcnt(14)\n"
"v_mfma_f32_16x16x32_bf16 v[48:51], v[176:179], a[16:19], v[48:51]\n"
"ds_read_b64_tr_b16 v[166:167], v66 offset:38912\n"
"v_add_u32 v81, 27648, v80\n"
"v_mfma_f32_16x16x32_bf16 v[48:51], v[180:183], a[20:23], v[48:51]\n"
"ds_read_b64_tr_b16 v[168:169], v67 offset:32768\n"
"v_lshlrev_b32 v82, 8, v234\n"
"v_mfma_f32_16x16x32_bf16 v[52:55], v[176:179], a[24:27], v[52:55]\n"
"ds_read_b64_tr_b16 v[170:171], v67 offset:34816\n"
"v_lshl_add_u32 v82, v235, 10, v82\n"
"v_mfma_f32_16x16x32_bf16 v[52:55], v[180:183], a[28:31], v[52:55]\n"
"ds_read_b64_tr_b16 v[172:173], v67 offset:36864\n"
"s_waitcnt lgkmcnt(14)\n"
"v_mfma_f32_16x16x32_bf16 v[56:59], v[184:187], a[16:19], v[56:59]\n"
"ds_read_b64_tr_b16 v[174:175], v67 offset:38912\n"
"buffer_load_dwordx4 v[88:91], v83, s[52:55], s68 offen offset:0\n"
"v_mfma_f32_16x16x32_bf16 v[56:59], v[188:191], a[20:23], v[56:59]\n"
"buffer_load_dwordx4 a[76:79], v77, s[40:43], s59 offen offset:1024 sc1\n"
"ds_read_b64_tr_b16 v[176:177], v64 offset:40960\n"
"v_mfma_f32_16x16x32_bf16 v[60:63], v[184:187], a[24:27], v[60:63]\n"
"ds_read_b64_tr_b16 v[178:179], v64 offset:43008\n"
"v_lshl_add_u32 v82, v85, 4, v82\n"
"v_mfma_f32_16x16x32_bf16 v[60:63], v[188:191], a[28:31], v[60:63]\n"
"ds_read_b64_tr_b16 v[180:181], v64 offset:45056\n"
"s_waitcnt vmcnt(9) lgkmcnt(14)\n"
"v_mfma_f32_16x16x32_bf16 v[0:3], v[192:195], a[32:35], v[0:3]\n"
"ds_read_b64_tr_b16 v[182:183], v64 offset:47104\n"
"s_and_b32 s16, s64, 1\n"
"v_mfma_f32_16x16x32_bf16 v[0:3], v[196:199], a[36:39], v[0:3]\n"
"ds_read_b64_tr_b16 v[184:185], v65 offset:40960\n"
"s_lshl_b32 s16, s16, 7\n"
"v_mfma_f32_16x16x32_bf16 v[4:7], v[192:195], a[40:43], v[4:7]\n"
"ds_read_b64_tr_b16 v[186:187], v65 offset:43008\n"
"v_add_u32 v82, s16, v82\n"
"v_mfma_f32_16x16x32_bf16 v[4:7], v[196:199], a[44:47], v[4:7]\n"
"ds_read_b64_tr_b16 v[188:189], v65 offset:45056\n"
"s_waitcnt lgkmcnt(14)\n"
"v_mfma_f32_16x16x32_bf16 v[8:11], v[200:203], a[32:35], v[8:11]\n"
"buffer_load_dwordx4 a[80:83], v76, s[40:43], s59 offen offset:2048 sc1\n"
"ds_read_b64_tr_b16 v[190:191], v65 offset:47104\n"
"v_mfma_f32_16x16x32_bf16 v[8:11], v[204:207], a[36:39], v[8:11]\n"
"ds_read_b64_tr_b16 v[192:193], v66 offset:40960\n"
"v_lshlrev_b32 v84, 1, v84\n"
"v_mfma_f32_16x16x32_bf16 v[12:15], v[200:203], a[40:43], v[12:15]\n"
"ds_read_b64_tr_b16 v[194:195], v66 offset:43008\n"
"s_lshl_b32 s69, s66, 16\n"
"v_mfma_f32_16x16x32_bf16 v[12:15], v[204:207], a[44:47], v[12:15]\n"
"ds_read_b64_tr_b16 v[196:197], v66 offset:45056\n"
"s_waitcnt lgkmcnt(14)\n"
"v_mfma_f32_16x16x32_bf16 v[16:19], v[160:163], a[32:35], v[16:19]\n"
"ds_read_b64_tr_b16 v[198:199], v66 offset:47104\n"
"s_lshl_b32 s15, s11, 9\n"
"v_mfma_f32_16x16x32_bf16 v[16:19], v[164:167], a[36:39], v[16:19]\n"
"ds_read_b64_tr_b16 v[200:201], v67 offset:40960\n"
"s_add_u32 s69, s69, s15\n"
"v_mfma_f32_16x16x32_bf16 v[20:23], v[160:163], a[40:43], v[20:23]\n"
"ds_read_b64_tr_b16 v[202:203], v67 offset:43008\n"
"s_add_u32 s69, s69, 524288\n"
"v_mfma_f32_16x16x32_bf16 v[20:23], v[164:167], a[44:47], v[20:23]\n"
"buffer_load_dwordx4 a[84:87], v76, s[40:43], s59 offen offset:3072 sc1\n"
"s_waitcnt lgkmcnt(14)\n"
"v_mfma_f32_16x16x32_bf16 v[24:27], v[168:171], a[32:35], v[24:27]\n"
"ds_read_b64_tr_b16 v[204:205], v67 offset:45056\n"
"s_add_u32 s70, s69, 4096\n"
"v_mfma_f32_16x16x32_bf16 v[24:27], v[172:175], a[36:39], v[24:27]\n"
"ds_read_b64_tr_b16 v[206:207], v67 offset:47104\n"
"buffer_load_dwordx4 v[96:99], v84, s[40:43], s69 offen offset:0 sc1\n"
"v_mfma_f32_16x16x32_bf16 v[28:31], v[168:171], a[40:43], v[28:31]\n"
"ds_read_b64_tr_b16 v[160:161], v64 offset:49152\n"
"s_lshl_b32 s79, s64, 7\n"
"v_mfma_f32_16x16x32_bf16 v[28:31], v[172:175], a[44:47], v[28:31]\n"
"ds_read_b64_tr_b16 v[162:163], v64 offset:51200\n"
"s_waitcnt lgkmcnt(14)\n"
"v_mfma_f32_16x16x32_bf16 v[32:35], v[176:179], a[32:35], v[32:35]\n"
"ds_read_b64_tr_b16 v[164:165], v64 offset:53248\n"
"v_lshlrev_b32 v85, 3, v85\n"
"v_mfma_f32_16x16x32_bf16 v[32:35], v[180:183], a[36:39], v[32:35]\n"
"ds_read_b64_tr_b16 v[166:167], v64 offset:55296\n"
"v_add_u32 v85, 32768, v85\n"
"v_mfma_f32_16x16x32_bf16 v[36:39], v[176:179], a[40:43], v[36:39]\n"
"buffer_load_dwordx4 a[88:91], v77, s[40:43], s59 offen offset:2048 sc1\n"
"ds_read_b64_tr_b16 v[168:169], v65 offset:49152\n"
"v_mfma_f32_16x16x32_bf16 v[36:39], v[180:183], a[44:47], v[36:39]\n"
"ds_read_b64_tr_b16 v[170:171], v65 offset:51200\n"
"s_waitcnt lgkmcnt(14)\n"
"v_mfma_f32_16x16x32_bf16 v[40:43], v[184:187], a[32:35], v[40:43]\n"
"ds_read_b64_tr_b16 v[172:173], v65 offset:53248\n"
"s_and_b32 s16, s64, 3\n"
"v_mfma_f32_16x16x32_bf16 v[40:43], v[188:191], a[36:39], v[40:43]\n"
"ds_read_b64_tr_b16 v[174:175], v65 offset:55296\n"
"s_lshl_b32 s34, s67, 14\n"
"v_mfma_f32_16x16x32_bf16 v[44:47], v[184:187], a[40:43], v[44:47]\n"
"ds_read_b64_tr_b16 v[176:177], v66 offset:49152\n"
"s_lshl_b32 s15, s16, 12\n"
"v_mfma_f32_16x16x32_bf16 v[44:47], v[188:191], a[44:47], v[44:47]\n"
"ds_read_b64_tr_b16 v[178:179], v66 offset:51200\n"
"s_waitcnt lgkmcnt(14)\n"
"v_mfma_f32_16x16x32_bf16 v[48:51], v[192:195], a[32:35], v[48:51]\n"
"ds_read_b64_tr_b16 v[180:181], v66 offset:53248\n"
"s_add_u32 s34, s34, s15\n"
"v_mfma_f32_16x16x32_bf16 v[48:51], v[196:199], a[36:39], v[48:51]\n"
"buffer_load_dwordx4 a[92:95], v77, s[40:43], s59 offen offset:3072 sc1\n"
"ds_read_b64_tr_b16 v[182:183], v66 offset:55296\n"
"v_mfma_f32_16x16x32_bf16 v[52:55], v[192:195], a[40:43], v[52:55]\n"
"s_mov_b32 s59, 12288\n"
"ds_read_b64_tr_b16 v[184:185], v67 offset:49152\n"
"v_mfma_f32_16x16x32_bf16 v[52:55], v[196:199], a[44:47], v[52:55]\n"
"ds_read_b64_tr_b16 v[186:187], v67 offset:51200\n"
"s_waitcnt lgkmcnt(14)\n"
"v_mfma_f32_16x16x32_bf16 v[56:59], v[200:203], a[32:35], v[56:59]\n"
"ds_read_b64_tr_b16 v[188:189], v67 offset:53248\n"
"buffer_load_dwordx4 v[128:131], v84, s[40:43], s69 offen offset:2048 sc1\n"
"v_mfma_f32_16x16x32_bf16 v[56:59], v[204:207], a[36:39], v[56:59]\n"
"ds_read_b64_tr_b16 v[190:191], v67 offset:55296\n"
"s_lshl_b32 s15, s66, 10\n"
"v_mfma_f32_16x16x32_bf16 v[60:63], v[200:203], a[40:43], v[60:63]\n"
"ds_read_b64_tr_b16 v[192:193], v64 offset:57344\n"
"s_add_u32 s34, s34, s15\n"
"v_mfma_f32_16x16x32_bf16 v[60:63], v[204:207], a[44:47], v[60:63]\n"
"ds_read_b64_tr_b16 v[194:195], v64 offset:59392\n"
"s_waitcnt vmcnt(11) lgkmcnt(14)\n"
"v_mfma_f32_16x16x32_bf16 v[0:3], v[160:163], a[48:51], v[0:3]\n"
"s_barrier\n"
"buffer_load_dwordx4 a[96:99], v76, s[40:43], s59 offen offset:0 sc1\n"
"v_mfma_f32_16x16x32_bf16 v[0:3], v[164:167], a[52:55], v[0:3]\n"
"ds_read_b64_tr_b16 v[196:197], v64 offset:61440\n"
"s_lshl_b32 s15, s11, 8\n"
"v_mfma_f32_16x16x32_bf16 v[4:7], v[160:163], a[56:59], v[4:7]\n"
"ds_read_b64_tr_b16 v[198:199], v64 offset:63488\n"
"s_add_u32 m0, s61, 0\n"
"v_mfma_f32_16x16x32_bf16 v[4:7], v[164:167], a[60:63], v[4:7]\n"
"ds_read_b64_tr_b16 v[200:201], v65 offset:57344\n"
"s_waitcnt lgkmcnt(13)\n"
"v_mfma_f32_16x16x32_bf16 v[8:11], v[168:171], a[48:51], v[8:11]\n"
"ds_read_b64_tr_b16 v[202:203], v65 offset:59392\n"
"s_mov_b32 s60, 655360\n"
"v_mfma_f32_16x16x32_bf16 v[8:11], v[172:175], a[52:55], v[8:11]\n"
"ds_read_b64_tr_b16 v[204:205], v65 offset:61440\n"
"buffer_load_dwordx4 v238, s[20:23], s60 offen offset:0 lds\n"
"v_mfma_f32_16x16x32_bf16 v[12:15], v[168:171], a[56:59], v[12:15]\n"
"ds_read_b64_tr_b16 v[206:207], v65 offset:63488\n"
"s_add_u32 s34, s34, s15\n"
"v_mfma_f32_16x16x32_bf16 v[12:15], v[172:175], a[60:63], v[12:15]\n"
"buffer_load_dwordx4 a[100:103], v76, s[40:43], s59 offen offset:1024 sc1\n"
"s_waitcnt lgkmcnt(12)\n"
"v_mfma_f32_16x16x32_bf16 v[16:19], v[176:179], a[48:51], v[16:19]\n"
"ds_read_b64_tr_b16 v[160:161], v66 offset:57344\n"
"s_add_u32 s34, s34, 13107200\n"
"v_mfma_f32_16x16x32_bf16 v[16:19], v[180:183], a[52:55], v[16:19]\n"
"ds_read_b64_tr_b16 v[162:163], v66 offset:59392\n"
"buffer_load_dwordx4 v239, s[20:23], s60 offen offset:1024 lds\n"
"v_mfma_f32_16x16x32_bf16 v[20:23], v[176:179], a[56:59], v[20:23]\n"
"ds_read_b64_tr_b16 v[164:165], v66 offset:61440\n"
"s_lshr_b32 s17, s64, 2\n"
"v_mfma_f32_16x16x32_bf16 v[20:23], v[180:183], a[60:63], v[20:23]\n"
"ds_read_b64_tr_b16 v[166:167], v66 offset:63488\n"
"s_waitcnt lgkmcnt(12)\n"
"v_mfma_f32_16x16x32_bf16 v[24:27], v[184:187], a[48:51], v[24:27]\n"
"ds_read_b64_tr_b16 v[168:169], v67 offset:57344\n"
"buffer_load_dwordx4 v[112:115], v84, s[40:43], s70 offen offset:0 sc1\n"
"v_mfma_f32_16x16x32_bf16 v[24:27], v[188:191], a[52:55], v[24:27]\n"
"ds_read_b64_tr_b16 v[170:171], v67 offset:59392\n"
"s_lshl_b32 s15, s11, 12\n"
"v_mfma_f32_16x16x32_bf16 v[28:31], v[184:187], a[56:59], v[28:31]\n"
"buffer_load_dwordx4 a[104:107], v77, s[40:43], s59 offen offset:0 sc1\n"
"ds_read_b64_tr_b16 v[172:173], v67 offset:61440\n"
"v_mfma_f32_16x16x32_bf16 v[28:31], v[188:191], a[60:63], v[28:31]\n"
"ds_read_b64_tr_b16 v[174:175], v67 offset:63488\n"
"s_waitcnt lgkmcnt(12)\n"
"v_mfma_f32_16x16x32_bf16 v[32:35], v[192:195], a[48:51], v[32:35]\n"
"ds_read_b64_tr_b16 v[176:177], v68 offset:0\n"
"s_add_u32 m0, s61, 8192\n"
"v_mfma_f32_16x16x32_bf16 v[32:35], v[196:199], a[52:55], v[32:35]\n"
"ds_read_b64_tr_b16 v[178:179], v68 offset:2048\n"
"s_mov_b32 s60, 655488\n"
"v_mfma_f32_16x16x32_bf16 v[36:39], v[192:195], a[56:59], v[36:39]\n"
"ds_read_b64_tr_b16 v[180:181], v68 offset:4096\n"
"buffer_load_dwordx4 v238, s[20:23], s60 offen offset:0 lds\n"
"v_mfma_f32_16x16x32_bf16 v[36:39], v[196:199], a[60:63], v[36:39]\n"
"ds_read_b64_tr_b16 v[182:183], v68 offset:6144\n"
"s_waitcnt lgkmcnt(12)\n"
"v_mfma_f32_16x16x32_bf16 v[40:43], v[200:203], a[48:51], v[40:43]\n"
"ds_read_b64_tr_b16 v[184:185], v69 offset:0\n"
"s_lshl_b32 s71, s67, 19\n"
"v_mfma_f32_16x16x32_bf16 v[40:43], v[204:207], a[52:55], v[40:43]\n"
"buffer_load_dwordx4 a[108:111], v77, s[40:43], s59 offen offset:1024 sc1\n"
"ds_read_b64_tr_b16 v[186:187], v69 offset:2048\n"
"v_mfma_f32_16x16x32_bf16 v[44:47], v[200:203], a[56:59], v[44:47]\n"
"ds_read_b64_tr_b16 v[188:189], v69 offset:4096\n"
"s_lshl_b32 s73, s65, 19\n"
"v_mfma_f32_16x16x32_bf16 v[44:47], v[204:207], a[60:63], v[44:47]\n"
"ds_read_b64_tr_b16 v[190:191], v69 offset:6144\n"
"s_waitcnt lgkmcnt(12)\n"
"v_mfma_f32_16x16x32_bf16 v[48:51], v[160:163], a[48:51], v[48:51]\n"
"ds_read_b64_tr_b16 v[192:193], v70 offset:0\n"
"buffer_load_dwordx4 v239, s[20:23], s60 offen offset:1024 lds\n"
"v_mfma_f32_16x16x32_bf16 v[48:51], v[164:167], a[52:55], v[48:51]\n"
"ds_read_b64_tr_b16 v[194:195], v70 offset:2048\n"
"s_waitcnt vmcnt(16)\n"
"v_permlane16_swap_b32 v88, v90\n"
"v_mfma_f32_16x16x32_bf16 v[52:55], v[160:163], a[56:59], v[52:55]\n"
"ds_read_b64_tr_b16 v[196:197], v70 offset:4096\n"
"v_permlane16_swap_b32 v89, v91\n"
"v_mfma_f32_16x16x32_bf16 v[52:55], v[164:167], a[60:63], v[52:55]\n"
"ds_read_b64_tr_b16 v[198:199], v70 offset:6144\n"
"s_waitcnt lgkmcnt(12)\n"
"v_mfma_f32_16x16x32_bf16 v[56:59], v[168:171], a[48:51], v[56:59]\n"
"buffer_load_dwordx4 a[112:115], v76, s[40:43], s59 offen offset:2048 sc1\n"
"ds_read_b64_tr_b16 v[200:201], v71 offset:0\n"
"v_mfma_f32_16x16x32_bf16 v[56:59], v[172:175], a[52:55], v[56:59]\n"
"ds_read_b64_tr_b16 v[202:203], v71 offset:2048\n"
"buffer_load_dwordx4 v[100:103], v84, s[40:43], s69 offen offset:64 sc1\n"
"v_mfma_f32_16x16x32_bf16 v[60:63], v[168:171], a[56:59], v[60:63]\n"
"ds_read_b64_tr_b16 v[204:205], v71 offset:4096\n"
"s_add_u32 m0, s61, 16384\n"
"v_mfma_f32_16x16x32_bf16 v[60:63], v[172:175], a[60:63], v[60:63]\n"
"ds_read_b64_tr_b16 v[206:207], v71 offset:6144\n"
"s_waitcnt vmcnt(17) lgkmcnt(12)\n"
"v_mfma_f32_16x16x32_bf16 v[0:3], v[176:179], a[64:67], v[0:3]\n"
"ds_read_b64_tr_b16 v[160:161], v68 offset:8192\n"
"s_mov_b32 s60, 720896\n"
"v_mfma_f32_16x16x32_bf16 v[0:3], v[180:183], a[68:71], v[0:3]\n"
"ds_read_b64_tr_b16 v[162:163], v68 offset:10240\n"
"buffer_load_dwordx4 v238, s[20:23], s60 offen offset:0 lds\n"
"v_mfma_f32_16x16x32_bf16 v[4:7], v[176:179], a[72:75], v[4:7]\n"
"ds_read_b64_tr_b16 v[164:165], v68 offset:12288\n"
"v_lshlrev_b32 v240, 16, v88\n"
"v_mfma_f32_16x16x32_bf16 v[4:7], v[180:183], a[76:79], v[4:7]\n"
"buffer_load_dwordx4 a[116:119], v76, s[40:43], s59 offen offset:3072 sc1\n"
"s_waitcnt lgkmcnt(11)\n"
"v_mfma_f32_16x16x32_bf16 v[8:11], v[184:187], a[64:67], v[8:11]\n"
"ds_read_b64_tr_b16 v[166:167], v68 offset:14336\n"
"v_and_b32 v241, 0xffff0000, v88\n"
"v_mfma_f32_16x16x32_bf16 v[8:11], v[188:191], a[68:71], v[8:11]\n"
"ds_read_b64_tr_b16 v[168:169], v69 offset:8192\n"
"buffer_load_dwordx4 v239, s[20:23], s60 offen offset:1024 lds\n"
"v_mfma_f32_16x16x32_bf16 v[12:15], v[184:187], a[72:75], v[12:15]\n"
"ds_read_b64_tr_b16 v[170:171], v69 offset:10240\n"
"v_lshlrev_b32 v242, 16, v89\n"
"v_mfma_f32_16x16x32_bf16 v[12:15], v[188:191], a[76:79], v[12:15]\n"
"ds_read_b64_tr_b16 v[172:173], v69 offset:12288\n"
"s_waitcnt lgkmcnt(11)\n"
"v_mfma_f32_16x16x32_bf16 v[16:19], v[192:195], a[64:67], v[16:19]\n"
"buffer_load_dwordx2 v[222:223], v236, s[40:43], s35 offen sc1\n"
"ds_read_b64_tr_b16 v[174:175], v69 offset:14336\n"
"v_mfma_f32_16x16x32_bf16 v[16:19], v[196:199], a[68:71], v[16:19]\n"
"ds_read_b64_tr_b16 v[176:177], v70 offset:8192\n"
"v_and_b32 v243, 0xffff0000, v89\n"
"v_mfma_f32_16x16x32_bf16 v[20:23], v[192:195], a[72:75], v[20:23]\n"
"buffer_load_dwordx4 a[120:123], v77, s[40:43], s59 offen offset:2048 sc1\n"
"ds_read_b64_tr_b16 v[178:179], v70 offset:10240\n"
"v_mfma_f32_16x16x32_bf16 v[20:23], v[196:199], a[76:79], v[20:23]\n"
"ds_read_b64_tr_b16 v[180:181], v70 offset:12288\n"
"s_waitcnt lgkmcnt(11)\n"
"v_mfma_f32_16x16x32_bf16 v[24:27], v[200:203], a[64:67], v[24:27]\n"
"ds_read_b64_tr_b16 v[182:183], v70 offset:14336\n"
"s_add_u32 m0, s61, 24576\n"
"v_mfma_f32_16x16x32_bf16 v[24:27], v[204:207], a[68:71], v[24:27]\n"
"ds_read_b64_tr_b16 v[184:185], v71 offset:8192\n"
"s_mov_b32 s60, 721024\n"
"v_mfma_f32_16x16x32_bf16 v[28:31], v[200:203], a[72:75], v[28:31]\n"
"ds_read_b64_tr_b16 v[186:187], v71 offset:10240\n"
"buffer_load_dwordx4 v238, s[20:23], s60 offen offset:0 lds\n"
"v_mfma_f32_16x16x32_bf16 v[28:31], v[204:207], a[76:79], v[28:31]\n"
"ds_read_b64_tr_b16 v[188:189], v71 offset:12288\n"
"s_waitcnt lgkmcnt(11)\n"
"v_mfma_f32_16x16x32_bf16 v[32:35], v[160:163], a[64:67], v[32:35]\n"
"ds_read_b64_tr_b16 v[190:191], v71 offset:14336\n"
"buffer_load_dwordx4 v[132:135], v84, s[40:43], s69 offen offset:2112 sc1\n"
"v_mfma_f32_16x16x32_bf16 v[32:35], v[164:167], a[68:71], v[32:35]\n"
"buffer_load_dwordx4 a[124:127], v77, s[40:43], s59 offen offset:3072 sc1\n"
"ds_read_b64_tr_b16 v[192:193], v68 offset:16384\n"
"v_mfma_f32_16x16x32_bf16 v[36:39], v[160:163], a[72:75], v[36:39]\n"
"ds_read_b64_tr_b16 v[194:195], v68 offset:18432\n"
"s_mov_b32 s59, 16384\n"
"v_mfma_f32_16x16x32_bf16 v[36:39], v[164:167], a[76:79], v[36:39]\n"
"ds_read_b64_tr_b16 v[196:197], v68 offset:20480\n"
"s_waitcnt lgkmcnt(11)\n"
"v_mfma_f32_16x16x32_bf16 v[40:43], v[168:171], a[64:67], v[40:43]\n"
"ds_read_b64_tr_b16 v[198:199], v68 offset:22528\n"
"buffer_load_dwordx4 v239, s[20:23], s60 offen offset:1024 lds\n"
"v_mfma_f32_16x16x32_bf16 v[40:43], v[172:175], a[68:71], v[40:43]\n"
"ds_read_b64_tr_b16 v[200:201], v69 offset:16384\n"
"v_lshlrev_b32 v244, 16, v90\n"
"v_mfma_f32_16x16x32_bf16 v[44:47], v[168:171], a[72:75], v[44:47]\n"
"ds_read_b64_tr_b16 v[202:203], v69 offset:18432\n"
"v_and_b32 v245, 0xffff0000, v90\n"
"v_mfma_f32_16x16x32_bf16 v[44:47], v[172:175], a[76:79], v[44:47]\n"
"ds_read_b64_tr_b16 v[204:205], v69 offset:20480\n"
"s_waitcnt lgkmcnt(11)\n"
"v_mfma_f32_16x16x32_bf16 v[48:51], v[176:179], a[64:67], v[48:51]\n"
"ds_read_b64_tr_b16 v[206:207], v69 offset:22528\n"
"v_lshlrev_b32 v246, 16, v91\n"
"v_mfma_f32_16x16x32_bf16 v[48:51], v[180:183], a[68:71], v[48:51]\n"
"ds_read_b64_tr_b16 v[160:161], v70 offset:16384\n"
"v_and_b32 v247, 0xffff0000, v91\n"
"v_mfma_f32_16x16x32_bf16 v[52:55], v[176:179], a[72:75], v[52:55]\n"
"ds_read_b64_tr_b16 v[162:163], v70 offset:18432\n"
"s_add_u32 m0, s61, 32768\n"
"v_mfma_f32_16x16x32_bf16 v[52:55], v[180:183], a[76:79], v[52:55]\n"
"ds_read_b64_tr_b16 v[164:165], v70 offset:20480\n"
"s_waitcnt lgkmcnt(11)\n"
"v_mfma_f32_16x16x32_bf16 v[56:59], v[184:187], a[64:67], v[56:59]\n"
"ds_read_b64_tr_b16 v[166:167], v70 offset:22528\n"
"s_mov_b32 s60, 786432\n"
"v_mfma_f32_16x16x32_bf16 v[56:59], v[188:191], a[68:71], v[56:59]\n"
"ds_read_b64_tr_b16 v[168:169], v71 offset:16384\n"
"buffer_load_dwordx4 v238, s[20:23], s60 offen offset:0 lds\n"
"v_mfma_f32_16x16x32_bf16 v[60:63], v[184:187], a[72:75], v[60:63]\n"
"ds_read_b64_tr_b16 v[170:171], v71 offset:18432\n"
"buffer_load_dwordx4 v[116:119], v84, s[40:43], s70 offen offset:64 sc1\n"
"v_mfma_f32_16x16x32_bf16 v[60:63], v[188:191], a[76:79], v[60:63]\n"
"ds_read_b64_tr_b16 v[172:173], v71 offset:20480\n"
"s_waitcnt vmcnt(23) lgkmcnt(11)\n"
"v_mfma_f32_16x16x32_bf16 v[0:3], v[192:195], a[80:83], v[0:3]\n"
"ds_read_b64_tr_b16 v[174:175], v71 offset:22528\n"
"s_lshl_b32 s74, s65, 17\n"
"v_mfma_f32_16x16x32_bf16 v[0:3], v[196:199], a[84:87], v[0:3]\n"
"ds_read_b64_tr_b16 v[176:177], v68 offset:24576\n"
"buffer_load_dwordx4 v239, s[20:23], s60 offen offset:1024 lds\n"
"v_mfma_f32_16x16x32_bf16 v[4:7], v[192:195], a[88:91], v[4:7]\n"
"ds_read_b64_tr_b16 v[178:179], v68 offset:26624\n"
"v_mfma_f32_16x16x32_bf16 v[4:7], v[196:199], a[92:95], v[4:7]\n"
"ds_read_b64_tr_b16 v[180:181], v68 offset:28672\n"
"s_waitcnt lgkmcnt(11)\n"
"v_mfma_f32_16x16x32_bf16 v[8:11], v[200:203], a[80:83], v[8:11]\n"
"ds_read_b64_tr_b16 v[182:183], v68 offset:30720\n"
"v_mfma_f32_16x16x32_bf16 v[8:11], v[204:207], a[84:87], v[8:11]\n"
"ds_read_b64_tr_b16 v[184:185], v69 offset:24576\n"
"v_mfma_f32_16x16x32_bf16 v[12:15], v[200:203], a[88:91], v[12:15]\n"
"ds_read_b64_tr_b16 v[186:187], v69 offset:26624\n"
"v_mfma_f32_16x16x32_bf16 v[12:15], v[204:207], a[92:95], v[12:15]\n"
"ds_read_b64_tr_b16 v[188:189], v69 offset:28672\n"
"s_waitcnt lgkmcnt(11)\n"
"v_mfma_f32_16x16x32_bf16 v[16:19], v[160:163], a[80:83], v[16:19]\n"
"ds_read_b64_tr_b16 v[190:191], v69 offset:30720\n"
"s_add_u32 m0, s61, 40960\n"
"v_mfma_f32_16x16x32_bf16 v[16:19], v[164:167], a[84:87], v[16:19]\n"
"ds_read_b64_tr_b16 v[192:193], v70 offset:24576\n"
"s_mov_b32 s60, 851968\n"
"v_mfma_f32_16x16x32_bf16 v[20:23], v[160:163], a[88:91], v[20:23]\n"
"ds_read_b64_tr_b16 v[194:195], v70 offset:26624\n"
"buffer_load_dwordx4 v238, s[20:23], s60 offen offset:0 lds\n"
"v_mfma_f32_16x16x32_bf16 v[20:23], v[164:167], a[92:95], v[20:23]\n"
"ds_read_b64_tr_b16 v[196:197], v70 offset:28672\n"
"s_waitcnt lgkmcnt(11)\n"
"v_mfma_f32_16x16x32_bf16 v[24:27], v[168:171], a[80:83], v[24:27]\n"
"ds_read_b64_tr_b16 v[198:199], v70 offset:30720\n"
"buffer_load_dwordx4 v[92:95], v83, s[52:55], s68 offen offset:128\n"
"v_mfma_f32_16x16x32_bf16 v[24:27], v[172:175], a[84:87], v[24:27]\n"
"ds_read_b64_tr_b16 v[200:201], v71 offset:24576\n"
"v_add_u32 v83, s58, v85\n"
"v_mfma_f32_16x16x32_bf16 v[28:31], v[168:171], a[88:91], v[28:31]\n"
"ds_read_b64_tr_b16 v[202:203], v71 offset:26624\n"
"v_mfma_f32_16x16x32_bf16 v[28:31], v[172:175], a[92:95], v[28:31]\n"
"ds_read_b64_tr_b16 v[204:205], v71 offset:28672\n"
"s_waitcnt lgkmcnt(11)\n"
"v_mfma_f32_16x16x32_bf16 v[32:35], v[176:179], a[80:83], v[32:35]\n"
"ds_read_b64_tr_b16 v[206:207], v71 offset:30720\n"
"buffer_load_dwordx4 v239, s[20:23], s60 offen offset:1024 lds\n"
"v_mfma_f32_16x16x32_bf16 v[32:35], v[180:183], a[84:87], v[32:35]\n"
"ds_read_b64_tr_b16 v[160:161], v68 offset:32768\n"
"v_mfma_f32_16x16x32_bf16 v[36:39], v[176:179], a[88:91], v[36:39]\n"
"ds_read_b64_tr_b16 v[162:163], v68 offset:34816\n"
"v_mfma_f32_16x16x32_bf16 v[36:39], v[180:183], a[92:95], v[36:39]\n"
"ds_read_b64_tr_b16 v[164:165], v68 offset:36864\n"
"s_waitcnt lgkmcnt(11)\n"
"v_mfma_f32_16x16x32_bf16 v[40:43], v[184:187], a[80:83], v[40:43]\n"
"ds_read_b64_tr_b16 v[166:167], v68 offset:38912\n"
"s_waitcnt vmcnt(11)\n"
"v_cmp_ne_u64 vcc, s[6:7], v[222:223]\n"
"v_mfma_f32_16x16x32_bf16 v[40:43], v[188:191], a[84:87], v[40:43]\n"
"s_cbranch_vccnz Lpd_%=\n"
"Lpd_ok_%=:\n"
"buffer_load_dwordx4 a[0:3], v76, s[40:43], s59 offen offset:0 sc1\n"
"buffer_load_dwordx4 a[4:7], v76, s[40:43], s59 offen offset:1024 sc1\n"
"v_mfma_f32_16x16x32_bf16 v[44:47], v[184:187], a[88:91], v[44:47]\n"
"buffer_load_dwordx4 a[8:11], v77, s[40:43], s59 offen offset:0 sc1\n"
"buffer_load_dwordx4 a[12:15], v77, s[40:43], s59 offen offset:1024 sc1\n"
"buffer_load_dwordx4 a[16:19], v76, s[40:43], s59 offen offset:2048 sc1\n"
"v_mfma_f32_16x16x32_bf16 v[44:47], v[188:191], a[92:95], v[44:47]\n"
"ds_read_b64_tr_b16 v[168:169], v69 offset:32768\n"
"s_waitcnt lgkmcnt(9)\n"
"v_mfma_f32_16x16x32_bf16 v[48:51], v[192:195], a[80:83], v[48:51]\n"
"ds_read_b64_tr_b16 v[170:171], v69 offset:34816\n"
"v_lshlrev_b32 v236, 6, v232\n"
"v_mfma_f32_16x16x32_bf16 v[48:51], v[196:199], a[84:87], v[48:51]\n"
"ds_read_b64_tr_b16 v[172:173], v69 offset:36864\n"
"v_and_b32 v236, 0xc00, v236\n"
"v_mfma_f32_16x16x32_bf16 v[52:55], v[192:195], a[88:91], v[52:55]\n"
"ds_read_b64_tr_b16 v[174:175], v69 offset:38912\n"
"s_lshl_b32 s35, s65, 14\n"
"v_mfma_f32_16x16x32_bf16 v[52:55], v[196:199], a[92:95], v[52:55]\n"
"buffer_load_dwordx4 a[20:23], v76, s[40:43], s59 offen offset:3072 sc1\n"
"s_waitcnt lgkmcnt(8)\n"
"v_mfma_f32_16x16x32_bf16 v[56:59], v[200:203], a[80:83], v[56:59]\n"
"ds_read_b64_tr_b16 v[176:177], v70 offset:32768\n"
"buffer_load_dwordx4 v[104:107], v84, s[40:43], s69 offen offset:256 sc1\n"
"v_mfma_f32_16x16x32_bf16 v[56:59], v[204:207], a[84:87], v[56:59]\n"
"ds_read_b64_tr_b16 v[178:179], v70 offset:34816\n"
"s_add_u32 s35, s35, s15\n"
"v_mfma_f32_16x16x32_bf16 v[60:63], v[200:203], a[88:91], v[60:63]\n"
"ds_read_b64_tr_b16 v[180:181], v70 offset:36864\n"
"s_lshl_b32 s15, s17, 8\n"
"ds_read_b64_tr_b16 v[182:183], v70 offset:38912\n"
"v_mfma_f32_16x16x32_bf16 v[60:63], v[204:207], a[92:95], v[60:63]\n"
"ds_read_b64_tr_b16 v[184:185], v71 offset:32768\n"
"s_waitcnt lgkmcnt(9)\n"
"v_mfma_f32_16x16x32_bf16 v[0:3], v[160:163], a[96:99], v[0:3]\n"
"s_barrier\n"
"v_mfma_f32_16x16x32_bf16 v[0:3], v[164:167], a[100:103], v[0:3]\n"
"ds_read_b64_tr_b16 v[186:187], v71 offset:34816\n"
"s_add_u32 s35, s35, s15\n"
"v_mfma_f32_16x16x32_bf16 v[4:7], v[160:163], a[104:107], v[4:7]\n"
"buffer_load_dwordx4 a[24:27], v77, s[40:43], s59 offen offset:2048 sc1\n"
"ds_read_b64_tr_b16 v[188:189], v71 offset:36864\n"
"ds_read_b64_tr_b16 v[190:191], v71 offset:38912\n"
"v_mfma_f32_16x16x32_bf16 v[4:7], v[164:167], a[108:111], v[4:7]\n"
"ds_read_b64_tr_b16 v[192:193], v68 offset:40960\n"
"s_waitcnt lgkmcnt(9)\n"
"v_mfma_f32_16x16x32_bf16 v[8:11], v[168:171], a[96:99], v[8:11]\n"
"ds_read_b64_tr_b16 v[194:195], v68 offset:43008\n"
"s_add_u32 m0, s61, 49152\n"
"v_mfma_f32_16x16x32_bf16 v[8:11], v[172:175], a[100:103], v[8:11]\n"
"ds_read_b64_tr_b16 v[196:197], v68 offset:45056\n"
"s_mov_b32 s60, 917504\n"
"v_mfma_f32_16x16x32_bf16 v[12:15], v[168:171], a[104:107], v[12:15]\n"
"ds_read_b64_tr_b16 v[198:199], v68 offset:47104\n"
"buffer_load_dwordx4 v238, s[20:23], s60 offen offset:0 lds\n"
"v_mfma_f32_16x16x32_bf16 v[12:15], v[172:175], a[108:111], v[12:15]\n"
"ds_read_b64_tr_b16 v[200:201], v69 offset:40960\n"
"s_waitcnt lgkmcnt(9)\n"
"v_mfma_f32_16x16x32_bf16 v[16:19], v[176:179], a[96:99], v[16:19]\n"
"ds_read_b64_tr_b16 v[202:203], v69 offset:43008\n"
"s_add_u32 s35, s35, 13107200\n"
"v_mfma_f32_16x16x32_bf16 v[16:19], v[180:183], a[100:103], v[16:19]\n"
"buffer_load_dwordx4 a[28:31], v77, s[40:43], s59 offen offset:3072 sc1\n"
"ds_read_b64_tr_b16 v[204:205], v69 offset:45056\n"
"v_mfma_f32_16x16x32_bf16 v[20:23], v[176:179], a[104:107], v[20:23]\n"
"ds_read_b64_tr_b16 v[206:207], v69 offset:47104\n"
"s_mov_b32 s59, 20480\n"
"v_mfma_f32_16x16x32_bf16 v[20:23], v[180:183], a[108:111], v[20:23]\n"
"ds_read_b64_tr_b16 v[160:161], v70 offset:40960\n"
"s_waitcnt lgkmcnt(9)\n"
"v_mfma_f32_16x16x32_bf16 v[24:27], v[184:187], a[96:99], v[24:27]\n"
"ds_read_b64_tr_b16 v[162:163], v70 offset:43008\n"
"buffer_load_dwordx4 v239, s[20:23], s60 offen offset:1024 lds\n"
"v_mfma_f32_16x16x32_bf16 v[24:27], v[188:191], a[100:103], v[24:27]\n"
"ds_read_b64_tr_b16 v[164:165], v70 offset:45056\n"
"buffer_load_dwordx4 v[136:139], v84, s[40:43], s69 offen offset:2304 sc1\n"
"v_mfma_f32_16x16x32_bf16 v[28:31], v[184:187], a[104:107], v[28:31]\n"
"ds_read_b64_tr_b16 v[166:167], v70 offset:47104\n"
"s_lshl_b32 s15, s11, 17\n"
"v_mfma_f32_16x16x32_bf16 v[28:31], v[188:191], a[108:111], v[28:31]\n"
"ds_read_b64_tr_b16 v[168:169], v71 offset:40960\n"
"s_waitcnt lgkmcnt(9)\n"
"v_mfma_f32_16x16x32_bf16 v[32:35], v[192:195], a[96:99], v[32:35]\n"
"buffer_load_dwordx4 a[32:35], v76, s[40:43], s59 offen offset:0 sc1\n"
"ds_read_b64_tr_b16 v[170:171], v71 offset:43008\n"
"v_mfma_f32_16x16x32_bf16 v[32:35], v[196:199], a[100:103], v[32:35]\n"
"ds_read_b64_tr_b16 v[172:173], v71 offset:45056\n"
"s_add_u32 m0, s61, 57344\n"
"v_mfma_f32_16x16x32_bf16 v[36:39], v[192:195], a[104:107], v[36:39]\n"
"ds_read_b64_tr_b16 v[174:175], v71 offset:47104\n"
"s_mov_b32 s60, 983040\n"
"v_mfma_f32_16x16x32_bf16 v[36:39], v[196:199], a[108:111], v[36:39]\n"
"ds_read_b64_tr_b16 v[176:177], v68 offset:49152\n"
"s_waitcnt lgkmcnt(9)\n"
"v_mfma_f32_16x16x32_bf16 v[40:43], v[200:203], a[96:99], v[40:43]\n"
"ds_read_b64_tr_b16 v[178:179], v68 offset:51200\n"
"buffer_load_dwordx4 v238, s[20:23], s60 offen offset:0 lds\n"
"v_mfma_f32_16x16x32_bf16 v[40:43], v[204:207], a[100:103], v[40:43]\n"
"ds_read_b64_tr_b16 v[180:181], v68 offset:53248\n"
"s_add_u32 s71, s71, s15\n"
"v_mfma_f32_16x16x32_bf16 v[44:47], v[200:203], a[104:107], v[44:47]\n"
"ds_read_b64_tr_b16 v[182:183], v68 offset:55296\n"
"s_lshl_b32 s15, s16, 14\n"
"v_mfma_f32_16x16x32_bf16 v[44:47], v[204:207], a[108:111], v[44:47]\n"
"buffer_load_dwordx4 a[36:39], v76, s[40:43], s59 offen offset:1024 sc1\n"
"s_waitcnt lgkmcnt(8)\n"
"v_mfma_f32_16x16x32_bf16 v[48:51], v[160:163], a[96:99], v[48:51]\n"
"ds_read_b64_tr_b16 v[184:185], v69 offset:49152\n"
"buffer_load_dwordx4 v239, s[20:23], s60 offen offset:1024 lds\n"
"v_mfma_f32_16x16x32_bf16 v[48:51], v[164:167], a[100:103], v[48:51]\n"
"ds_read_b64_tr_b16 v[186:187], v69 offset:51200\n"
"s_add_u32 s71, s71, s15\n"
"v_mfma_f32_16x16x32_bf16 v[52:55], v[160:163], a[104:107], v[52:55]\n"
"ds_read_b64_tr_b16 v[188:189], v69 offset:53248\n"
"s_lshl_b32 s15, s66, 12\n"
"ds_read_b64_tr_b16 v[190:191], v69 offset:55296\n"
"v_mfma_f32_16x16x32_bf16 v[52:55], v[164:167], a[108:111], v[52:55]\n"
"ds_read_b64_tr_b16 v[192:193], v70 offset:49152\n"
"s_waitcnt lgkmcnt(9)\n"
"v_mfma_f32_16x16x32_bf16 v[56:59], v[168:171], a[96:99], v[56:59]\n"
"ds_read_b64_tr_b16 v[194:195], v70 offset:51200\n"
"buffer_load_dwordx4 v[120:123], v84, s[40:43], s70 offen offset:256 sc1\n"
"v_mfma_f32_16x16x32_bf16 v[56:59], v[172:175], a[100:103], v[56:59]\n"
"ds_read_b64_tr_b16 v[196:197], v70 offset:53248\n"
"s_add_u32 s71, s71, s15\n"
"v_mfma_f32_16x16x32_bf16 v[60:63], v[168:171], a[104:107], v[60:63]\n"
"buffer_load_dwordx4 a[40:43], v77, s[40:43], s59 offen offset:0 sc1\n"
"ds_read_b64_tr_b16 v[198:199], v70 offset:55296\n"
"v_mfma_f32_16x16x32_bf16 v[60:63], v[172:175], a[108:111], v[60:63]\n"
"ds_read_b64_tr_b16 v[200:201], v71 offset:49152\n"
"s_waitcnt vmcnt(25) lgkmcnt(9)\n"
"v_mfma_f32_16x16x32_bf16 v[0:3], v[176:179], a[112:115], v[0:3]\n"
"ds_read_b64_tr_b16 v[202:203], v71 offset:51200\n"
"s_add_u32 m0, s61, 65536\n"
"v_mfma_f32_16x16x32_bf16 v[0:3], v[180:183], a[116:119], v[0:3]\n"
"ds_read_b64_tr_b16 v[204:205], v71 offset:53248\n"
"s_mov_b32 s60, 786560\n"
"v_mfma_f32_16x16x32_bf16 v[4:7], v[176:179], a[120:123], v[4:7]\n"
"ds_read_b64_tr_b16 v[206:207], v71 offset:55296\n"
"buffer_load_dwordx4 v238, s[20:23], s60 offen offset:0 lds\n"
"v_mfma_f32_16x16x32_bf16 v[4:7], v[180:183], a[124:127], v[4:7]\n"
"ds_read_b64_tr_b16 v[160:161], v68 offset:57344\n"
"s_waitcnt lgkmcnt(9)\n"
"v_mfma_f32_16x16x32_bf16 v[8:11], v[184:187], a[112:115], v[8:11]\n"
"ds_read_b64_tr_b16 v[162:163], v68 offset:59392\n"
"s_add_u32 s71, s71, 4194304\n"
"v_mfma_f32_16x16x32_bf16 v[8:11], v[188:191], a[116:119], v[8:11]\n"
"buffer_load_dwordx4 a[44:47], v77, s[40:43], s59 offen offset:1024 sc1\n"
"ds_read_b64_tr_b16 v[164:165], v68 offset:61440\n"
"v_mfma_f32_16x16x32_bf16 v[12:15], v[184:187], a[120:123], v[12:15]\n"
"ds_read_b64_tr_b16 v[166:167], v68 offset:63488\n"
"buffer_load_dwordx4 v239, s[20:23], s60 offen offset:1024 lds\n"
"v_mfma_f32_16x16x32_bf16 v[12:15], v[188:191], a[124:127], v[12:15]\n"
"ds_read_b64_tr_b16 v[168:169], v69 offset:57344\n"
"s_waitcnt lgkmcnt(9)\n"
"v_mfma_f32_16x16x32_bf16 v[16:19], v[192:195], a[112:115], v[16:19]\n"
"ds_read_b64_tr_b16 v[170:171], v69 offset:59392\n"
"s_waitcnt vmcnt(22)\n"
"v_permlane16_swap_b32 v92, v94\n"
"v_mfma_f32_16x16x32_bf16 v[16:19], v[196:199], a[116:119], v[16:19]\n"
"ds_read_b64_tr_b16 v[172:173], v69 offset:61440\n"
"v_permlane16_swap_b32 v93, v95\n"
"v_mfma_f32_16x16x32_bf16 v[20:23], v[192:195], a[120:123], v[20:23]\n"
"ds_read_b64_tr_b16 v[174:175], v69 offset:63488\n"
"v_lshlrev_b32 v248, 16, v92\n"
"v_mfma_f32_16x16x32_bf16 v[20:23], v[196:199], a[124:127], v[20:23]\n"
"ds_read_b64_tr_b16 v[176:177], v70 offset:57344\n"
"s_waitcnt lgkmcnt(9)\n"
"v_mfma_f32_16x16x32_bf16 v[24:27], v[200:203], a[112:115], v[24:27]\n"
"buffer_load_dwordx4 a[48:51], v76, s[40:43], s59 offen offset:2048 sc1\n"
"ds_read_b64_tr_b16 v[178:179], v70 offset:59392\n"
"v_mfma_f32_16x16x32_bf16 v[24:27], v[204:207], a[116:119], v[24:27]\n"
"ds_read_b64_tr_b16 v[180:181], v70 offset:61440\n"
"s_add_u32 m0, s61, 73728\n"
"v_mfma_f32_16x16x32_bf16 v[28:31], v[200:203], a[120:123], v[28:31]\n"
"ds_read_b64_tr_b16 v[182:183], v70 offset:63488\n"
"s_mov_b32 s60, 852096\n"
"v_mfma_f32_16x16x32_bf16 v[28:31], v[204:207], a[124:127], v[28:31]\n"
"ds_read_b64_tr_b16 v[184:185], v71 offset:57344\n"
"s_waitcnt lgkmcnt(9)\n"
"v_mfma_f32_16x16x32_bf16 v[32:35], v[160:163], a[112:115], v[32:35]\n"
"ds_read_b64_tr_b16 v[186:187], v71 offset:59392\n"
"buffer_load_dwordx4 v238, s[20:23], s60 offen offset:0 lds\n"
"v_mfma_f32_16x16x32_bf16 v[32:35], v[164:167], a[116:119], v[32:35]\n"
"ds_read_b64_tr_b16 v[188:189], v71 offset:61440\n"
"buffer_load_dwordx4 v[108:111], v84, s[40:43], s69 offen offset:320 sc1\n"
"v_mfma_f32_16x16x32_bf16 v[36:39], v[160:163], a[120:123], v[36:39]\n"
"ds_read_b64_tr_b16 v[190:191], v71 offset:63488\n"
"v_and_b32 v249, 0xffff0000, v92\n"
"v_mfma_f32_16x16x32_bf16 v[36:39], v[164:167], a[124:127], v[36:39]\n"
"buffer_load_dwordx4 a[52:55], v76, s[40:43], s59 offen offset:3072 sc1\n"
"s_waitcnt lgkmcnt(8)\n"
"v_mfma_f32_16x16x32_bf16 v[40:43], v[168:171], a[112:115], v[40:43]\n"
"ds_read_b64_tr_b16 v[192:193], v72 offset:0\n"
"buffer_load_dwordx4 v239, s[20:23], s60 offen offset:1024 lds\n"
"v_mfma_f32_16x16x32_bf16 v[40:43], v[172:175], a[116:119], v[40:43]\n"
"ds_read_b64_tr_b16 v[194:195], v72 offset:2048\n"
"v_lshlrev_b32 v250, 16, v93\n"
"v_mfma_f32_16x16x32_bf16 v[44:47], v[168:171], a[120:123], v[44:47]\n"
"ds_read_b64_tr_b16 v[196:197], v72 offset:4096\n"
"v_and_b32 v251, 0xffff0000, v93\n"
"ds_read_b64_tr_b16 v[198:199], v72 offset:6144\n"
"v_mfma_f32_16x16x32_bf16 v[44:47], v[172:175], a[124:127], v[44:47]\n"
"ds_read_b64_tr_b16 v[200:201], v73 offset:0\n"
"s_waitcnt lgkmcnt(9)\n"
"v_mfma_f32_16x16x32_bf16 v[48:51], v[176:179], a[112:115], v[48:51]\n"
"ds_read_b64_tr_b16 v[202:203], v73 offset:2048\n"
"v_lshlrev_b32 v252, 16, v94\n"
"v_mfma_f32_16x16x32_bf16 v[48:51], v[180:183], a[116:119], v[48:51]\n"
"ds_read_b64_tr_b16 v[204:205], v73 offset:4096\n"
"v_and_b32 v253, 0xffff0000, v94\n"
"v_mfma_f32_16x16x32_bf16 v[52:55], v[176:179], a[120:123], v[52:55]\n"
"buffer_load_dwordx4 a[56:59], v77, s[40:43], s59 offen offset:2048 sc1\n"
"ds_read_b64_tr_b16 v[206:207], v73 offset:6144\n"
"v_mfma_f32_16x16x32_bf16 v[52:55], v[180:183], a[124:127], v[52:55]\n"
"ds_read_b64_tr_b16 v[160:161], v74 offset:0\n"
"s_waitcnt lgkmcnt(9)\n"
"v_mfma_f32_16x16x32_bf16 v[56:59], v[184:187], a[112:115], v[56:59]\n"
"ds_read_b64_tr_b16 v[162:163], v74 offset:2048\n"
"s_add_u32 m0, s61, 81920\n"
"v_mfma_f32_16x16x32_bf16 v[56:59], v[188:191], a[116:119], v[56:59]\n"
"ds_read_b64_tr_b16 v[164:165], v74 offset:4096\n"
"s_mov_b32 s60, 917632\n"
"v_mfma_f32_16x16x32_bf16 v[60:63], v[184:187], a[120:123], v[60:63]\n"
"ds_read_b64_tr_b16 v[166:167], v74 offset:6144\n"
"buffer_load_dwordx4 v238, s[20:23], s60 offen offset:0 lds\n"
"v_mfma_f32_16x16x32_bf16 v[60:63], v[188:191], a[124:127], v[60:63]\n"
"ds_read_b64_tr_b16 v[168:169], v75 offset:0\n"
"s_waitcnt vmcnt(24) lgkmcnt(9)\n"
"v_mfma_f32_16x16x32_bf16 v[0:3], v[192:195], a[0:3], v[0:3]\n"
"ds_read_b64_tr_b16 v[170:171], v75 offset:2048\n"
"buffer_load_dwordx4 v[140:143], v84, s[40:43], s69 offen offset:2368 sc1\n"
"v_mfma_f32_16x16x32_bf16 v[0:3], v[196:199], a[4:7], v[0:3]\n"
"buffer_load_dwordx4 a[60:63], v77, s[40:43], s59 offen offset:3072 sc1\n"
"ds_read_b64_tr_b16 v[172:173], v75 offset:4096\n"
"v_mfma_f32_16x16x32_bf16 v[4:7], v[192:195], a[8:11], v[4:7]\n"
"ds_read_b64_tr_b16 v[174:175], v75 offset:6144\n"
"s_mov_b32 s59, 24576\n"
"v_mfma_f32_16x16x32_bf16 v[4:7], v[196:199], a[12:15], v[4:7]\n"
"ds_read_b64_tr_b16 v[176:177], v72 offset:8192\n"
"s_waitcnt lgkmcnt(9)\n"
"v_mfma_f32_16x16x32_bf16 v[8:11], v[200:203], a[0:3], v[8:11]\n"
"ds_read_b64_tr_b16 v[178:179], v72 offset:10240\n"
"buffer_load_dwordx4 v239, s[20:23], s60 offen offset:1024 lds\n"
"v_mfma_f32_16x16x32_bf16 v[8:11], v[204:207], a[4:7], v[8:11]\n"
"ds_read_b64_tr_b16 v[180:181], v72 offset:12288\n"
"v_lshlrev_b32 v254, 16, v95\n"
"v_mfma_f32_16x16x32_bf16 v[12:15], v[200:203], a[8:11], v[12:15]\n"
"ds_read_b64_tr_b16 v[182:183], v72 offset:14336\n"
"v_and_b32 v255, 0xffff0000, v95\n"
"v_mfma_f32_16x16x32_bf16 v[12:15], v[204:207], a[12:15], v[12:15]\n"
"ds_read_b64_tr_b16 v[184:185], v73 offset:8192\n"
"s_waitcnt lgkmcnt(9)\n"
"v_mfma_f32_16x16x32_bf16 v[16:19], v[160:163], a[0:3], v[16:19]\n"
"buffer_load_dwordx4 a[64:67], v76, s[40:43], s59 offen offset:0 sc1\n"
"ds_read_b64_tr_b16 v[186:187], v73 offset:10240\n"
"v_mfma_f32_16x16x32_bf16 v[16:19], v[164:167], a[4:7], v[16:19]\n"
"ds_read_b64_tr_b16 v[188:189], v73 offset:12288\n"
"s_add_u32 m0, s61, 90112\n"
"v_mfma_f32_16x16x32_bf16 v[20:23], v[160:163], a[8:11], v[20:23]\n"
"ds_read_b64_tr_b16 v[190:191], v73 offset:14336\n"
"s_mov_b32 s60, 983168\n"
"v_mfma_f32_16x16x32_bf16 v[20:23], v[164:167], a[12:15], v[20:23]\n"
"ds_read_b64_tr_b16 v[192:193], v74 offset:8192\n"
"s_waitcnt lgkmcnt(9)\n"
"v_mfma_f32_16x16x32_bf16 v[24:27], v[168:171], a[0:3], v[24:27]\n"
"ds_read_b64_tr_b16 v[194:195], v74 offset:10240\n"
"buffer_load_dwordx4 v238, s[20:23], s60 offen offset:0 lds\n"
"v_mfma_f32_16x16x32_bf16 v[24:27], v[172:175], a[4:7], v[24:27]\n"
"ds_read_b64_tr_b16 v[196:197], v74 offset:12288\n"
"buffer_load_dwordx4 v[124:127], v84, s[40:43], s70 offen offset:320 sc1\n"
"v_mfma_f32_16x16x32_bf16 v[28:31], v[168:171], a[8:11], v[28:31]\n"
"ds_read_b64_tr_b16 v[198:199], v74 offset:14336\n"
"v_add_u32 v84, 81920, v85\n"
"v_mfma_f32_16x16x32_bf16 v[28:31], v[172:175], a[12:15], v[28:31]\n"
"buffer_load_dwordx4 a[68:71], v76, s[40:43], s59 offen offset:1024 sc1\n"
"s_waitcnt lgkmcnt(8)\n"
"v_mfma_f32_16x16x32_bf16 v[32:35], v[176:179], a[0:3], v[32:35]\n"
"ds_read_b64_tr_b16 v[200:201], v75 offset:8192\n"
"buffer_load_dwordx4 v239, s[20:23], s60 offen offset:1024 lds\n"
"v_mfma_f32_16x16x32_bf16 v[32:35], v[180:183], a[4:7], v[32:35]\n"
"ds_read_b64_tr_b16 v[202:203], v75 offset:10240\n"
"s_add_u32 s72, s71, 65536\n"
"v_mfma_f32_16x16x32_bf16 v[36:39], v[176:179], a[8:11], v[36:39]\n"
"ds_read_b64_tr_b16 v[204:205], v75 offset:12288\n"
"s_lshr_b32 s16, s64, 1\n"
"ds_read_b64_tr_b16 v[206:207], v75 offset:14336\n"
"v_mfma_f32_16x16x32_bf16 v[36:39], v[180:183], a[12:15], v[36:39]\n"
"ds_read_b64_tr_b16 v[160:161], v72 offset:16384\n"
"s_waitcnt lgkmcnt(9)\n"
"v_mfma_f32_16x16x32_bf16 v[40:43], v[184:187], a[0:3], v[40:43]\n"
"ds_read_b64_tr_b16 v[162:163], v72 offset:18432\n"
"s_lshl_b32 s15, s16, 16\n"
"v_mfma_f32_16x16x32_bf16 v[40:43], v[188:191], a[4:7], v[40:43]\n"
"ds_read_b64_tr_b16 v[164:165], v72 offset:20480\n"
"s_add_u32 s73, s73, s15\n"
"v_mfma_f32_16x16x32_bf16 v[44:47], v[184:187], a[8:11], v[44:47]\n"
"buffer_load_dwordx4 a[72:75], v77, s[40:43], s59 offen offset:0 sc1\n"
"ds_read_b64_tr_b16 v[166:167], v72 offset:22528\n"
"v_mfma_f32_16x16x32_bf16 v[44:47], v[188:191], a[12:15], v[44:47]\n"
"ds_read_b64_tr_b16 v[168:169], v73 offset:16384\n"
"s_waitcnt lgkmcnt(9)\n"
"v_mfma_f32_16x16x32_bf16 v[48:51], v[192:195], a[0:3], v[48:51]\n"
"ds_read_b64_tr_b16 v[170:171], v73 offset:18432\n"
"s_lshl_b32 s15, s11, 14\n"
"v_mfma_f32_16x16x32_bf16 v[48:51], v[196:199], a[4:7], v[48:51]\n"
"ds_read_b64_tr_b16 v[172:173], v73 offset:20480\n"
"s_add_u32 s73, s73, s15\n"
"v_mfma_f32_16x16x32_bf16 v[52:55], v[192:195], a[8:11], v[52:55]\n"
"ds_read_b64_tr_b16 v[174:175], v73 offset:22528\n"
"s_add_u32 s73, s73, 4194304\n"
"v_mfma_f32_16x16x32_bf16 v[52:55], v[196:199], a[12:15], v[52:55]\n"
"ds_read_b64_tr_b16 v[176:177], v74 offset:16384\n"
"s_waitcnt lgkmcnt(9)\n"
"v_mfma_f32_16x16x32_bf16 v[56:59], v[200:203], a[0:3], v[56:59]\n"
"ds_read_b64_tr_b16 v[178:179], v74 offset:18432\n"
"buffer_load_dwordx2 v[144:145], v86, s[48:51], s79 offen offset:0\n"
"v_mfma_f32_16x16x32_bf16 v[56:59], v[204:207], a[4:7], v[56:59]\n"
"buffer_load_dwordx4 a[76:79], v77, s[40:43], s59 offen offset:1024 sc1\n"
"ds_read_b64_tr_b16 v[180:181], v74 offset:20480\n"
"v_mfma_f32_16x16x32_bf16 v[60:63], v[200:203], a[8:11], v[60:63]\n"
"ds_read_b64_tr_b16 v[182:183], v74 offset:22528\n"
"s_lshl_b32 s15, s16, 14\n"
"v_mfma_f32_16x16x32_bf16 v[60:63], v[204:207], a[12:15], v[60:63]\n"
"ds_read_b64_tr_b16 v[184:185], v75 offset:16384\n"
"s_waitcnt vmcnt(29) lgkmcnt(9)\n"
"v_mfma_f32_16x16x32_bf16 v[0:3], v[160:163], a[16:19], v[0:3]\n"
"s_barrier\n"
"v_mfma_f32_16x16x32_bf16 v[0:3], v[164:167], a[20:23], v[0:3]\n"
"ds_read_b64_tr_b16 v[186:187], v75 offset:18432\n"
"s_add_u32 s74, s74, s15\n"
"v_mfma_f32_16x16x32_bf16 v[4:7], v[160:163], a[24:27], v[4:7]\n"
"ds_read_b64_tr_b16 v[188:189], v75 offset:20480\n"
"s_lshl_b32 s15, s11, 12\n"
"ds_read_b64_tr_b16 v[190:191], v75 offset:22528\n"
"v_mfma_f32_16x16x32_bf16 v[4:7], v[164:167], a[28:31], v[4:7]\n"
"ds_read_b64_tr_b16 v[192:193], v72 offset:24576\n"
"s_waitcnt lgkmcnt(9)\n"
"v_mfma_f32_16x16x32_bf16 v[8:11], v[168:171], a[16:19], v[8:11]\n"
"buffer_load_dwordx4 a[80:83], v76, s[40:43], s59 offen offset:2048 sc1\n"
"ds_read_b64_tr_b16 v[194:195], v72 offset:26624\n"
"v_mfma_f32_16x16x32_bf16 v[8:11], v[172:175], a[20:23], v[8:11]\n"
"ds_read_b64_tr_b16 v[196:197], v72 offset:28672\n"
"s_add_u32 s74, s74, s15\n"
"v_mfma_f32_16x16x32_bf16 v[12:15], v[168:171], a[24:27], v[12:15]\n"
"ds_read_b64_tr_b16 v[198:199], v72 offset:30720\n"
"s_add_u32 s74, s74, 54525952\n"
"v_mfma_f32_16x16x32_bf16 v[12:15], v[172:175], a[28:31], v[12:15]\n"
"ds_read_b64_tr_b16 v[200:201], v73 offset:24576\n"
"s_waitcnt lgkmcnt(9)\n"
"v_mfma_f32_16x16x32_bf16 v[16:19], v[176:179], a[16:19], v[16:19]\n"
"ds_read_b64_tr_b16 v[202:203], v73 offset:26624\n"
"s_add_u32 s75, s74, 2097152\n"
"v_mfma_f32_16x16x32_bf16 v[16:19], v[180:183], a[20:23], v[16:19]\n"
"ds_read_b64_tr_b16 v[204:205], v73 offset:28672\n"
"v_mfma_f32_16x16x32_bf16 v[20:23], v[176:179], a[24:27], v[20:23]\n"
"ds_read_b64_tr_b16 v[206:207], v73 offset:30720\n"
"v_mfma_f32_16x16x32_bf16 v[20:23], v[180:183], a[28:31], v[20:23]\n"
"buffer_load_dwordx4 a[84:87], v76, s[40:43], s59 offen offset:3072 sc1\n"
"s_waitcnt lgkmcnt(8)\n"
"v_mfma_f32_16x16x32_bf16 v[24:27], v[184:187], a[16:19], v[24:27]\n"
"ds_read_b64_tr_b16 v[160:161], v74 offset:24576\n"
"buffer_load_dwordx2 v[146:147], v86, s[48:51], s79 offen offset:32\n"
"v_mfma_f32_16x16x32_bf16 v[24:27], v[188:191], a[20:23], v[24:27]\n"
"ds_read_b64_tr_b16 v[162:163], v74 offset:26624\n"
"v_mfma_f32_16x16x32_bf16 v[28:31], v[184:187], a[24:27], v[28:31]\n"
"ds_read_b64_tr_b16 v[164:165], v74 offset:28672\n"
"ds_read_b64_tr_b16 v[166:167], v74 offset:30720\n"
"v_mfma_f32_16x16x32_bf16 v[28:31], v[188:191], a[28:31], v[28:31]\n"
"ds_read_b64_tr_b16 v[168:169], v75 offset:24576\n"
"s_waitcnt lgkmcnt(9)\n"
"v_mfma_f32_16x16x32_bf16 v[32:35], v[192:195], a[16:19], v[32:35]\n"
"ds_read_b64_tr_b16 v[170:171], v75 offset:26624\n"
"v_mfma_f32_16x16x32_bf16 v[32:35], v[196:199], a[20:23], v[32:35]\n"
"ds_read_b64_tr_b16 v[172:173], v75 offset:28672\n"
"v_mfma_f32_16x16x32_bf16 v[36:39], v[192:195], a[24:27], v[36:39]\n"
"buffer_load_dwordx4 a[88:91], v77, s[40:43], s59 offen offset:2048 sc1\n"
"ds_read_b64_tr_b16 v[174:175], v75 offset:30720\n"
"v_mfma_f32_16x16x32_bf16 v[36:39], v[196:199], a[28:31], v[36:39]\n"
"ds_read_b64_tr_b16 v[176:177], v64 offset:0\n"
"s_waitcnt lgkmcnt(9)\n"
"v_mfma_f32_16x16x32_bf16 v[40:43], v[200:203], a[16:19], v[40:43]\n"
"ds_read_b64_tr_b16 v[178:179], v64 offset:2048\n"
"v_mfma_f32_16x16x32_bf16 v[40:43], v[204:207], a[20:23], v[40:43]\n"
"ds_read_b64_tr_b16 v[180:181], v64 offset:4096\n"
"v_mfma_f32_16x16x32_bf16 v[44:47], v[200:203], a[24:27], v[44:47]\n"
"ds_read_b64_tr_b16 v[182:183], v64 offset:6144\n"
"v_mfma_f32_16x16x32_bf16 v[44:47], v[204:207], a[28:31], v[44:47]\n"
"ds_read_b64_tr_b16 v[184:185], v65 offset:0\n"
"s_waitcnt lgkmcnt(9)\n"
"v_mfma_f32_16x16x32_bf16 v[48:51], v[160:163], a[16:19], v[48:51]\n"
"ds_read_b64_tr_b16 v[186:187], v65 offset:2048\n"
"v_mfma_f32_16x16x32_bf16 v[48:51], v[164:167], a[20:23], v[48:51]\n"
"buffer_load_dwordx4 a[92:95], v77, s[40:43], s59 offen offset:3072 sc1\n"
"ds_read_b64_tr_b16 v[188:189], v65 offset:4096\n"
"v_mfma_f32_16x16x32_bf16 v[52:55], v[160:163], a[24:27], v[52:55]\n"
"ds_read_b64_tr_b16 v[190:191], v65 offset:6144\n"
"s_mov_b32 s59, 28672\n"
"v_mfma_f32_16x16x32_bf16 v[52:55], v[164:167], a[28:31], v[52:55]\n"
"ds_read_b64_tr_b16 v[192:193], v66 offset:0\n"
"s_waitcnt lgkmcnt(9)\n"
"v_mfma_f32_16x16x32_bf16 v[56:59], v[168:171], a[16:19], v[56:59]\n"
"ds_read_b64_tr_b16 v[194:195], v66 offset:2048\n"
"buffer_load_dwordx2 v[148:149], v86, s[48:51], s79 offen offset:64\n"
"v_mfma_f32_16x16x32_bf16 v[56:59], v[172:175], a[20:23], v[56:59]\n"
"ds_read_b64_tr_b16 v[196:197], v66 offset:4096\n"
"v_mfma_f32_16x16x32_bf16 v[60:63], v[168:171], a[24:27], v[60:63]\n"
"ds_read_b64_tr_b16 v[198:199], v66 offset:6144\n"
"v_mfma_f32_16x16x32_bf16 v[60:63], v[172:175], a[28:31], v[60:63]\n"
"ds_read_b64_tr_b16 v[200:201], v67 offset:0\n"
"s_waitcnt vmcnt(25) lgkmcnt(9)\n"
"v_mfma_f32_16x16x32_bf16 v[0:3], v[176:179], a[32:35], v[0:3]\n"
"buffer_load_dwordx4 a[96:99], v76, s[40:43], s59 offen offset:0 sc1\n"
"ds_read_b64_tr_b16 v[202:203], v67 offset:2048\n"
"v_mfma_f32_16x16x32_bf16 v[0:3], v[180:183], a[36:39], v[0:3]\n"
"ds_read_b64_tr_b16 v[204:205], v67 offset:4096\n"
"s_lshr_b32 s96, s10, 3\n"
"v_mfma_f32_16x16x32_bf16 v[4:7], v[176:179], a[40:43], v[4:7]\n"
"ds_read_b64_tr_b16 v[206:207], v67 offset:6144\n"
"s_lshl_b32 s96, s96, 2\n"
"v_mfma_f32_16x16x32_bf16 v[4:7], v[180:183], a[44:47], v[4:7]\n"
"ds_read_b64_tr_b16 v[160:161], v64 offset:8192\n"
"s_waitcnt lgkmcnt(9)\n"
"v_mfma_f32_16x16x32_bf16 v[8:11], v[184:187], a[32:35], v[8:11]\n"
"ds_read_b64_tr_b16 v[162:163], v64 offset:10240\n"
"s_add_u32 s96, s96, s11\n"
"v_mfma_f32_16x16x32_bf16 v[8:11], v[188:191], a[36:39], v[8:11]\n"
"ds_read_b64_tr_b16 v[164:165], v64 offset:12288\n"
"s_lshl_b32 s96, s96, 7\n"
"v_mfma_f32_16x16x32_bf16 v[12:15], v[184:187], a[40:43], v[12:15]\n"
"ds_read_b64_tr_b16 v[166:167], v64 offset:14336\n"
"s_add_u32 s96, s96, 2048\n"
"v_mfma_f32_16x16x32_bf16 v[12:15], v[188:191], a[44:47], v[12:15]\n"
"buffer_load_dwordx4 a[100:103], v76, s[40:43], s59 offen offset:1024 sc1\n"
"s_waitcnt lgkmcnt(8)\n"
"v_mfma_f32_16x16x32_bf16 v[16:19], v[192:195], a[32:35], v[16:19]\n"
"ds_read_b64_tr_b16 v[168:169], v65 offset:8192\n"
"v_lshl_add_u32 v87, v232, 10, s96\n"
"v_mfma_f32_16x16x32_bf16 v[16:19], v[196:199], a[36:39], v[16:19]\n"
"ds_read_b64_tr_b16 v[170:171], v65 offset:10240\n"
"v_min_u32 v87, Lslow_%=-Lct3_%=, v87\n"
"v_mfma_f32_16x16x32_bf16 v[20:23], v[192:195], a[40:43], v[20:23]\n"
"ds_read_b64_tr_b16 v[172:173], v65 offset:12288\n"
"ds_read_b64_tr_b16 v[174:175], v65 offset:14336\n"
"v_mfma_f32_16x16x32_bf16 v[20:23], v[196:199], a[44:47], v[20:23]\n"
"ds_read_b64_tr_b16 v[176:177], v66 offset:8192\n"
"s_waitcnt lgkmcnt(9)\n"
"v_mfma_f32_16x16x32_bf16 v[24:27], v[200:203], a[32:35], v[24:27]\n"
"s_getpc_b64 s[96:97]\n"
"Lct3_%=:\n"
"s_mov_b64 exec, 1\n"
"global_load_dword a151, v87, s[96:97]\n"
"s_mov_b64 exec, -1\n"
"v_mfma_f32_16x16x32_bf16 v[24:27], v[204:207], a[36:39], v[24:27]\n"
"ds_read_b64_tr_b16 v[178:179], v66 offset:10240\n"
"buffer_load_dwordx2 v[150:151], v86, s[48:51], s79 offen offset:96\n"
"v_mfma_f32_16x16x32_bf16 v[28:31], v[200:203], a[40:43], v[28:31]\n"
"buffer_load_dwordx4 a[104:107], v77, s[40:43], s59 offen offset:0 sc1\n"
"ds_read_b64_tr_b16 v[180:181], v66 offset:12288\n"
"ds_read_b64_tr_b16 v[182:183], v66 offset:14336\n"
"v_mfma_f32_16x16x32_bf16 v[28:31], v[204:207], a[44:47], v[28:31]\n"
"ds_read_b64_tr_b16 v[184:185], v67 offset:8192\n"
"s_waitcnt lgkmcnt(9)\n"
"v_mfma_f32_16x16x32_bf16 v[32:35], v[160:163], a[32:35], v[32:35]\n"
"ds_read_b64_tr_b16 v[186:187], v67 offset:10240\n"
"v_mfma_f32_16x16x32_bf16 v[32:35], v[164:167], a[36:39], v[32:35]\n"
"ds_read_b64_tr_b16 v[188:189], v67 offset:12288\n"
"v_mfma_f32_16x16x32_bf16 v[36:39], v[160:163], a[40:43], v[36:39]\n"
"ds_read_b64_tr_b16 v[190:191], v67 offset:14336\n"
"v_mfma_f32_16x16x32_bf16 v[36:39], v[164:167], a[44:47], v[36:39]\n"
"ds_read_b64_tr_b16 v[192:193], v64 offset:16384\n"
"s_waitcnt lgkmcnt(9)\n"
"v_mfma_f32_16x16x32_bf16 v[40:43], v[168:171], a[32:35], v[40:43]\n"
"ds_read_b64_tr_b16 v[194:195], v64 offset:18432\n"
"v_mfma_f32_16x16x32_bf16 v[40:43], v[172:175], a[36:39], v[40:43]\n"
"buffer_load_dwordx4 a[108:111], v77, s[40:43], s59 offen offset:1024 sc1\n"
"ds_read_b64_tr_b16 v[196:197], v64 offset:20480\n"
"v_mfma_f32_16x16x32_bf16 v[44:47], v[168:171], a[40:43], v[44:47]\n"
"ds_read_b64_tr_b16 v[198:199], v64 offset:22528\n"
"v_mfma_f32_16x16x32_bf16 v[44:47], v[172:175], a[44:47], v[44:47]\n"
"ds_read_b64_tr_b16 v[200:201], v65 offset:16384\n"
"s_waitcnt lgkmcnt(9)\n"
"v_mfma_f32_16x16x32_bf16 v[48:51], v[176:179], a[32:35], v[48:51]\n"
"ds_read_b64_tr_b16 v[202:203], v65 offset:18432\n"
"v_mfma_f32_16x16x32_bf16 v[48:51], v[180:183], a[36:39], v[48:51]\n"
"ds_read_b64_tr_b16 v[204:205], v65 offset:20480\n"
"v_mfma_f32_16x16x32_bf16 v[52:55], v[176:179], a[40:43], v[52:55]\n"
"ds_read_b64_tr_b16 v[206:207], v65 offset:22528\n"
"v_mfma_f32_16x16x32_bf16 v[52:55], v[180:183], a[44:47], v[52:55]\n"
"ds_read_b64_tr_b16 v[160:161], v66 offset:16384\n"
"s_waitcnt lgkmcnt(9)\n"
"v_mfma_f32_16x16x32_bf16 v[56:59], v[184:187], a[32:35], v[56:59]\n"
"buffer_load_dwordx4 a[112:115], v76, s[40:43], s59 offen offset:2048 sc1\n"
"ds_read_b64_tr_b16 v[162:163], v66 offset:18432\n"
"v_mfma_f32_16x16x32_bf16 v[56:59], v[188:191], a[36:39], v[56:59]\n"
"ds_read_b64_tr_b16 v[164:165], v66 offset:20480\n"
"buffer_load_dwordx2 v[152:153], v86, s[48:51], s78 offen offset:0\n"
"v_mfma_f32_16x16x32_bf16 v[60:63], v[184:187], a[40:43], v[60:63]\n"
"ds_read_b64_tr_b16 v[166:167], v66 offset:22528\n"
"v_mfma_f32_16x16x32_bf16 v[60:63], v[188:191], a[44:47], v[60:63]\n"
"ds_read_b64_tr_b16 v[168:169], v67 offset:16384\n"
"s_waitcnt vmcnt(23) lgkmcnt(9)\n"
"v_mfma_f32_16x16x32_bf16 v[0:3], v[192:195], a[48:51], v[0:3]\n"
"ds_read_b64_tr_b16 v[170:171], v67 offset:18432\n"
"v_mfma_f32_16x16x32_bf16 v[0:3], v[196:199], a[52:55], v[0:3]\n"
"ds_read_b64_tr_b16 v[172:173], v67 offset:20480\n"
"v_mfma_f32_16x16x32_bf16 v[4:7], v[192:195], a[56:59], v[4:7]\n"
"ds_read_b64_tr_b16 v[174:175], v67 offset:22528\n"
"v_mfma_f32_16x16x32_bf16 v[4:7], v[196:199], a[60:63], v[4:7]\n"
"buffer_load_dwordx4 a[116:119], v76, s[40:43], s59 offen offset:3072 sc1\n"
"s_waitcnt lgkmcnt(8)\n"
"v_mfma_f32_16x16x32_bf16 v[8:11], v[200:203], a[48:51], v[8:11]\n"
"ds_read_b64_tr_b16 v[176:177], v64 offset:24576\n"
"v_mfma_f32_16x16x32_bf16 v[8:11], v[204:207], a[52:55], v[8:11]\n"
"ds_read_b64_tr_b16 v[178:179], v64 offset:26624\n"
"v_mfma_f32_16x16x32_bf16 v[12:15], v[200:203], a[56:59], v[12:15]\n"
"ds_read_b64_tr_b16 v[180:181], v64 offset:28672\n"
"ds_read_b64_tr_b16 v[182:183], v64 offset:30720\n"
"v_mfma_f32_16x16x32_bf16 v[12:15], v[204:207], a[60:63], v[12:15]\n"
"ds_read_b64_tr_b16 v[184:185], v65 offset:24576\n"
"s_waitcnt lgkmcnt(9)\n"
"v_mfma_f32_16x16x32_bf16 v[16:19], v[160:163], a[48:51], v[16:19]\n"
"ds_read_b64_tr_b16 v[186:187], v65 offset:26624\n"
"v_mfma_f32_16x16x32_bf16 v[16:19], v[164:167], a[52:55], v[16:19]\n"
"ds_read_b64_tr_b16 v[188:189], v65 offset:28672\n"
"v_mfma_f32_16x16x32_bf16 v[20:23], v[160:163], a[56:59], v[20:23]\n"
"buffer_load_dwordx4 a[120:123], v77, s[40:43], s59 offen offset:2048 sc1\n"
"ds_read_b64_tr_b16 v[190:191], v65 offset:30720\n"
"v_mfma_f32_16x16x32_bf16 v[20:23], v[164:167], a[60:63], v[20:23]\n"
"ds_read_b64_tr_b16 v[192:193], v66 offset:24576\n"
"s_waitcnt lgkmcnt(9)\n"
"v_mfma_f32_16x16x32_bf16 v[24:27], v[168:171], a[48:51], v[24:27]\n"
"ds_read_b64_tr_b16 v[194:195], v66 offset:26624\n"
"buffer_load_dwordx2 v[154:155], v86, s[48:51], s78 offen offset:32\n"
"v_mfma_f32_16x16x32_bf16 v[24:27], v[172:175], a[52:55], v[24:27]\n"
"ds_read_b64_tr_b16 v[196:197], v66 offset:28672\n"
"v_mfma_f32_16x16x32_bf16 v[28:31], v[168:171], a[56:59], v[28:31]\n"
"ds_read_b64_tr_b16 v[198:199], v66 offset:30720\n"
"v_mfma_f32_16x16x32_bf16 v[28:31], v[172:175], a[60:63], v[28:31]\n"
"ds_read_b64_tr_b16 v[200:201], v67 offset:24576\n"
"s_waitcnt lgkmcnt(9)\n"
"v_mfma_f32_16x16x32_bf16 v[32:35], v[176:179], a[48:51], v[32:35]\n"
"ds_read_b64_tr_b16 v[202:203], v67 offset:26624\n"
"v_mfma_f32_16x16x32_bf16 v[32:35], v[180:183], a[52:55], v[32:35]\n"
"buffer_load_dwordx4 a[124:127], v77, s[40:43], s59 offen offset:3072 sc1\n"
"ds_read_b64_tr_b16 v[204:205], v67 offset:28672\n"
"v_mfma_f32_16x16x32_bf16 v[36:39], v[176:179], a[56:59], v[36:39]\n"
"ds_read_b64_tr_b16 v[206:207], v67 offset:30720\n"
"v_mfma_f32_16x16x32_bf16 v[36:39], v[180:183], a[60:63], v[36:39]\n"
"ds_read_b64_tr_b16 v[160:161], v64 offset:32768\n"
"s_waitcnt lgkmcnt(9)\n"
"v_mfma_f32_16x16x32_bf16 v[40:43], v[184:187], a[48:51], v[40:43]\n"
"ds_read_b64_tr_b16 v[162:163], v64 offset:34816\n"
"v_mfma_f32_16x16x32_bf16 v[40:43], v[188:191], a[52:55], v[40:43]\n"
"ds_read_b64_tr_b16 v[164:165], v64 offset:36864\n"
"v_mfma_f32_16x16x32_bf16 v[44:47], v[184:187], a[56:59], v[44:47]\n"
"ds_read_b64_tr_b16 v[166:167], v64 offset:38912\n"
"v_mfma_f32_16x16x32_bf16 v[44:47], v[188:191], a[60:63], v[44:47]\n"
"ds_read_b64_tr_b16 v[168:169], v65 offset:32768\n"
"s_waitcnt lgkmcnt(9)\n"
"v_mfma_f32_16x16x32_bf16 v[48:51], v[192:195], a[48:51], v[48:51]\n"
"ds_read_b64_tr_b16 v[170:171], v65 offset:34816\n"
"v_mfma_f32_16x16x32_bf16 v[48:51], v[196:199], a[52:55], v[48:51]\n"
"ds_read_b64_tr_b16 v[172:173], v65 offset:36864\n"
"v_mfma_f32_16x16x32_bf16 v[52:55], v[192:195], a[56:59], v[52:55]\n"
"ds_read_b64_tr_b16 v[174:175], v65 offset:38912\n"
"v_mfma_f32_16x16x32_bf16 v[52:55], v[196:199], a[60:63], v[52:55]\n"
"ds_read_b64_tr_b16 v[176:177], v66 offset:32768\n"
"s_waitcnt lgkmcnt(9)\n"
"v_mfma_f32_16x16x32_bf16 v[56:59], v[200:203], a[48:51], v[56:59]\n"
"ds_read_b64_tr_b16 v[178:179], v66 offset:34816\n"
"buffer_load_dwordx2 v[156:157], v86, s[48:51], s78 offen offset:64\n"
"v_mfma_f32_16x16x32_bf16 v[56:59], v[204:207], a[52:55], v[56:59]\n"
"ds_read_b64_tr_b16 v[180:181], v66 offset:36864\n"
"v_mfma_f32_16x16x32_bf16 v[60:63], v[200:203], a[56:59], v[60:63]\n"
"ds_read_b64_tr_b16 v[182:183], v66 offset:38912\n"
"v_mfma_f32_16x16x32_bf16 v[60:63], v[204:207], a[60:63], v[60:63]\n"
"ds_read_b64_tr_b16 v[184:185], v67 offset:32768\n"
"s_waitcnt vmcnt(19) lgkmcnt(9)\n"
"v_mfma_f32_16x16x32_bf16 v[0:3], v[160:163], a[64:67], v[0:3]\n"
"s_barrier\n"
"v_mfma_f32_16x16x32_bf16 v[0:3], v[164:167], a[68:71], v[0:3]\n"
"ds_read_b64_tr_b16 v[186:187], v67 offset:34816\n"
"v_mfma_f32_16x16x32_bf16 v[4:7], v[160:163], a[72:75], v[4:7]\n"
"ds_read_b64_tr_b16 v[188:189], v67 offset:36864\n"
"s_add_u32 m0, s61, 98304\n"
"ds_read_b64_tr_b16 v[190:191], v67 offset:38912\n"
"v_mfma_f32_16x16x32_bf16 v[4:7], v[164:167], a[76:79], v[4:7]\n"
"ds_read_b64_tr_b16 v[192:193], v64 offset:40960\n"
"s_waitcnt lgkmcnt(9)\n"
"v_mfma_f32_16x16x32_bf16 v[8:11], v[168:171], a[64:67], v[8:11]\n"
"ds_read_b64_tr_b16 v[194:195], v64 offset:43008\n"
"s_mov_b32 s60, 0\n"
"v_mfma_f32_16x16x32_bf16 v[8:11], v[172:175], a[68:71], v[8:11]\n"
"ds_read_b64_tr_b16 v[196:197], v64 offset:45056\n"
"buffer_load_dwordx4 v80, s[24:27], s60 offen offset:0 lds\n"
"v_mfma_f32_16x16x32_bf16 v[12:15], v[168:171], a[72:75], v[12:15]\n"
"ds_read_b64_tr_b16 v[198:199], v64 offset:47104\n"
"v_mfma_f32_16x16x32_bf16 v[12:15], v[172:175], a[76:79], v[12:15]\n"
"ds_read_b64_tr_b16 v[200:201], v65 offset:40960\n"
"s_waitcnt lgkmcnt(9)\n"
"v_mfma_f32_16x16x32_bf16 v[16:19], v[176:179], a[64:67], v[16:19]\n"
"ds_read_b64_tr_b16 v[202:203], v65 offset:43008\n"
"v_mfma_f32_16x16x32_bf16 v[16:19], v[180:183], a[68:71], v[16:19]\n"
"ds_read_b64_tr_b16 v[204:205], v65 offset:45056\n"
"buffer_load_dwordx4 v81, s[24:27], s60 offen offset:1024 lds\n"
"v_mfma_f32_16x16x32_bf16 v[20:23], v[176:179], a[72:75], v[20:23]\n"
"ds_read_b64_tr_b16 v[206:207], v65 offset:47104\n"
"v_mfma_f32_16x16x32_bf16 v[20:23], v[180:183], a[76:79], v[20:23]\n"
"ds_read_b64_tr_b16 v[160:161], v66 offset:40960\n"
"s_waitcnt lgkmcnt(9)\n"
"v_mfma_f32_16x16x32_bf16 v[24:27], v[184:187], a[64:67], v[24:27]\n"
"ds_read_b64_tr_b16 v[162:163], v66 offset:43008\n"
"v_mfma_f32_16x16x32_bf16 v[24:27], v[188:191], a[68:71], v[24:27]\n"
"ds_read_b64_tr_b16 v[164:165], v66 offset:45056\n"
"v_mfma_f32_16x16x32_bf16 v[28:31], v[184:187], a[72:75], v[28:31]\n"
"ds_read_b64_tr_b16 v[166:167], v66 offset:47104\n"
"v_mfma_f32_16x16x32_bf16 v[28:31], v[188:191], a[76:79], v[28:31]\n"
"ds_read_b64_tr_b16 v[168:169], v67 offset:40960\n"
"s_waitcnt vmcnt(16) lgkmcnt(9)\n"
"v_mfma_f32_16x16x32_bf16 v[0:3], v[192:195], a[80:83], v[0:3]\n"
"ds_read_b64_tr_b16 v[170:171], v67 offset:43008\n"
"s_add_u32 m0, s61, 106496\n"
"v_mfma_f32_16x16x32_bf16 v[0:3], v[196:199], a[84:87], v[0:3]\n"
"ds_read_b64_tr_b16 v[172:173], v67 offset:45056\n"
"s_mov_b32 s60, 229376\n"
"v_mfma_f32_16x16x32_bf16 v[4:7], v[192:195], a[88:91], v[4:7]\n"
"ds_read_b64_tr_b16 v[174:175], v67 offset:47104\n"
"buffer_load_dwordx4 v80, s[24:27], s60 offen offset:0 lds\n"
"v_mfma_f32_16x16x32_bf16 v[4:7], v[196:199], a[92:95], v[4:7]\n"
"ds_read_b64_tr_b16 v[176:177], v64 offset:49152\n"
"s_waitcnt lgkmcnt(9)\n"
"v_mfma_f32_16x16x32_bf16 v[8:11], v[200:203], a[80:83], v[8:11]\n"
"ds_read_b64_tr_b16 v[178:179], v64 offset:51200\n"
"v_mfma_f32_16x16x32_bf16 v[8:11], v[204:207], a[84:87], v[8:11]\n"
"ds_read_b64_tr_b16 v[180:181], v64 offset:53248\n"
"v_mfma_f32_16x16x32_bf16 v[12:15], v[200:203], a[88:91], v[12:15]\n"
"ds_read_b64_tr_b16 v[182:183], v64 offset:55296\n"
"v_mfma_f32_16x16x32_bf16 v[12:15], v[204:207], a[92:95], v[12:15]\n"
"ds_read_b64_tr_b16 v[184:185], v65 offset:49152\n"
"s_waitcnt lgkmcnt(9)\n"
"v_mfma_f32_16x16x32_bf16 v[16:19], v[160:163], a[80:83], v[16:19]\n"
"ds_read_b64_tr_b16 v[186:187], v65 offset:51200\n"
"buffer_load_dwordx4 v81, s[24:27], s60 offen offset:1024 lds\n"
"v_mfma_f32_16x16x32_bf16 v[16:19], v[164:167], a[84:87], v[16:19]\n"
"ds_read_b64_tr_b16 v[188:189], v65 offset:53248\n"
"v_mfma_f32_16x16x32_bf16 v[20:23], v[160:163], a[88:91], v[20:23]\n"
"ds_read_b64_tr_b16 v[190:191], v65 offset:55296\n"
"v_mfma_f32_16x16x32_bf16 v[20:23], v[164:167], a[92:95], v[20:23]\n"
"ds_read_b64_tr_b16 v[192:193], v66 offset:49152\n"
"s_waitcnt lgkmcnt(9)\n"
"v_mfma_f32_16x16x32_bf16 v[24:27], v[168:171], a[80:83], v[24:27]\n"
"ds_read_b64_tr_b16 v[194:195], v66 offset:51200\n"
"v_mfma_f32_16x16x32_bf16 v[24:27], v[172:175], a[84:87], v[24:27]\n"
"ds_read_b64_tr_b16 v[196:197], v66 offset:53248\n"
"v_mfma_f32_16x16x32_bf16 v[28:31], v[168:171], a[88:91], v[28:31]\n"
"ds_read_b64_tr_b16 v[198:199], v66 offset:55296\n"
"s_add_u32 m0, s61, 122880\n"
"v_mfma_f32_16x16x32_bf16 v[28:31], v[172:175], a[92:95], v[28:31]\n"
"ds_read_b64_tr_b16 v[200:201], v67 offset:49152\n"
"s_waitcnt vmcnt(11) lgkmcnt(9)\n"
"v_mfma_f32_16x16x32_bf16 v[0:3], v[176:179], a[96:99], v[0:3]\n"
"ds_read_b64_tr_b16 v[202:203], v67 offset:51200\n"
"s_mov_b32 s60, 0\n"
"v_mfma_f32_16x16x32_bf16 v[0:3], v[180:183], a[100:103], v[0:3]\n"
"ds_read_b64_tr_b16 v[204:205], v67 offset:53248\n"
"buffer_load_dwordx4 v78, s[24:27], s60 offen offset:0 lds\n"
"v_mfma_f32_16x16x32_bf16 v[4:7], v[176:179], a[104:107], v[4:7]\n"
"ds_read_b64_tr_b16 v[206:207], v67 offset:55296\n"
"v_mfma_f32_16x16x32_bf16 v[4:7], v[180:183], a[108:111], v[4:7]\n"
"ds_read_b64_tr_b16 v[160:161], v64 offset:57344\n"
"s_waitcnt lgkmcnt(9)\n"
"v_mfma_f32_16x16x32_bf16 v[8:11], v[184:187], a[96:99], v[8:11]\n"
"ds_read_b64_tr_b16 v[162:163], v64 offset:59392\n"
"v_mfma_f32_16x16x32_bf16 v[8:11], v[188:191], a[100:103], v[8:11]\n"
"ds_read_b64_tr_b16 v[164:165], v64 offset:61440\n"
"buffer_load_dwordx4 v79, s[24:27], s60 offen offset:1024 lds\n"
"v_mfma_f32_16x16x32_bf16 v[12:15], v[184:187], a[104:107], v[12:15]\n"
"ds_read_b64_tr_b16 v[166:167], v64 offset:63488\n"
"v_mfma_f32_16x16x32_bf16 v[12:15], v[188:191], a[108:111], v[12:15]\n"
"ds_read_b64_tr_b16 v[168:169], v65 offset:57344\n"
"s_waitcnt lgkmcnt(9)\n"
"v_mfma_f32_16x16x32_bf16 v[16:19], v[192:195], a[96:99], v[16:19]\n"
"ds_read_b64_tr_b16 v[170:171], v65 offset:59392\n"
"v_mfma_f32_16x16x32_bf16 v[16:19], v[196:199], a[100:103], v[16:19]\n"
"ds_read_b64_tr_b16 v[172:173], v65 offset:61440\n"
"v_mfma_f32_16x16x32_bf16 v[20:23], v[192:195], a[104:107], v[20:23]\n"
"ds_read_b64_tr_b16 v[174:175], v65 offset:63488\n"
"v_mfma_f32_16x16x32_bf16 v[20:23], v[196:199], a[108:111], v[20:23]\n"
"ds_read_b64_tr_b16 v[176:177], v66 offset:57344\n"
"s_waitcnt lgkmcnt(9)\n"
"v_mfma_f32_16x16x32_bf16 v[24:27], v[200:203], a[96:99], v[24:27]\n"
"ds_read_b64_tr_b16 v[178:179], v66 offset:59392\n"
"s_add_u32 m0, s61, 131072\n"
"v_mfma_f32_16x16x32_bf16 v[24:27], v[204:207], a[100:103], v[24:27]\n"
"ds_read_b64_tr_b16 v[180:181], v66 offset:61440\n"
"s_mov_b32 s60, 229376\n"
"v_mfma_f32_16x16x32_bf16 v[28:31], v[200:203], a[104:107], v[28:31]\n"
"ds_read_b64_tr_b16 v[182:183], v66 offset:63488\n"
"buffer_load_dwordx4 v78, s[24:27], s60 offen offset:0 lds\n"
"v_mfma_f32_16x16x32_bf16 v[28:31], v[204:207], a[108:111], v[28:31]\n"
"ds_read_b64_tr_b16 v[184:185], v67 offset:57344\n"
"s_waitcnt vmcnt(8) lgkmcnt(9)\n"
"v_mfma_f32_16x16x32_bf16 v[0:3], v[160:163], a[112:115], v[0:3]\n"
"ds_read_b64_tr_b16 v[186:187], v67 offset:59392\n"
"v_mfma_f32_16x16x32_bf16 v[0:3], v[164:167], a[116:119], v[0:3]\n"
"ds_read_b64_tr_b16 v[188:189], v67 offset:61440\n"
"v_mfma_f32_16x16x32_bf16 v[4:7], v[160:163], a[120:123], v[4:7]\n"
"ds_read_b64_tr_b16 v[190:191], v67 offset:63488\n"
"v_mfma_f32_16x16x32_bf16 v[4:7], v[164:167], a[124:127], v[4:7]\n"
"ds_read_b64_tr_b16 v[192:193], v68 offset:0\n"
"s_waitcnt lgkmcnt(9)\n"
"v_mfma_f32_16x16x32_bf16 v[8:11], v[168:171], a[112:115], v[8:11]\n"
"ds_read_b64_tr_b16 v[194:195], v68 offset:2048\n"
"buffer_load_dwordx4 v79, s[24:27], s60 offen offset:1024 lds\n"
"v_mfma_f32_16x16x32_bf16 v[8:11], v[172:175], a[116:119], v[8:11]\n"
"ds_read_b64_tr_b16 v[196:197], v68 offset:4096\n"
"v_cvt_pk_bf16_f32 v0, v0, v1\n"
"v_mfma_f32_16x16x32_bf16 v[12:15], v[168:171], a[120:123], v[12:15]\n"
"ds_read_b64_tr_b16 v[198:199], v68 offset:6144\n"
"v_cvt_pk_bf16_f32 v1, v2, v3\n"
"v_mfma_f32_16x16x32_bf16 v[12:15], v[172:175], a[124:127], v[12:15]\n"
"ds_read_b64_tr_b16 v[200:201], v69 offset:0\n"
"s_waitcnt lgkmcnt(9)\n"
"v_mfma_f32_16x16x32_bf16 v[16:19], v[176:179], a[112:115], v[16:19]\n"
"ds_read_b64_tr_b16 v[202:203], v69 offset:2048\n"
"v_cvt_pk_bf16_f32 v4, v4, v5\n"
"v_mfma_f32_16x16x32_bf16 v[16:19], v[180:183], a[116:119], v[16:19]\n"
"ds_read_b64_tr_b16 v[204:205], v69 offset:4096\n"
"v_cvt_pk_bf16_f32 v2, v8, v9\n"
"v_mfma_f32_16x16x32_bf16 v[20:23], v[176:179], a[120:123], v[20:23]\n"
"ds_read_b64_tr_b16 v[206:207], v69 offset:6144\n"
"v_cvt_pk_bf16_f32 v3, v10, v11\n"
"v_mfma_f32_16x16x32_bf16 v[20:23], v[180:183], a[124:127], v[20:23]\n"
"ds_read_b64_tr_b16 v[160:161], v70 offset:0\n"
"s_waitcnt lgkmcnt(9)\n"
"v_mfma_f32_16x16x32_bf16 v[24:27], v[184:187], a[112:115], v[24:27]\n"
"ds_read_b64_tr_b16 v[162:163], v70 offset:2048\n"
"buffer_store_dwordx4 v[0:3], v232, s[40:43], s71 offen offset:0 sc1\n"
"v_mfma_f32_16x16x32_bf16 v[24:27], v[188:191], a[116:119], v[24:27]\n"
"ds_read_b64_tr_b16 v[164:165], v70 offset:4096\n"
"v_cvt_pk_bf16_f32 v5, v6, v7\n"
"v_mfma_f32_16x16x32_bf16 v[28:31], v[184:187], a[120:123], v[28:31]\n"
"ds_read_b64_tr_b16 v[166:167], v70 offset:6144\n"
"v_cvt_pk_bf16_f32 v6, v12, v13\n"
"v_mfma_f32_16x16x32_bf16 v[28:31], v[188:191], a[124:127], v[28:31]\n"
"ds_read_b64_tr_b16 v[168:169], v71 offset:0\n"
"s_waitcnt lgkmcnt(9)\n"
"v_mfma_f32_16x16x32_bf16 v[32:35], v[192:195], a[64:67], v[32:35]\n"
"ds_read_b64_tr_b16 v[170:171], v71 offset:2048\n"
"v_cvt_pk_bf16_f32 v7, v14, v15\n"
"v_mfma_f32_16x16x32_bf16 v[32:35], v[196:199], a[68:71], v[32:35]\n"
"ds_read_b64_tr_b16 v[172:173], v71 offset:4096\n"
"buffer_store_dwordx4 v[4:7], v232, s[40:43], s72 offen offset:0 sc1\n"
"v_mfma_f32_16x16x32_bf16 v[36:39], v[192:195], a[72:75], v[36:39]\n"
"ds_read_b64_tr_b16 v[174:175], v71 offset:6144\n"
"v_cvt_pk_bf16_f32 v16, v16, v17\n"
"v_mfma_f32_16x16x32_bf16 v[36:39], v[196:199], a[76:79], v[36:39]\n"
"ds_read_b64_tr_b16 v[176:177], v68 offset:8192\n"
"s_waitcnt lgkmcnt(9)\n"
"v_mfma_f32_16x16x32_bf16 v[40:43], v[200:203], a[64:67], v[40:43]\n"
"ds_read_b64_tr_b16 v[178:179], v68 offset:10240\n"
"v_cvt_pk_bf16_f32 v17, v18, v19\n"
"v_mfma_f32_16x16x32_bf16 v[40:43], v[204:207], a[68:71], v[40:43]\n"
"ds_read_b64_tr_b16 v[180:181], v68 offset:12288\n"
"v_cvt_pk_bf16_f32 v18, v24, v25\n"
"v_mfma_f32_16x16x32_bf16 v[44:47], v[200:203], a[72:75], v[44:47]\n"
"ds_read_b64_tr_b16 v[182:183], v68 offset:14336\n"
"v_cvt_pk_bf16_f32 v19, v26, v27\n"
"v_mfma_f32_16x16x32_bf16 v[44:47], v[204:207], a[76:79], v[44:47]\n"
"ds_read_b64_tr_b16 v[184:185], v69 offset:8192\n"
"s_waitcnt lgkmcnt(9)\n"
"v_mfma_f32_16x16x32_bf16 v[48:51], v[160:163], a[64:67], v[48:51]\n"
"ds_read_b64_tr_b16 v[186:187], v69 offset:10240\n"
"buffer_store_dwordx4 v[16:19], v232, s[40:43], s71 offen offset:1024 sc1\n"
"v_mfma_f32_16x16x32_bf16 v[48:51], v[164:167], a[68:71], v[48:51]\n"
"ds_read_b64_tr_b16 v[188:189], v69 offset:12288\n"
"v_cvt_pk_bf16_f32 v20, v20, v21\n"
"v_mfma_f32_16x16x32_bf16 v[52:55], v[160:163], a[72:75], v[52:55]\n"
"ds_read_b64_tr_b16 v[190:191], v69 offset:14336\n"
"v_cvt_pk_bf16_f32 v21, v22, v23\n"
"v_mfma_f32_16x16x32_bf16 v[52:55], v[164:167], a[76:79], v[52:55]\n"
"ds_read_b64_tr_b16 v[192:193], v70 offset:8192\n"
"s_waitcnt lgkmcnt(9)\n"
"v_mfma_f32_16x16x32_bf16 v[56:59], v[168:171], a[64:67], v[56:59]\n"
"ds_read_b64_tr_b16 v[194:195], v70 offset:10240\n"
"v_cvt_pk_bf16_f32 v22, v28, v29\n"
"v_mfma_f32_16x16x32_bf16 v[56:59], v[172:175], a[68:71], v[56:59]\n"
"ds_read_b64_tr_b16 v[196:197], v70 offset:12288\n"
"v_cvt_pk_bf16_f32 v23, v30, v31\n"
"v_mfma_f32_16x16x32_bf16 v[60:63], v[168:171], a[72:75], v[60:63]\n"
"ds_read_b64_tr_b16 v[198:199], v70 offset:14336\n"
"buffer_store_dwordx4 v[20:23], v232, s[40:43], s72 offen offset:1024 sc1\n"
"v_mfma_f32_16x16x32_bf16 v[60:63], v[172:175], a[76:79], v[60:63]\n"
"ds_read_b64_tr_b16 v[200:201], v71 offset:8192\n"
"s_waitcnt lgkmcnt(9)\n"
"v_mfma_f32_16x16x32_bf16 v[32:35], v[176:179], a[80:83], v[32:35]\n"
"ds_read_b64_tr_b16 v[202:203], v71 offset:10240\n"
"v_mfma_f32_16x16x32_bf16 v[32:35], v[180:183], a[84:87], v[32:35]\n"
"ds_read_b64_tr_b16 v[204:205], v71 offset:12288\n"
"v_mfma_f32_16x16x32_bf16 v[36:39], v[176:179], a[88:91], v[36:39]\n"
"ds_read_b64_tr_b16 v[206:207], v71 offset:14336\n"
"v_mfma_f32_16x16x32_bf16 v[36:39], v[180:183], a[92:95], v[36:39]\n"
"ds_read_b64_tr_b16 v[160:161], v68 offset:16384\n"
"s_waitcnt lgkmcnt(9)\n"
"v_mfma_f32_16x16x32_bf16 v[40:43], v[184:187], a[80:83], v[40:43]\n"
"ds_read_b64_tr_b16 v[162:163], v68 offset:18432\n"
"v_mfma_f32_16x16x32_bf16 v[40:43], v[188:191], a[84:87], v[40:43]\n"
"ds_read_b64_tr_b16 v[164:165], v68 offset:20480\n"
"v_mfma_f32_16x16x32_bf16 v[44:47], v[184:187], a[88:91], v[44:47]\n"
"ds_read_b64_tr_b16 v[166:167], v68 offset:22528\n"
"v_mfma_f32_16x16x32_bf16 v[44:47], v[188:191], a[92:95], v[44:47]\n"
"ds_read_b64_tr_b16 v[168:169], v69 offset:16384\n"
"s_waitcnt lgkmcnt(9)\n"
"v_mfma_f32_16x16x32_bf16 v[48:51], v[192:195], a[80:83], v[48:51]\n"
"ds_read_b64_tr_b16 v[170:171], v69 offset:18432\n"
"v_mfma_f32_16x16x32_bf16 v[48:51], v[196:199], a[84:87], v[48:51]\n"
"ds_read_b64_tr_b16 v[172:173], v69 offset:20480\n"
"v_mfma_f32_16x16x32_bf16 v[52:55], v[192:195], a[88:91], v[52:55]\n"
"ds_read_b64_tr_b16 v[174:175], v69 offset:22528\n"
"v_mfma_f32_16x16x32_bf16 v[52:55], v[196:199], a[92:95], v[52:55]\n"
"ds_read_b64_tr_b16 v[176:177], v70 offset:16384\n"
"s_waitcnt lgkmcnt(9)\n"
"v_mfma_f32_16x16x32_bf16 v[56:59], v[200:203], a[80:83], v[56:59]\n"
"ds_read_b64_tr_b16 v[178:179], v70 offset:18432\n"
"v_mfma_f32_16x16x32_bf16 v[56:59], v[204:207], a[84:87], v[56:59]\n"
"ds_read_b64_tr_b16 v[180:181], v70 offset:20480\n"
"v_mfma_f32_16x16x32_bf16 v[60:63], v[200:203], a[88:91], v[60:63]\n"
"ds_read_b64_tr_b16 v[182:183], v70 offset:22528\n"
"v_mfma_f32_16x16x32_bf16 v[60:63], v[204:207], a[92:95], v[60:63]\n"
"ds_read_b64_tr_b16 v[184:185], v71 offset:16384\n"
"s_waitcnt lgkmcnt(9)\n"
"v_mfma_f32_16x16x32_bf16 v[32:35], v[160:163], a[96:99], v[32:35]\n"
"ds_read_b64_tr_b16 v[186:187], v71 offset:18432\n"
"v_mfma_f32_16x16x32_bf16 v[32:35], v[164:167], a[100:103], v[32:35]\n"
"ds_read_b64_tr_b16 v[188:189], v71 offset:20480\n"
"v_mfma_f32_16x16x32_bf16 v[36:39], v[160:163], a[104:107], v[36:39]\n"
"ds_read_b64_tr_b16 v[190:191], v71 offset:22528\n"
"v_mfma_f32_16x16x32_bf16 v[36:39], v[164:167], a[108:111], v[36:39]\n"
"ds_read_b64_tr_b16 v[192:193], v68 offset:24576\n"
"s_waitcnt lgkmcnt(9)\n"
"v_mfma_f32_16x16x32_bf16 v[40:43], v[168:171], a[96:99], v[40:43]\n"
"ds_read_b64_tr_b16 v[194:195], v68 offset:26624\n"
"v_mfma_f32_16x16x32_bf16 v[40:43], v[172:175], a[100:103], v[40:43]\n"
"ds_read_b64_tr_b16 v[196:197], v68 offset:28672\n"
"v_mfma_f32_16x16x32_bf16 v[44:47], v[168:171], a[104:107], v[44:47]\n"
"ds_read_b64_tr_b16 v[198:199], v68 offset:30720\n"
"v_mfma_f32_16x16x32_bf16 v[44:47], v[172:175], a[108:111], v[44:47]\n"
"ds_read_b64_tr_b16 v[200:201], v69 offset:24576\n"
"s_waitcnt lgkmcnt(9)\n"
"v_mfma_f32_16x16x32_bf16 v[48:51], v[176:179], a[96:99], v[48:51]\n"
"ds_read_b64_tr_b16 v[202:203], v69 offset:26624\n"
"v_mfma_f32_16x16x32_bf16 v[48:51], v[180:183], a[100:103], v[48:51]\n"
"ds_read_b64_tr_b16 v[204:205], v69 offset:28672\n"
"v_mfma_f32_16x16x32_bf16 v[52:55], v[176:179], a[104:107], v[52:55]\n"
"ds_read_b64_tr_b16 v[206:207], v69 offset:30720\n"
"v_mfma_f32_16x16x32_bf16 v[52:55], v[180:183], a[108:111], v[52:55]\n"
"ds_read_b64_tr_b16 v[160:161], v70 offset:24576\n"
"s_waitcnt lgkmcnt(9)\n"
"v_mfma_f32_16x16x32_bf16 v[56:59], v[184:187], a[96:99], v[56:59]\n"
"ds_read_b64_tr_b16 v[162:163], v70 offset:26624\n"
"v_mfma_f32_16x16x32_bf16 v[56:59], v[188:191], a[100:103], v[56:59]\n"
"ds_read_b64_tr_b16 v[164:165], v70 offset:28672\n"
"v_mfma_f32_16x16x32_bf16 v[60:63], v[184:187], a[104:107], v[60:63]\n"
"ds_read_b64_tr_b16 v[166:167], v70 offset:30720\n"
"v_mfma_f32_16x16x32_bf16 v[60:63], v[188:191], a[108:111], v[60:63]\n"
"ds_read_b64_tr_b16 v[168:169], v71 offset:24576\n"
"s_waitcnt lgkmcnt(9)\n"
"v_mfma_f32_16x16x32_bf16 v[32:35], v[192:195], a[112:115], v[32:35]\n"
"ds_read_b64_tr_b16 v[170:171], v71 offset:26624\n"
"v_mfma_f32_16x16x32_bf16 v[32:35], v[196:199], a[116:119], v[32:35]\n"
"ds_read_b64_tr_b16 v[172:173], v71 offset:28672\n"
"v_mfma_f32_16x16x32_bf16 v[36:39], v[192:195], a[120:123], v[36:39]\n"
"ds_read_b64_tr_b16 v[174:175], v71 offset:30720\n"
"v_mfma_f32_16x16x32_bf16 v[36:39], v[196:199], a[124:127], v[36:39]\n"
"s_waitcnt lgkmcnt(8)\n"
"v_mfma_f32_16x16x32_bf16 v[40:43], v[200:203], a[112:115], v[40:43]\n"
"v_mfma_f32_16x16x32_bf16 v[40:43], v[204:207], a[116:119], v[40:43]\n"
"v_mfma_f32_16x16x32_bf16 v[44:47], v[200:203], a[120:123], v[44:47]\n"
"v_mfma_f32_16x16x32_bf16 v[44:47], v[204:207], a[124:127], v[44:47]\n"
"v_cvt_pk_bf16_f32 v32, v32, v33\n"
"s_waitcnt lgkmcnt(4)\n"
"v_mfma_f32_16x16x32_bf16 v[48:51], v[160:163], a[112:115], v[48:51]\n"
"v_cvt_pk_bf16_f32 v33, v34, v35\n"
"v_mfma_f32_16x16x32_bf16 v[48:51], v[164:167], a[116:119], v[48:51]\n"
"v_cvt_pk_bf16_f32 v36, v36, v37\n"
"v_cvt_pk_bf16_f32 v37, v38, v39\n"
"v_mfma_f32_16x16x32_bf16 v[52:55], v[160:163], a[120:123], v[52:55]\n"
"v_cvt_pk_bf16_f32 v34, v40, v41\n"
"v_cvt_pk_bf16_f32 v35, v42, v43\n"
"v_mfma_f32_16x16x32_bf16 v[52:55], v[164:167], a[124:127], v[52:55]\n"
"buffer_store_dwordx4 v[32:35], v232, s[40:43], s71 offen offset:2048 sc1\n"
"s_waitcnt lgkmcnt(0)\n"
"v_mfma_f32_16x16x32_bf16 v[56:59], v[168:171], a[112:115], v[56:59]\n"
"v_cvt_pk_bf16_f32 v38, v44, v45\n"
"v_cvt_pk_bf16_f32 v39, v46, v47\n"
"v_mfma_f32_16x16x32_bf16 v[56:59], v[172:175], a[116:119], v[56:59]\n"
"buffer_store_dwordx4 v[36:39], v232, s[40:43], s72 offen offset:2048 sc1\n"
"v_cvt_pk_bf16_f32 v48, v48, v49\n"
"v_mfma_f32_16x16x32_bf16 v[60:63], v[168:171], a[120:123], v[60:63]\n"
"v_cvt_pk_bf16_f32 v49, v50, v51\n"
"v_mfma_f32_16x16x32_bf16 v[60:63], v[172:175], a[124:127], v[60:63]\n"
"v_cvt_pk_bf16_f32 v52, v52, v53\n"
"v_cvt_pk_bf16_f32 v53, v54, v55\n"
"s_nop 0\n"
"v_cvt_pk_bf16_f32 v50, v56, v57\n"
"v_cvt_pk_bf16_f32 v51, v58, v59\n"
"buffer_store_dwordx4 v[48:51], v232, s[40:43], s71 offen offset:3072 sc1\n"
"s_nop 1\n"
"v_cvt_pk_bf16_f32 v54, v60, v61\n"
"v_cvt_pk_bf16_f32 v55, v62, v63\n"
"buffer_store_dwordx4 v[52:55], v232, s[40:43], s72 offen offset:3072 sc1\n"
"s_barrier\n"
"s_waitcnt vmcnt(0)\n"
"buffer_store_dwordx2 v[220:221], v237, s[40:43], s34 offen sc1\n"
"s_add_u32 m0, s61, 0\n"
"s_mov_b32 s60, 458752\n"
"buffer_load_dwordx4 v78, s[24:27], s60 offen offset:0 lds\n"
"buffer_load_dwordx4 v79, s[24:27], s60 offen offset:1024 lds\n"
"s_add_u32 m0, s61, 8192\n"
"s_mov_b32 s60, 688128\n"
"buffer_load_dwordx4 v78, s[24:27], s60 offen offset:0 lds\n"
"buffer_load_dwordx4 v79, s[24:27], s60 offen offset:1024 lds\n"
"s_add_u32 m0, s61, 40960\n"
"s_mov_b32 s60, 917504\n"
"buffer_load_dwordx4 v78, s[24:27], s60 offen offset:0 lds\n"
"buffer_load_dwordx4 v79, s[24:27], s60 offen offset:1024 lds\n"
"s_add_u32 m0, s61, 49152\n"
"s_mov_b32 s60, 1146880\n"
"buffer_load_dwordx4 v78, s[24:27], s60 offen offset:0 lds\n"
"buffer_load_dwordx4 v79, s[24:27], s60 offen offset:1024 lds\n"
"s_add_u32 m0, s61, 81920\n"
"s_mov_b32 s60, 1376256\n"
"buffer_load_dwordx4 v78, s[24:27], s60 offen offset:0 lds\n"
"buffer_load_dwordx4 v79, s[24:27], s60 offen offset:1024 lds\n"
"s_add_u32 m0, s61, 90112\n"
"s_mov_b32 s60, 1605632\n"
"buffer_load_dwordx4 v78, s[24:27], s60 offen offset:0 lds\n"
"buffer_load_dwordx4 v79, s[24:27], s60 offen offset:1024 lds\n"
"s_add_u32 m0, s61, 16384\n"
"s_mov_b32 s60, 458752\n"
"buffer_load_dwordx4 v80, s[24:27], s60 offen offset:0 lds\n"
"buffer_load_dwordx4 v81, s[24:27], s60 offen offset:1024 lds\n"
"s_add_u32 m0, s61, 24576\n"
"s_mov_b32 s60, 688128\n"
"buffer_load_dwordx4 v80, s[24:27], s60 offen offset:0 lds\n"
"buffer_load_dwordx4 v81, s[24:27], s60 offen offset:1024 lds\n"
"s_add_u32 m0, s61, 57344\n"
"s_mov_b32 s60, 917504\n"
"buffer_load_dwordx4 v80, s[24:27], s60 offen offset:0 lds\n"
"buffer_load_dwordx4 v81, s[24:27], s60 offen offset:1024 lds\n"
"s_add_u32 m0, s61, 65536\n"
"s_mov_b32 s60, 1146880\n"
"buffer_load_dwordx4 v80, s[24:27], s60 offen offset:0 lds\n"
"buffer_load_dwordx4 v81, s[24:27], s60 offen offset:1024 lds\n"
"s_add_u32 m0, s61, 139264\n"
"s_mov_b32 s60, 1376256\n"
"buffer_load_dwordx4 v80, s[24:27], s60 offen offset:0 lds\n"
"buffer_load_dwordx4 v81, s[24:27], s60 offen offset:1024 lds\n"
"s_add_u32 m0, s61, 147456\n"
"s_mov_b32 s60, 1605632\n"
"buffer_load_dwordx4 v80, s[24:27], s60 offen offset:0 lds\n"
"buffer_load_dwordx4 v81, s[24:27], s60 offen offset:1024 lds\n"
"s_mov_b64 exec, 0xf\n"
"v_mov_b64 v[222:223], 0\n"
"buffer_atomic_add_x2 v[222:223], v236, s[40:43], s35 offen sc0 sc1\n"
"s_mov_b64 exec, -1\n"
"s_waitcnt vmcnt(0)\n"
"v_cmp_ne_u64 vcc, s[6:7], v[222:223]\n"
"s_nop 0\n"
"s_cbranch_vccnz Lpp_%=\n"
"Lpp_ok_%=:\n"
"buffer_load_dwordx4 v[160:163], v82, s[40:43], s73 offen offset:0 sc1\n"
"buffer_load_dwordx4 v[164:167], v82, s[40:43], s73 offen offset:2048 sc1\n"
"s_add_u32 s73, s73, 4096\n"
"buffer_load_dwordx4 v[168:171], v82, s[40:43], s73 offen offset:0 sc1\n"
"buffer_load_dwordx4 v[172:175], v82, s[40:43], s73 offen offset:2048 sc1\n"
"s_add_u32 s73, s73, 4096\n"
"buffer_load_dwordx4 v[176:179], v82, s[40:43], s73 offen offset:0 sc1\n"
"buffer_load_dwordx4 v[180:183], v82, s[40:43], s73 offen offset:2048 sc1\n"
"s_add_u32 s73, s73, 4096\n"
"buffer_load_dwordx4 v[184:187], v82, s[40:43], s73 offen offset:0 sc1\n"
"buffer_load_dwordx4 v[188:191], v82, s[40:43], s73 offen offset:2048 sc1\n"
"s_lshl_b32 s34, s65, 14\n"
"s_lshl_b32 s15, s64, 10\n"
"s_add_u32 s34, s34, s15\n"
"s_lshl_b32 s15, s11, 8\n"
"s_add_u32 s34, s34, s15\n"
"s_add_u32 s34, s34, 13369344\n"
"s_lshl_b32 s35, s65, 14\n"
"s_lshl_b32 s15, s11, 12\n"
"s_add_u32 s35, s35, s15\n"
"s_add_u32 s35, s35, 13369344\n"
"v_lshlrev_b32 v236, 4, v232\n"
"v_and_b32 v236, 0xf00, v236\n"
"s_waitcnt vmcnt(7)\n"
"v_lshlrev_b32 v0, 16, v160\n"
"v_and_b32 v1, 0xffff0000, v160\n"
"v_lshlrev_b32 v2, 16, v161\n"
"v_and_b32 v3, 0xffff0000, v161\n"
"v_lshlrev_b32 v4, 16, v162\n"
"v_and_b32 v5, 0xffff0000, v162\n"
"v_lshlrev_b32 v6, 16, v163\n"
"v_and_b32 v7, 0xffff0000, v163\n"
"s_waitcnt vmcnt(6)\n"
"v_lshlrev_b32 v8, 16, v164\n"
"v_and_b32 v9, 0xffff0000, v164\n"
"v_lshlrev_b32 v10, 16, v165\n"
"v_and_b32 v11, 0xffff0000, v165\n"
"v_lshlrev_b32 v12, 16, v166\n"
"v_and_b32 v13, 0xffff0000, v166\n"
"v_lshlrev_b32 v14, 16, v167\n"
"v_and_b32 v15, 0xffff0000, v167\n"
"s_waitcnt vmcnt(5)\n"
"v_lshlrev_b32 v16, 16, v168\n"
"v_and_b32 v17, 0xffff0000, v168\n"
"v_pk_add_f32 v[0:1], v[0:1], v[16:17]\n"
"v_lshlrev_b32 v18, 16, v169\n"
"v_and_b32 v19, 0xffff0000, v169\n"
"v_pk_add_f32 v[2:3], v[2:3], v[18:19]\n"
"v_lshlrev_b32 v20, 16, v170\n"
"v_and_b32 v21, 0xffff0000, v170\n"
"v_pk_add_f32 v[4:5], v[4:5], v[20:21]\n"
"v_lshlrev_b32 v22, 16, v171\n"
"v_and_b32 v23, 0xffff0000, v171\n"
"v_pk_add_f32 v[6:7], v[6:7], v[22:23]\n"
"s_waitcnt vmcnt(4)\n"
"v_lshlrev_b32 v24, 16, v172\n"
"v_and_b32 v25, 0xffff0000, v172\n"
"v_pk_add_f32 v[8:9], v[8:9], v[24:25]\n"
"v_lshlrev_b32 v26, 16, v173\n"
"v_and_b32 v27, 0xffff0000, v173\n"
"v_pk_add_f32 v[10:11], v[10:11], v[26:27]\n"
"v_lshlrev_b32 v28, 16, v174\n"
"v_and_b32 v29, 0xffff0000, v174\n"
"v_pk_add_f32 v[12:13], v[12:13], v[28:29]\n"
"v_lshlrev_b32 v30, 16, v175\n"
"v_and_b32 v31, 0xffff0000, v175\n"
"v_pk_add_f32 v[14:15], v[14:15], v[30:31]\n"
"s_waitcnt vmcnt(3)\n"
"v_lshlrev_b32 v16, 16, v176\n"
"v_and_b32 v17, 0xffff0000, v176\n"
"v_pk_add_f32 v[0:1], v[0:1], v[16:17]\n"
"v_lshlrev_b32 v18, 16, v177\n"
"v_and_b32 v19, 0xffff0000, v177\n"
"v_pk_add_f32 v[2:3], v[2:3], v[18:19]\n"
"v_lshlrev_b32 v20, 16, v178\n"
"v_and_b32 v21, 0xffff0000, v178\n"
"v_pk_add_f32 v[4:5], v[4:5], v[20:21]\n"
"v_lshlrev_b32 v22, 16, v179\n"
"v_and_b32 v23, 0xffff0000, v179\n"
"v_pk_add_f32 v[6:7], v[6:7], v[22:23]\n"
"s_waitcnt vmcnt(2)\n"
"v_lshlrev_b32 v24, 16, v180\n"
"v_and_b32 v25, 0xffff0000, v180\n"
"v_pk_add_f32 v[8:9], v[8:9], v[24:25]\n"
"v_lshlrev_b32 v26, 16, v181\n"
"v_and_b32 v27, 0xffff0000, v181\n"
"v_pk_add_f32 v[10:11], v[10:11], v[26:27]\n"
"v_lshlrev_b32 v28, 16, v182\n"
"v_and_b32 v29, 0xffff0000, v182\n"
"v_pk_add_f32 v[12:13], v[12:13], v[28:29]\n"
"v_lshlrev_b32 v30, 16, v183\n"
"v_and_b32 v31, 0xffff0000, v183\n"
"v_pk_add_f32 v[14:15], v[14:15], v[30:31]\n"
"s_waitcnt vmcnt(1)\n"
"v_lshlrev_b32 v16, 16, v184\n"
"v_and_b32 v17, 0xffff0000, v184\n"
"v_pk_add_f32 v[0:1], v[0:1], v[16:17]\n"
"v_lshlrev_b32 v18, 16, v185\n"
"v_and_b32 v19, 0xffff0000, v185\n"
"v_pk_add_f32 v[2:3], v[2:3], v[18:19]\n"
"v_lshlrev_b32 v20, 16, v186\n"
"v_and_b32 v21, 0xffff0000, v186\n"
"v_pk_add_f32 v[4:5], v[4:5], v[20:21]\n"
"v_lshlrev_b32 v22, 16, v187\n"
"v_and_b32 v23, 0xffff0000, v187\n"
"v_pk_add_f32 v[6:7], v[6:7], v[22:23]\n"
"s_waitcnt vmcnt(0)\n"
"v_lshlrev_b32 v24, 16, v188\n"
"v_and_b32 v25, 0xffff0000, v188\n"
"v_pk_add_f32 v[8:9], v[8:9], v[24:25]\n"
"v_lshlrev_b32 v26, 16, v189\n"
"v_and_b32 v27, 0xffff0000, v189\n"
"v_pk_add_f32 v[10:11], v[10:11], v[26:27]\n"
"v_lshlrev_b32 v28, 16, v190\n"
"v_and_b32 v29, 0xffff0000, v190\n"
"v_pk_add_f32 v[12:13], v[12:13], v[28:29]\n"
"v_lshlrev_b32 v30, 16, v191\n"
"v_and_b32 v31, 0xffff0000, v191\n"
"v_pk_add_f32 v[14:15], v[14:15], v[30:31]\n"
"v_pk_fma_f32 v[240:241], v[96:97], v[0:1], v[240:241]\n"
"v_pk_fma_f32 v[242:243], v[98:99], v[2:3], v[242:243]\n"
"v_pk_fma_f32 v[244:245], v[100:101], v[4:5], v[244:245]\n"
"v_pk_fma_f32 v[246:247], v[102:103], v[6:7], v[246:247]\n"
"v_pk_fma_f32 v[248:249], v[104:105], v[8:9], v[248:249]\n"
"v_pk_fma_f32 v[250:251], v[106:107], v[10:11], v[250:251]\n"
"v_pk_fma_f32 v[252:253], v[108:109], v[12:13], v[252:253]\n"
"v_pk_fma_f32 v[254:255], v[110:111], v[14:15], v[254:255]\n"
"v_pk_add_f32 v[16:17], v[240:241], v[242:243]\n"
"v_pk_add_f32 v[18:19], v[244:245], v[246:247]\n"
"v_pk_add_f32 v[16:17], v[16:17], v[248:249]\n"
"v_pk_add_f32 v[18:19], v[18:19], v[250:251]\n"
"v_pk_add_f32 v[16:17], v[16:17], v[252:253]\n"
"v_pk_add_f32 v[18:19], v[18:19], v[254:255]\n"
"v_pk_add_f32 v[16:17], v[16:17], v[18:19]\n"
"v_add_f32 v16, v16, v17\n"
"s_nop 1\n"
"v_add_f32_dpp v16, v16, v16 row_ror:8 row_mask:0xf bank_mask:0xf\n"
"v_mov_b32 v17, v16\n"
"s_nop 1\n"
"v_permlane16_swap_b32 v16, v17\n"
"v_add_f32 v16, v16, v17\n"
"v_mov_b32 v17, v16\n"
"s_nop 1\n"
"v_permlane32_swap_b32 v16, v17\n"
"v_add_f32 v16, v16, v17\n"
"v_mul_f32 v16, 0x3c000000, v16\n"
"v_pk_add_f32 v[0:1], v[240:241], v[16:17] op_sel_hi:[1,0] neg_lo:[0,1] neg_hi:[0,1]\n"
"v_pk_add_f32 v[2:3], v[242:243], v[16:17] op_sel_hi:[1,0] neg_lo:[0,1] neg_hi:[0,1]\n"
"v_pk_add_f32 v[4:5], v[244:245], v[16:17] op_sel_hi:[1,0] neg_lo:[0,1] neg_hi:[0,1]\n"
"v_pk_add_f32 v[6:7], v[246:247], v[16:17] op_sel_hi:[1,0] neg_lo:[0,1] neg_hi:[0,1]\n"
"v_pk_add_f32 v[8:9], v[248:249], v[16:17] op_sel_hi:[1,0] neg_lo:[0,1] neg_hi:[0,1]\n"
"v_pk_add_f32 v[10:11], v[250:251], v[16:17] op_sel_hi:[1,0] neg_lo:[0,1] neg_hi:[0,1]\n"
"v_pk_add_f32 v[12:13], v[252:253], v[16:17] op_sel_hi:[1,0] neg_lo:[0,1] neg_hi:[0,1]\n"
"v_pk_add_f32 v[14:15], v[254:255], v[16:17] op_sel_hi:[1,0] neg_lo:[0,1] neg_hi:[0,1]\n"
"v_pk_mul_f32 v[18:19], v[0:1], v[0:1]\n"
"v_pk_mul_f32 v[20:21], v[2:3], v[2:3]\n"
"v_pk_fma_f32 v[18:19], v[4:5], v[4:5], v[18:19]\n"
"v_pk_fma_f32 v[20:21], v[6:7], v[6:7], v[20:21]\n"
"v_pk_fma_f32 v[18:19], v[8:9], v[8:9], v[18:19]\n"
"v_pk_fma_f32 v[20:21], v[10:11], v[10:11], v[20:21]\n"
"v_pk_fma_f32 v[18:19], v[12:13], v[12:13], v[18:19]\n"
"v_pk_fma_f32 v[20:21], v[14:15], v[14:15], v[20:21]\n"
"v_pk_add_f32 v[18:19], v[18:19], v[20:21]\n"
"v_add_f32 v17, v18, v19\n"
"s_nop 1\n"
"v_add_f32_dpp v17, v17, v17 row_ror:8 row_mask:0xf bank_mask:0xf\n"
"v_mov_b32 v18, v17\n"
"s_nop 1\n"
"v_permlane16_swap_b32 v17, v18\n"
"v_add_f32 v17, v17, v18\n"
"v_mov_b32 v18, v17\n"
"s_nop 1\n"
"v_permlane32_swap_b32 v17, v18\n"
"v_add_f32 v17, v17, v18\n"
"ds_write_b64 v83, v[16:17]\n"
"v_cvt_pk_bf16_f32 v32, v240, v241\n"
"v_cvt_pk_bf16_f32 v33, v242, v243\n"
"v_cvt_pk_bf16_f32 v34, v244, v245\n"
"v_cvt_pk_bf16_f32 v35, v246, v247\n"
"v_cvt_pk_bf16_f32 v36, v248, v249\n"
"v_cvt_pk_bf16_f32 v37, v250, v251\n"
"v_cvt_pk_bf16_f32 v38, v252, v253\n"
"v_cvt_pk_bf16_f32 v39, v254, v255\n"
"buffer_store_dwordx4 v[32:35], v82, s[40:43], s75 offen offset:0 sc1\n"
"buffer_store_dwordx4 v[36:39], v82, s[40:43], s75 offen offset:2048 sc1\n"
"s_waitcnt lgkmcnt(0)\n"
"s_barrier\n"
"ds_read_b64 v[20:21], v85 offset:0\n"
"ds_read_b64 v[22:23], v85 offset:40960\n"
"ds_read_b64 v[24:25], v84 offset:0\n"
"ds_read_b64 v[26:27], v84 offset:40960\n"
"s_waitcnt lgkmcnt(2)\n"
"v_pk_add_f32 v[28:29], v[20:21], v[22:23]\n"
"s_waitcnt lgkmcnt(0)\n"
"v_pk_add_f32 v[30:31], v[24:25], v[26:27]\n"
"v_pk_add_f32 v[28:29], v[28:29], v[30:31]\n"
"v_mul_f32 v28, 0x3e800000, v28\n"
"v_sub_f32 v20, v20, v28\n"
"v_sub_f32 v22, v22, v28\n"
"v_sub_f32 v24, v24, v28\n"
"v_sub_f32 v26, v26, v28\n"
"v_mul_f32 v30, v20, v20\n"
"v_fmac_f32 v30, v22, v22\n"
"v_fmac_f32 v30, v24, v24\n"
"v_fmac_f32 v30, v26, v26\n"
"v_mul_f32 v29, 0x3b000000, v29\n"
"v_fmac_f32 v29, 0x3e800000, v30\n"
"v_add_f32 v29, 0x3727c5ac, v29\n"
"v_rsq_f32 v18, v29\n"
"v_sub_f32 v16, v16, v28\n"
"v_mul_f32 v16, v16, v18\n"
"v_pk_fma_f32 v[0:1], v[0:1], v[18:19], v[16:17] op_sel_hi:[1,0,0]\n"
"v_pk_fma_f32 v[2:3], v[2:3], v[18:19], v[16:17] op_sel_hi:[1,0,0]\n"
"v_pk_fma_f32 v[4:5], v[4:5], v[18:19], v[16:17] op_sel_hi:[1,0,0]\n"
"v_pk_fma_f32 v[6:7], v[6:7], v[18:19], v[16:17] op_sel_hi:[1,0,0]\n"
"v_pk_fma_f32 v[8:9], v[8:9], v[18:19], v[16:17] op_sel_hi:[1,0,0]\n"
"v_pk_fma_f32 v[10:11], v[10:11], v[18:19], v[16:17] op_sel_hi:[1,0,0]\n"
"v_pk_fma_f32 v[12:13], v[12:13], v[18:19], v[16:17] op_sel_hi:[1,0,0]\n"
"v_pk_fma_f32 v[14:15], v[14:15], v[18:19], v[16:17] op_sel_hi:[1,0,0]\n"
"v_pk_fma_f32 v[0:1], v[0:1], v[112:113], v[128:129]\n"
"v_pk_fma_f32 v[2:3], v[2:3], v[114:115], v[130:131]\n"
"v_pk_fma_f32 v[4:5], v[4:5], v[116:117], v[132:133]\n"
"v_pk_fma_f32 v[6:7], v[6:7], v[118:119], v[134:135]\n"
"v_pk_fma_f32 v[8:9], v[8:9], v[120:121], v[136:137]\n"
"v_pk_fma_f32 v[10:11], v[10:11], v[122:123], v[138:139]\n"
"v_pk_fma_f32 v[12:13], v[12:13], v[124:125], v[140:141]\n"
"v_pk_fma_f32 v[14:15], v[14:15], v[126:127], v[142:143]\n"
"v_cvt_pk_bf16_f32 v0, v0, v1\n"
"v_cvt_pk_bf16_f32 v1, v2, v3\n"
"v_cvt_pk_bf16_f32 v2, v4, v5\n"
"v_cvt_pk_bf16_f32 v3, v6, v7\n"
"buffer_store_dwordx4 v[0:3], v82, s[40:43], s74 offen offset:0 sc1\n"
"v_cvt_pk_bf16_f32 v8, v8, v9\n"
"v_cvt_pk_bf16_f32 v9, v10, v11\n"
"v_cvt_pk_bf16_f32 v10, v12, v13\n"
"v_cvt_pk_bf16_f32 v11, v14, v15\n"
"buffer_store_dwordx4 v[8:11], v82, s[40:43], s74 offen offset:2048 sc1\n"
"s_barrier\n"
"s_lshl_b32 s78, s11, 18\n"
"s_mul_i32 s79, s11, 196608\n"
"s_add_u32 s79, s79, 1048576\n"
"v_lshlrev_b32 v88, 16, v144\n"
"v_and_b32 v89, 0xffff0000, v144\n"
"v_lshlrev_b32 v90, 16, v145\n"
"v_and_b32 v91, 0xffff0000, v145\n"
"v_lshlrev_b32 v92, 16, v146\n"
"v_and_b32 v93, 0xffff0000, v146\n"
"v_lshlrev_b32 v94, 16, v147\n"
"v_and_b32 v95, 0xffff0000, v147\n"
"v_lshlrev_b32 v96, 16, v148\n"
"v_and_b32 v97, 0xffff0000, v148\n"
"v_lshlrev_b32 v98, 16, v149\n"
"v_and_b32 v99, 0xffff0000, v149\n"
"v_lshlrev_b32 v100, 16, v150\n"
"v_and_b32 v101, 0xffff0000, v150\n"
"v_lshlrev_b32 v102, 16, v151\n"
"v_and_b32 v103, 0xffff0000, v151\n"
"v_lshlrev_b32 v104, 16, v152\n"
"v_and_b32 v105, 0xffff0000, v152\n"
"v_lshlrev_b32 v106, 16, v153\n"
"v_and_b32 v107, 0xffff0000, v153\n"
"v_lshlrev_b32 v108, 16, v154\n"
"v_and_b32 v109, 0xffff0000, v154\n"
"v_lshlrev_b32 v110, 16, v155\n"
"v_and_b32 v111, 0xffff0000, v155\n"
"v_lshlrev_b32 v112, 16, v156\n"
"v_and_b32 v113, 0xffff0000, v156\n"
"v_lshlrev_b32 v114, 16, v157\n"
"v_and_b32 v115, 0xffff0000, v157\n"
"v_mov_b32 v78, 0x3fb8aa3b\n"
"v_pk_mul_f32 v[88:89], v[88:89], v[78:79] op_sel_hi:[1,0]\n"
"v_pk_mul_f32 v[90:91], v[90:91], v[78:79] op_sel_hi:[1,0]\n"
"v_pk_mul_f32 v[92:93], v[92:93], v[78:79] op_sel_hi:[1,0]\n"
"v_pk_mul_f32 v[94:95], v[94:95], v[78:79] op_sel_hi:[1,0]\n"
"v_pk_mul_f32 v[96:97], v[96:97], v[78:79] op_sel_hi:[1,0]\n"
"v_pk_mul_f32 v[98:99], v[98:99], v[78:79] op_sel_hi:[1,0]\n"
"v_pk_mul_f32 v[100:101], v[100:101], v[78:79] op_sel_hi:[1,0]\n"
"v_pk_mul_f32 v[102:103], v[102:103], v[78:79] op_sel_hi:[1,0]\n"
"v_pk_mul_f32 v[104:105], v[104:105], v[78:79] op_sel_hi:[1,0]\n"
"v_pk_mul_f32 v[106:107], v[106:107], v[78:79] op_sel_hi:[1,0]\n"
"v_pk_mul_f32 v[108:109], v[108:109], v[78:79] op_sel_hi:[1,0]\n"
"v_pk_mul_f32 v[110:111], v[110:111], v[78:79] op_sel_hi:[1,0]\n"
"v_pk_mul_f32 v[112:113], v[112:113], v[78:79] op_sel_hi:[1,0]\n"
"v_pk_mul_f32 v[114:115], v[114:115], v[78:79] op_sel_hi:[1,0]\n"
"s_waitcnt vmcnt(0)\n"
"buffer_store_dwordx2 v[220:221], v237, s[40:43], s34 offen sc1\n"
"s_add_u32 m0, s58, 32768\n"
"s_add_u32 s60, s78, 0\n"
"buffer_load_dwordx4 v228, s[28:31], s60 offen offset:0 lds\n"
"buffer_load_dwordx4 v229, s[28:31], s60 offen offset:1024 lds\n"
"buffer_load_dwordx4 v230, s[28:31], s60 offen offset:2048 lds\n"
"buffer_load_dwordx4 v231, s[28:31], s60 offen offset:3072 lds\n"
"s_add_u32 m0, s58, 36864\n"
"s_add_u32 s60, s78, 32768\n"
"buffer_load_dwordx4 v228, s[28:31], s60 offen offset:0 lds\n"
"buffer_load_dwordx4 v229, s[28:31], s60 offen offset:1024 lds\n"
"buffer_load_dwordx4 v230, s[28:31], s60 offen offset:2048 lds\n"
"buffer_load_dwordx4 v231, s[28:31], s60 offen offset:3072 lds\n"
"v_mov_b32 v76, v232\n"
"v_add_u32 v77, 0x4000, v232\n"
"v_lshrrev_b32 v78, 1, v232\n"
"s_lshl_b32 s68, s65, 17\n"
"s_lshl_b32 s15, s11, 15\n"
"s_add_u32 s68, s68, s15\n"
"s_add_u32 s68, s68, 54525952\n"
"s_add_u32 s69, s68, 4096\n"
"s_add_u32 s70, s68, 8192\n"
"s_add_u32 s71, s68, 12288\n"
"s_waitcnt vmcnt(0)\n"
"ds_read_b64_tr_b16 a[128:129], v224 offset:32768\n"
"ds_read_b64_tr_b16 a[130:131], v224 offset:34816\n"
"ds_read_b64_tr_b16 a[132:133], v224 offset:36864\n"
"ds_read_b64_tr_b16 a[134:135], v224 offset:38912\n"
"ds_read_b64_tr_b16 a[136:137], v225 offset:32768\n"
"ds_read_b64_tr_b16 a[138:139], v225 offset:34816\n"
"ds_read_b64_tr_b16 a[140:141], v225 offset:36864\n"
"ds_read_b64_tr_b16 a[142:143], v225 offset:38912\n"
"ds_read_b64_tr_b16 a[144:145], v226 offset:32768\n"
"ds_read_b64_tr_b16 a[146:147], v226 offset:34816\n"
"ds_read_b64_tr_b16 a[148:149], v226 offset:36864\n"
"ds_read_b64_tr_b16 a[150:151], v226 offset:38912\n"
"ds_read_b64_tr_b16 a[152:153], v227 offset:32768\n"
"ds_read_b64_tr_b16 a[154:155], v227 offset:34816\n"
"ds_read_b64_tr_b16 a[156:157], v227 offset:36864\n"
"ds_read_b64_tr_b16 a[158:159], v227 offset:38912\n"
"s_waitcnt lgkmcnt(0)\n"
"s_add_u32 m0, s58, 32768\n"
"s_add_u32 s60, s78, 65536\n"
"buffer_load_dwordx4 v228, s[28:31], s60 offen offset:0 lds\n"
"buffer_load_dwordx4 v229, s[28:31], s60 offen offset:1024 lds\n"
"buffer_load_dwordx4 v230, s[28:31], s60 offen offset:2048 lds\n"
"buffer_load_dwordx4 v231, s[28:31], s60 offen offset:3072 lds\n"
"s_add_u32 m0, s58, 36864\n"
"s_add_u32 s60, s78, 98304\n"
"buffer_load_dwordx4 v228, s[28:31], s60 offen offset:0 lds\n"
"buffer_load_dwordx4 v229, s[28:31], s60 offen offset:1024 lds\n"
"buffer_load_dwordx4 v230, s[28:31], s60 offen offset:2048 lds\n"
"buffer_load_dwordx4 v231, s[28:31], s60 offen offset:3072 lds\n"
"s_mov_b64 exec, 0xffff\n"
"v_mov_b64 v[222:223], 0\n"
"buffer_atomic_add_x2 v[222:223], v236, s[40:43], s35 offen sc0 sc1\n"
"s_mov_b64 exec, -1\n"
"ds_read_b64_tr_b16 v[144:145], v68 offset:57344\n"
"ds_read_b64_tr_b16 v[146:147], v68 offset:59392\n"
"ds_read_b64_tr_b16 v[148:149], v68 offset:61440\n"
"ds_read_b64_tr_b16 v[150:151], v68 offset:63488\n"
"ds_read_b64_tr_b16 v[152:153], v69 offset:57344\n"
"ds_read_b64_tr_b16 v[154:155], v69 offset:59392\n"
"ds_read_b64_tr_b16 v[156:157], v69 offset:61440\n"
"ds_read_b64_tr_b16 v[158:159], v69 offset:63488\n"
"ds_read_b64_tr_b16 v[160:161], v70 offset:57344\n"
"ds_read_b64_tr_b16 v[162:163], v70 offset:59392\n"
"ds_read_b64_tr_b16 v[164:165], v70 offset:61440\n"
"ds_read_b64_tr_b16 v[166:167], v70 offset:63488\n"
"ds_read_b64_tr_b16 v[168:169], v71 offset:57344\n"
"ds_read_b64_tr_b16 v[170:171], v71 offset:59392\n"
"ds_read_b64_tr_b16 v[172:173], v71 offset:61440\n"
"ds_read_b64_tr_b16 v[174:175], v71 offset:63488\n"
"ds_read_b64_tr_b16 v[176:177], v72 offset:0\n"
"ds_read_b64_tr_b16 v[178:179], v72 offset:2048\n"
"ds_read_b64_tr_b16 v[180:181], v72 offset:4096\n"
"ds_read_b64_tr_b16 v[182:183], v72 offset:6144\n"
"ds_read_b64_tr_b16 v[184:185], v73 offset:0\n"
"ds_read_b64_tr_b16 v[186:187], v73 offset:2048\n"
"ds_read_b64_tr_b16 v[188:189], v73 offset:4096\n"
"ds_read_b64_tr_b16 v[190:191], v73 offset:6144\n"
"s_waitcnt vmcnt(0)\n"
"v_cmp_ne_u64 vcc, s[6:7], v[222:223]\n"
"s_nop 0\n"
"s_cbranch_vccnz Lpy_%=\n"
"Lpy_ok_%=:\n"
"buffer_load_dwordx4 a[0:3], v76, s[40:43], s68 offen offset:0 sc1\n"
"buffer_load_dwordx4 a[8:11], v76, s[40:43], s68 offen offset:1024 sc1\n"
"buffer_load_dwordx4 a[4:7], v77, s[40:43], s68 offen offset:0 sc1\n"
"buffer_load_dwordx4 a[12:15], v77, s[40:43], s68 offen offset:1024 sc1\n"
"buffer_load_dwordx4 a[16:19], v76, s[40:43], s68 offen offset:2048 sc1\n"
"buffer_load_dwordx4 a[24:27], v76, s[40:43], s68 offen offset:3072 sc1\n"
"buffer_load_dwordx4 a[20:23], v77, s[40:43], s68 offen offset:2048 sc1\n"
"buffer_load_dwordx4 a[28:31], v77, s[40:43], s68 offen offset:3072 sc1\n"
"buffer_load_dwordx4 a[32:35], v76, s[40:43], s69 offen offset:0 sc1\n"
"buffer_load_dwordx4 a[40:43], v76, s[40:43], s69 offen offset:1024 sc1\n"
"ds_read_b64_tr_b16 a[160:161], v224 offset:32768\n"
"ds_read_b64_tr_b16 a[162:163], v224 offset:34816\n"
"ds_read_b64_tr_b16 a[164:165], v224 offset:36864\n"
"ds_read_b64_tr_b16 a[166:167], v224 offset:38912\n"
"ds_read_b64_tr_b16 a[168:169], v225 offset:32768\n"
"ds_read_b64_tr_b16 a[170:171], v225 offset:34816\n"
"ds_read_b64_tr_b16 a[172:173], v225 offset:36864\n"
"ds_read_b64_tr_b16 a[174:175], v225 offset:38912\n"
"ds_read_b64_tr_b16 a[176:177], v226 offset:32768\n"
"ds_read_b64_tr_b16 a[178:179], v226 offset:34816\n"
"ds_read_b64_tr_b16 a[180:181], v226 offset:36864\n"
"ds_read_b64_tr_b16 a[182:183], v226 offset:38912\n"
"ds_read_b64_tr_b16 a[184:185], v227 offset:32768\n"
"ds_read_b64_tr_b16 a[186:187], v227 offset:34816\n"
"ds_read_b64_tr_b16 a[188:189], v227 offset:36864\n"
"ds_read_b64_tr_b16 a[190:191], v227 offset:38912\n"
"s_waitcnt vmcnt(8) lgkmcnt(14)\n"
"v_mfma_f32_16x16x32_bf16 v[0:3], v[144:147], a[0:3], v[88:91]\n"
"buffer_load_dwordx4 a[36:39], v77, s[40:43], s69 offen offset:0 sc1\n"
"v_mov_b32 v80, 0xbfb17218\n"
"v_mfma_f32_16x16x32_bf16 v[0:3], v[148:151], a[8:11], v[0:3]\n"
"v_mov_b32 v82, 0x3f317218\n"
"s_waitcnt vmcnt(7)\n"
"v_mfma_f32_16x16x32_bf16 v[4:7], v[144:147], a[4:7], v[88:91]\n"
"ds_read_b64_tr_b16 v[144:145], v74 offset:0\n"
"v_mov_b32 v81, 0xbfb17218\n"
"v_mfma_f32_16x16x32_bf16 v[4:7], v[148:151], a[12:15], v[4:7]\n"
"ds_read_b64_tr_b16 v[146:147], v74 offset:2048\n"
"v_mfma_f32_16x16x32_bf16 v[8:11], v[152:155], a[0:3], v[92:95]\n"
"buffer_load_dwordx4 a[44:47], v77, s[40:43], s69 offen offset:1024 sc1\n"
"ds_read_b64_tr_b16 v[148:149], v74 offset:4096\n"
"v_mfma_f32_16x16x32_bf16 v[8:11], v[156:159], a[8:11], v[8:11]\n"
"ds_read_b64_tr_b16 v[150:151], v74 offset:6144\n"
"v_mov_b32 v83, 0x3f317218\n"
"v_mfma_f32_16x16x32_bf16 v[12:15], v[152:155], a[4:7], v[92:95]\n"
"ds_read_b64_tr_b16 v[152:153], v75 offset:0\n"
"s_mul_i32 s72, s65, 458752\n"
"v_mfma_f32_16x16x32_bf16 v[12:15], v[156:159], a[12:15], v[12:15]\n"
"ds_read_b64_tr_b16 v[154:155], v75 offset:2048\n"
"v_mfma_f32_16x16x32_bf16 v[16:19], v[160:163], a[0:3], v[96:99]\n"
"buffer_load_dwordx4 a[48:51], v76, s[40:43], s69 offen offset:2048 sc1\n"
"ds_read_b64_tr_b16 v[156:157], v75 offset:4096\n"
"v_mfma_f32_16x16x32_bf16 v[16:19], v[164:167], a[8:11], v[16:19]\n"
"ds_read_b64_tr_b16 v[158:159], v75 offset:6144\n"
"s_mul_i32 s15, s11, 114688\n"
"v_mfma_f32_16x16x32_bf16 v[20:23], v[160:163], a[4:7], v[96:99]\n"
"ds_read_b64_tr_b16 v[160:161], v64 offset:0\n"
"s_add_u32 s72, s72, s15\n"
"v_mfma_f32_16x16x32_bf16 v[20:23], v[164:167], a[12:15], v[20:23]\n"
"ds_read_b64_tr_b16 v[162:163], v64 offset:2048\n"
"v_mfma_f32_16x16x32_bf16 v[24:27], v[168:171], a[0:3], v[100:103]\n"
"buffer_load_dwordx4 a[56:59], v76, s[40:43], s69 offen offset:3072 sc1\n"
"ds_read_b64_tr_b16 v[164:165], v64 offset:4096\n"
"v_mfma_f32_16x16x32_bf16 v[24:27], v[172:175], a[8:11], v[24:27]\n"
"ds_read_b64_tr_b16 v[166:167], v64 offset:6144\n"
"s_mul_i32 s15, s64, 3584\n"
"v_mfma_f32_16x16x32_bf16 v[28:31], v[168:171], a[4:7], v[100:103]\n"
"ds_read_b64_tr_b16 v[168:169], v65 offset:0\n"
"s_add_u32 s72, s72, s15\n"
"v_mfma_f32_16x16x32_bf16 v[28:31], v[172:175], a[12:15], v[28:31]\n"
"ds_read_b64_tr_b16 v[170:171], v65 offset:2048\n"
"s_waitcnt vmcnt(8)\n"
"v_mfma_f32_16x16x32_bf16 v[0:3], v[176:179], a[16:19], v[0:3]\n"
"buffer_load_dwordx4 a[52:55], v77, s[40:43], s69 offen offset:2048 sc1\n"
"ds_read_b64_tr_b16 v[172:173], v65 offset:4096\n"
"v_mfma_f32_16x16x32_bf16 v[0:3], v[180:183], a[24:27], v[0:3]\n"
"ds_read_b64_tr_b16 v[174:175], v65 offset:6144\n"
"s_waitcnt vmcnt(7)\n"
"v_mfma_f32_16x16x32_bf16 v[4:7], v[176:179], a[20:23], v[4:7]\n"
"ds_read_b64_tr_b16 v[176:177], v66 offset:0\n"
"s_add_u32 s72, s72, 58720256\n"
"v_mfma_f32_16x16x32_bf16 v[4:7], v[180:183], a[28:31], v[4:7]\n"
"ds_read_b64_tr_b16 v[178:179], v66 offset:2048\n"
"v_mfma_f32_16x16x32_bf16 v[8:11], v[184:187], a[16:19], v[8:11]\n"
"buffer_load_dwordx4 a[60:63], v77, s[40:43], s69 offen offset:3072 sc1\n"
"ds_read_b64_tr_b16 v[180:181], v66 offset:4096\n"
"v_mfma_f32_16x16x32_bf16 v[8:11], v[188:191], a[24:27], v[8:11]\n"
"ds_read_b64_tr_b16 v[182:183], v66 offset:6144\n"
"s_add_u32 s73, s72, 57344\n"
"v_mfma_f32_16x16x32_bf16 v[12:15], v[184:187], a[20:23], v[12:15]\n"
"ds_read_b64_tr_b16 v[184:185], v67 offset:0\n"
"s_add_u32 s34, s34, 262144\n"
"v_mfma_f32_16x16x32_bf16 v[12:15], v[188:191], a[28:31], v[12:15]\n"
"ds_read_b64_tr_b16 v[186:187], v67 offset:2048\n"
"s_waitcnt lgkmcnt(14)\n"
"v_mfma_f32_16x16x32_bf16 v[16:19], v[144:147], a[16:19], v[16:19]\n"
"buffer_load_dwordx4 a[64:67], v76, s[40:43], s70 offen offset:0 sc1\n"
"ds_read_b64_tr_b16 v[188:189], v67 offset:4096\n"
"v_mfma_f32_16x16x32_bf16 v[16:19], v[148:151], a[24:27], v[16:19]\n"
"ds_read_b64_tr_b16 v[190:191], v67 offset:6144\n"
"s_and_b32 s16, s62, 1\n"
"v_mfma_f32_16x16x32_bf16 v[20:23], v[144:147], a[20:23], v[20:23]\n"
"ds_read_b64_tr_b16 v[144:145], v64 offset:8192\n"
"s_lshr_b32 s17, s62, 1\n"
"v_mfma_f32_16x16x32_bf16 v[20:23], v[148:151], a[28:31], v[20:23]\n"
"ds_read_b64_tr_b16 v[146:147], v64 offset:10240\n"
"v_mfma_f32_16x16x32_bf16 v[24:27], v[152:155], a[16:19], v[24:27]\n"
"buffer_load_dwordx4 a[72:75], v76, s[40:43], s70 offen offset:1024 sc1\n"
"ds_read_b64_tr_b16 v[148:149], v64 offset:12288\n"
"v_mfma_f32_16x16x32_bf16 v[24:27], v[156:159], a[24:27], v[24:27]\n"
"ds_read_b64_tr_b16 v[150:151], v64 offset:14336\n"
"s_lshl_b32 s35, s17, 14\n"
"v_mfma_f32_16x16x32_bf16 v[28:31], v[152:155], a[20:23], v[28:31]\n"
"ds_read_b64_tr_b16 v[152:153], v65 offset:8192\n"
"s_lshl_b32 s15, s11, 12\n"
"v_mfma_f32_16x16x32_bf16 v[28:31], v[156:159], a[28:31], v[28:31]\n"
"ds_read_b64_tr_b16 v[154:155], v65 offset:10240\n"
"s_waitcnt vmcnt(8) lgkmcnt(14)\n"
"v_mfma_f32_16x16x32_bf16 v[0:3], v[160:163], a[32:35], v[0:3]\n"
"buffer_load_dwordx4 a[68:71], v77, s[40:43], s70 offen offset:0 sc1\n"
"ds_read_b64_tr_b16 v[156:157], v65 offset:12288\n"
"v_mfma_f32_16x16x32_bf16 v[0:3], v[164:167], a[40:43], v[0:3]\n"
"ds_read_b64_tr_b16 v[158:159], v65 offset:14336\n"
"s_waitcnt vmcnt(7)\n"
"v_mfma_f32_16x16x32_bf16 v[4:7], v[160:163], a[36:39], v[4:7]\n"
"ds_read_b64_tr_b16 v[160:161], v66 offset:8192\n"
"s_add_u32 s35, s35, s15\n"
"v_mfma_f32_16x16x32_bf16 v[4:7], v[164:167], a[44:47], v[4:7]\n"
"ds_read_b64_tr_b16 v[162:163], v66 offset:10240\n"
"v_mfma_f32_16x16x32_bf16 v[8:11], v[168:171], a[32:35], v[8:11]\n"
"buffer_load_dwordx4 a[76:79], v77, s[40:43], s70 offen offset:1024 sc1\n"
"ds_read_b64_tr_b16 v[164:165], v66 offset:12288\n"
"v_mfma_f32_16x16x32_bf16 v[8:11], v[172:175], a[40:43], v[8:11]\n"
"ds_read_b64_tr_b16 v[166:167], v66 offset:14336\n"
"s_lshl_b32 s15, s16, 9\n"
"v_mfma_f32_16x16x32_bf16 v[12:15], v[168:171], a[36:39], v[12:15]\n"
"ds_read_b64_tr_b16 v[168:169], v67 offset:8192\n"
"s_add_u32 s35, s35, s15\n"
"v_mfma_f32_16x16x32_bf16 v[12:15], v[172:175], a[44:47], v[12:15]\n"
"ds_read_b64_tr_b16 v[170:171], v67 offset:10240\n"
"s_waitcnt lgkmcnt(14)\n"
"v_mfma_f32_16x16x32_bf16 v[16:19], v[176:179], a[32:35], v[16:19]\n"
"buffer_load_dwordx4 a[80:83], v76, s[40:43], s70 offen offset:2048 sc1\n"
"ds_read_b64_tr_b16 v[172:173], v67 offset:12288\n"
"v_mfma_f32_16x16x32_bf16 v[16:19], v[180:183], a[40:43], v[16:19]\n"
"ds_read_b64_tr_b16 v[174:175], v67 offset:14336\n"
"s_add_u32 s35, s35, 13631488\n"
"v_mfma_f32_16x16x32_bf16 v[20:23], v[176:179], a[36:39], v[20:23]\n"
"ds_read_b64_tr_b16 v[176:177], v64 offset:40960\n"
"v_and_b32 v236, 0xd00, v236\n"
"v_mfma_f32_16x16x32_bf16 v[20:23], v[180:183], a[44:47], v[20:23]\n"
"ds_read_b64_tr_b16 v[178:179], v64 offset:43008\n"
"v_mfma_f32_16x16x32_bf16 v[24:27], v[184:187], a[32:35], v[24:27]\n"
"buffer_load_dwordx4 a[88:91], v76, s[40:43], s70 offen offset:3072 sc1\n"
"ds_read_b64_tr_b16 v[180:181], v64 offset:45056\n"
"v_mfma_f32_16x16x32_bf16 v[24:27], v[188:191], a[40:43], v[24:27]\n"
"ds_read_b64_tr_b16 v[182:183], v64 offset:47104\n"
"v_mfma_f32_16x16x32_bf16 v[28:31], v[184:187], a[36:39], v[28:31]\n"
"ds_read_b64_tr_b16 v[184:185], v65 offset:40960\n"
"v_mfma_f32_16x16x32_bf16 v[28:31], v[188:191], a[44:47], v[28:31]\n"
"ds_read_b64_tr_b16 v[186:187], v65 offset:43008\n"
"s_waitcnt vmcnt(8) lgkmcnt(14)\n"
"v_mfma_f32_16x16x32_bf16 v[0:3], v[144:147], a[48:51], v[0:3]\n"
"buffer_load_dwordx4 a[84:87], v77, s[40:43], s70 offen offset:2048 sc1\n"
"ds_read_b64_tr_b16 v[188:189], v65 offset:45056\n"
"v_mfma_f32_16x16x32_bf16 v[0:3], v[148:151], a[56:59], v[0:3]\n"
"ds_read_b64_tr_b16 v[190:191], v65 offset:47104\n"
"s_waitcnt vmcnt(7)\n"
"v_mfma_f32_16x16x32_bf16 v[4:7], v[144:147], a[52:55], v[4:7]\n"
"ds_read_b64_tr_b16 v[144:145], v66 offset:40960\n"
"v_mfma_f32_16x16x32_bf16 v[4:7], v[148:151], a[60:63], v[4:7]\n"
"ds_read_b64_tr_b16 v[146:147], v66 offset:43008\n"
"v_mfma_f32_16x16x32_bf16 v[8:11], v[152:155], a[48:51], v[8:11]\n"
"buffer_load_dwordx4 a[92:95], v77, s[40:43], s70 offen offset:3072 sc1\n"
"ds_read_b64_tr_b16 v[148:149], v66 offset:45056\n"
"v_mfma_f32_16x16x32_bf16 v[8:11], v[156:159], a[56:59], v[8:11]\n"
"ds_read_b64_tr_b16 v[150:151], v66 offset:47104\n"
"v_mfma_f32_16x16x32_bf16 v[12:15], v[152:155], a[52:55], v[12:15]\n"
"ds_read_b64_tr_b16 v[152:153], v67 offset:40960\n"
"v_mfma_f32_16x16x32_bf16 v[12:15], v[156:159], a[60:63], v[12:15]\n"
"ds_read_b64_tr_b16 v[154:155], v67 offset:43008\n"
"s_waitcnt lgkmcnt(14)\n"
"v_mfma_f32_16x16x32_bf16 v[16:19], v[160:163], a[48:51], v[16:19]\n"
"buffer_load_dwordx4 a[96:99], v76, s[40:43], s71 offen offset:0 sc1\n"
"ds_read_b64_tr_b16 v[156:157], v67 offset:45056\n"
"v_mfma_f32_16x16x32_bf16 v[16:19], v[164:167], a[56:59], v[16:19]\n"
"ds_read_b64_tr_b16 v[158:159], v67 offset:47104\n"
"v_mfma_f32_16x16x32_bf16 v[20:23], v[160:163], a[52:55], v[20:23]\n"
"ds_read_b64_tr_b16 v[160:161], v64 offset:49152\n"
"v_mfma_f32_16x16x32_bf16 v[20:23], v[164:167], a[60:63], v[20:23]\n"
"ds_read_b64_tr_b16 v[162:163], v64 offset:51200\n"
"v_mfma_f32_16x16x32_bf16 v[24:27], v[168:171], a[48:51], v[24:27]\n"
"buffer_load_dwordx4 a[104:107], v76, s[40:43], s71 offen offset:1024 sc1\n"
"ds_read_b64_tr_b16 v[164:165], v64 offset:53248\n"
"v_mfma_f32_16x16x32_bf16 v[24:27], v[172:175], a[56:59], v[24:27]\n"
"ds_read_b64_tr_b16 v[166:167], v64 offset:55296\n"
"v_mfma_f32_16x16x32_bf16 v[28:31], v[168:171], a[52:55], v[28:31]\n"
"ds_read_b64_tr_b16 v[168:169], v65 offset:49152\n"
"v_mfma_f32_16x16x32_bf16 v[28:31], v[172:175], a[60:63], v[28:31]\n"
"ds_read_b64_tr_b16 v[170:171], v65 offset:51200\n"
"s_waitcnt vmcnt(8) lgkmcnt(14)\n"
"v_mfma_f32_16x16x32_bf16 v[0:3], v[176:179], a[64:67], v[0:3]\n"
"buffer_load_dwordx4 a[100:103], v77, s[40:43], s71 offen offset:0 sc1\n"
"ds_read_b64_tr_b16 v[172:173], v65 offset:53248\n"
"v_mfma_f32_16x16x32_bf16 v[0:3], v[180:183], a[72:75], v[0:3]\n"
"ds_read_b64_tr_b16 v[174:175], v65 offset:55296\n"
"s_waitcnt vmcnt(7)\n"
"v_mfma_f32_16x16x32_bf16 v[4:7], v[176:179], a[68:71], v[4:7]\n"
"ds_read_b64_tr_b16 v[176:177], v66 offset:49152\n"
"v_mfma_f32_16x16x32_bf16 v[4:7], v[180:183], a[76:79], v[4:7]\n"
"ds_read_b64_tr_b16 v[178:179], v66 offset:51200\n"
"v_mfma_f32_16x16x32_bf16 v[8:11], v[184:187], a[64:67], v[8:11]\n"
"buffer_load_dwordx4 a[108:111], v77, s[40:43], s71 offen offset:1024 sc1\n"
"ds_read_b64_tr_b16 v[180:181], v66 offset:53248\n"
"v_mfma_f32_16x16x32_bf16 v[8:11], v[188:191], a[72:75], v[8:11]\n"
"ds_read_b64_tr_b16 v[182:183], v66 offset:55296\n"
"v_mfma_f32_16x16x32_bf16 v[12:15], v[184:187], a[68:71], v[12:15]\n"
"ds_read_b64_tr_b16 v[184:185], v67 offset:49152\n"
"v_mfma_f32_16x16x32_bf16 v[12:15], v[188:191], a[76:79], v[12:15]\n"
"ds_read_b64_tr_b16 v[186:187], v67 offset:51200\n"
"s_waitcnt lgkmcnt(14)\n"
"v_mfma_f32_16x16x32_bf16 v[16:19], v[144:147], a[64:67], v[16:19]\n"
"buffer_load_dwordx4 a[112:115], v76, s[40:43], s71 offen offset:2048 sc1\n"
"ds_read_b64_tr_b16 v[188:189], v67 offset:53248\n"
"v_mfma_f32_16x16x32_bf16 v[16:19], v[148:151], a[72:75], v[16:19]\n"
"ds_read_b64_tr_b16 v[190:191], v67 offset:55296\n"
"v_mfma_f32_16x16x32_bf16 v[20:23], v[144:147], a[68:71], v[20:23]\n"
"ds_read_b64_tr_b16 v[144:145], v68 offset:16384\n"
"v_mfma_f32_16x16x32_bf16 v[20:23], v[148:151], a[76:79], v[20:23]\n"
"ds_read_b64_tr_b16 v[146:147], v68 offset:18432\n"
"v_mfma_f32_16x16x32_bf16 v[24:27], v[152:155], a[64:67], v[24:27]\n"
"buffer_load_dwordx4 a[120:123], v76, s[40:43], s71 offen offset:3072 sc1\n"
"ds_read_b64_tr_b16 v[148:149], v68 offset:20480\n"
"v_mfma_f32_16x16x32_bf16 v[24:27], v[156:159], a[72:75], v[24:27]\n"
"ds_read_b64_tr_b16 v[150:151], v68 offset:22528\n"
"v_mfma_f32_16x16x32_bf16 v[28:31], v[152:155], a[68:71], v[28:31]\n"
"ds_read_b64_tr_b16 v[152:153], v69 offset:16384\n"
"v_mfma_f32_16x16x32_bf16 v[28:31], v[156:159], a[76:79], v[28:31]\n"
"ds_read_b64_tr_b16 v[154:155], v69 offset:18432\n"
"s_waitcnt vmcnt(8) lgkmcnt(14)\n"
"v_mfma_f32_16x16x32_bf16 v[0:3], v[160:163], a[80:83], v[0:3]\n"
"buffer_load_dwordx4 a[116:119], v77, s[40:43], s71 offen offset:2048 sc1\n"
"ds_read_b64_tr_b16 v[156:157], v69 offset:20480\n"
"v_mfma_f32_16x16x32_bf16 v[0:3], v[164:167], a[88:91], v[0:3]\n"
"ds_read_b64_tr_b16 v[158:159], v69 offset:22528\n"
"s_waitcnt vmcnt(7)\n"
"v_mfma_f32_16x16x32_bf16 v[4:7], v[160:163], a[84:87], v[4:7]\n"
"ds_read_b64_tr_b16 v[160:161], v70 offset:16384\n"
"v_mfma_f32_16x16x32_bf16 v[4:7], v[164:167], a[92:95], v[4:7]\n"
"ds_read_b64_tr_b16 v[162:163], v70 offset:18432\n"
"v_mfma_f32_16x16x32_bf16 v[8:11], v[168:171], a[80:83], v[8:11]\n"
"buffer_load_dwordx4 a[124:127], v77, s[40:43], s71 offen offset:3072 sc1\n"
"ds_read_b64_tr_b16 v[164:165], v70 offset:20480\n"
"v_mfma_f32_16x16x32_bf16 v[8:11], v[172:175], a[88:91], v[8:11]\n"
"ds_read_b64_tr_b16 v[166:167], v70 offset:22528\n"
"v_mfma_f32_16x16x32_bf16 v[12:15], v[168:171], a[84:87], v[12:15]\n"
"ds_read_b64_tr_b16 v[168:169], v71 offset:16384\n"
"v_mfma_f32_16x16x32_bf16 v[12:15], v[172:175], a[92:95], v[12:15]\n"
"ds_read_b64_tr_b16 v[170:171], v71 offset:18432\n"
"s_waitcnt lgkmcnt(14)\n"
"v_mfma_f32_16x16x32_bf16 v[16:19], v[176:179], a[80:83], v[16:19]\n"
"ds_read_b64_tr_b16 v[172:173], v71 offset:20480\n"
"s_add_u32 m0, s58, 32768\n"
"v_mfma_f32_16x16x32_bf16 v[16:19], v[180:183], a[88:91], v[16:19]\n"
"ds_read_b64_tr_b16 v[174:175], v71 offset:22528\n"
"s_add_u32 s60, s78, 131072\n"
"v_mfma_f32_16x16x32_bf16 v[20:23], v[176:179], a[84:87], v[20:23]\n"
"ds_read_b64_tr_b16 v[176:177], v68 offset:24576\n"
"buffer_load_dwordx4 v228, s[28:31], s60 offen offset:0 lds\n"
"v_mfma_f32_16x16x32_bf16 v[20:23], v[180:183], a[92:95], v[20:23]\n"
"ds_read_b64_tr_b16 v[178:179], v68 offset:26624\n"
"v_mfma_f32_16x16x32_bf16 v[24:27], v[184:187], a[80:83], v[24:27]\n"
"ds_read_b64_tr_b16 v[180:181], v68 offset:28672\n"
"buffer_load_dwordx4 v229, s[28:31], s60 offen offset:1024 lds\n"
"v_mfma_f32_16x16x32_bf16 v[24:27], v[188:191], a[88:91], v[24:27]\n"
"ds_read_b64_tr_b16 v[182:183], v68 offset:30720\n"
"v_mfma_f32_16x16x32_bf16 v[28:31], v[184:187], a[84:87], v[28:31]\n"
"ds_read_b64_tr_b16 v[184:185], v69 offset:24576\n"
"v_mfma_f32_16x16x32_bf16 v[28:31], v[188:191], a[92:95], v[28:31]\n"
"ds_read_b64_tr_b16 v[186:187], v69 offset:26624\n"
"s_waitcnt vmcnt(8) lgkmcnt(14)\n"
"v_mfma_f32_16x16x32_bf16 v[0:3], v[144:147], a[96:99], v[0:3]\n"
"ds_read_b64_tr_b16 v[188:189], v69 offset:28672\n"
"buffer_load_dwordx4 v230, s[28:31], s60 offen offset:2048 lds\n"
"v_mfma_f32_16x16x32_bf16 v[0:3], v[148:151], a[104:107], v[0:3]\n"
"ds_read_b64_tr_b16 v[190:191], v69 offset:30720\n"
"s_waitcnt vmcnt(7)\n"
"v_mfma_f32_16x16x32_bf16 v[4:7], v[144:147], a[100:103], v[4:7]\n"
"ds_read_b64_tr_b16 v[144:145], v70 offset:24576\n"
"v_mfma_f32_16x16x32_bf16 v[4:7], v[148:151], a[108:111], v[4:7]\n"
"ds_read_b64_tr_b16 v[146:147], v70 offset:26624\n"
"v_mfma_f32_16x16x32_bf16 v[8:11], v[152:155], a[96:99], v[8:11]\n"
"ds_read_b64_tr_b16 v[148:149], v70 offset:28672\n"
"buffer_load_dwordx4 v231, s[28:31], s60 offen offset:3072 lds\n"
"v_mfma_f32_16x16x32_bf16 v[8:11], v[156:159], a[104:107], v[8:11]\n"
"ds_read_b64_tr_b16 v[150:151], v70 offset:30720\n"
"v_mfma_f32_16x16x32_bf16 v[12:15], v[152:155], a[100:103], v[12:15]\n"
"ds_read_b64_tr_b16 v[152:153], v71 offset:24576\n"
"v_mfma_f32_16x16x32_bf16 v[12:15], v[156:159], a[108:111], v[12:15]\n"
"ds_read_b64_tr_b16 v[154:155], v71 offset:26624\n"
"s_waitcnt lgkmcnt(14)\n"
"v_mfma_f32_16x16x32_bf16 v[16:19], v[160:163], a[96:99], v[16:19]\n"
"ds_read_b64_tr_b16 v[156:157], v71 offset:28672\n"
"s_add_u32 m0, s58, 36864\n"
"v_mfma_f32_16x16x32_bf16 v[16:19], v[164:167], a[104:107], v[16:19]\n"
"ds_read_b64_tr_b16 v[158:159], v71 offset:30720\n"
"s_add_u32 s60, s78, 163840\n"
"v_mfma_f32_16x16x32_bf16 v[20:23], v[160:163], a[100:103], v[20:23]\n"
"buffer_load_dwordx4 v228, s[28:31], s60 offen offset:0 lds\n"
"ds_read_b64_tr_b16 v[160:161], v68 offset:32768\n"
"v_mfma_f32_16x16x32_bf16 v[20:23], v[164:167], a[108:111], v[20:23]\n"
"ds_read_b64_tr_b16 v[162:163], v68 offset:34816\n"
"v_mfma_f32_16x16x32_bf16 v[24:27], v[168:171], a[96:99], v[24:27]\n"
"buffer_load_dwordx4 v229, s[28:31], s60 offen offset:1024 lds\n"
"ds_read_b64_tr_b16 v[164:165], v68 offset:36864\n"
"v_mfma_f32_16x16x32_bf16 v[24:27], v[172:175], a[104:107], v[24:27]\n"
"ds_read_b64_tr_b16 v[166:167], v68 offset:38912\n"
"v_mfma_f32_16x16x32_bf16 v[28:31], v[168:171], a[100:103], v[28:31]\n"
"ds_read_b64_tr_b16 v[168:169], v69 offset:32768\n"
"v_mfma_f32_16x16x32_bf16 v[28:31], v[172:175], a[108:111], v[28:31]\n"
"ds_read_b64_tr_b16 v[170:171], v69 offset:34816\n"
"s_waitcnt vmcnt(8) lgkmcnt(14)\n"
"v_mfma_f32_16x16x32_bf16 v[0:3], v[176:179], a[112:115], v[0:3]\n"
"buffer_load_dwordx4 v230, s[28:31], s60 offen offset:2048 lds\n"
"ds_read_b64_tr_b16 v[172:173], v69 offset:36864\n"
"v_mfma_f32_16x16x32_bf16 v[0:3], v[180:183], a[120:123], v[0:3]\n"
"ds_read_b64_tr_b16 v[174:175], v69 offset:38912\n"
"s_waitcnt vmcnt(7)\n"
"v_mfma_f32_16x16x32_bf16 v[4:7], v[176:179], a[116:119], v[4:7]\n"
"ds_read_b64_tr_b16 v[176:177], v70 offset:32768\n"
"v_mfma_f32_16x16x32_bf16 v[4:7], v[180:183], a[124:127], v[4:7]\n"
"ds_read_b64_tr_b16 v[178:179], v70 offset:34816\n"
"v_mfma_f32_16x16x32_bf16 v[8:11], v[184:187], a[112:115], v[8:11]\n"
"buffer_load_dwordx4 v231, s[28:31], s60 offen offset:3072 lds\n"
"ds_read_b64_tr_b16 v[180:181], v70 offset:36864\n"
"v_mfma_f32_16x16x32_bf16 v[8:11], v[188:191], a[120:123], v[8:11]\n"
"ds_read_b64_tr_b16 v[182:183], v70 offset:38912\n"
"v_mfma_f32_16x16x32_bf16 v[12:15], v[184:187], a[116:119], v[12:15]\n"
"ds_read_b64_tr_b16 v[184:185], v68 offset:40960\n"
"v_mfma_f32_16x16x32_bf16 v[12:15], v[188:191], a[124:127], v[12:15]\n"
"ds_read_b64_tr_b16 v[186:187], v68 offset:43008\n"
"s_waitcnt lgkmcnt(14)\n"
"v_mfma_f32_16x16x32_bf16 v[16:19], v[144:147], a[112:115], v[16:19]\n"
"ds_read_b64_tr_b16 v[188:189], v68 offset:45056\n"
"v_mfma_f32_16x16x32_bf16 v[16:19], v[148:151], a[120:123], v[16:19]\n"
"ds_read_b64_tr_b16 v[190:191], v68 offset:47104\n"
"v_mfma_f32_16x16x32_bf16 v[20:23], v[144:147], a[116:119], v[20:23]\n"
"ds_read_b64_tr_b16 v[144:145], v69 offset:40960\n"
"v_mfma_f32_16x16x32_bf16 v[20:23], v[148:151], a[124:127], v[20:23]\n"
"ds_read_b64_tr_b16 v[146:147], v69 offset:43008\n"
"v_mfma_f32_16x16x32_bf16 v[24:27], v[152:155], a[112:115], v[24:27]\n"
"ds_read_b64_tr_b16 v[148:149], v69 offset:45056\n"
"v_mfma_f32_16x16x32_bf16 v[24:27], v[156:159], a[120:123], v[24:27]\n"
"ds_read_b64_tr_b16 v[150:151], v69 offset:47104\n"
"v_mfma_f32_16x16x32_bf16 v[28:31], v[152:155], a[116:119], v[28:31]\n"
"ds_read_b64_tr_b16 v[152:153], v70 offset:40960\n"
"v_mfma_f32_16x16x32_bf16 v[28:31], v[156:159], a[124:127], v[28:31]\n"
"ds_read_b64_tr_b16 v[154:155], v70 offset:43008\n"
"ds_read_b64_tr_b16 v[156:157], v70 offset:45056\n"
"ds_read_b64_tr_b16 v[158:159], v70 offset:47104\n"
"s_barrier\n"
"v_exp_f32 v192, v0\n"
"v_exp_f32 v193, v1\n"
"v_exp_f32 v194, v2\n"
"v_exp_f32 v195, v3\n"
"v_exp_f32 v196, v8\n"
"v_exp_f32 v197, v9\n"
"v_exp_f32 v198, v10\n"
"v_exp_f32 v199, v11\n"
"v_pk_add_f32 v[192:193], v[192:193], 1.0 op_sel_hi:[1,0]\n"
"v_pk_add_f32 v[194:195], v[194:195], 1.0 op_sel_hi:[1,0]\n"
"v_pk_add_f32 v[196:197], v[196:197], 1.0 op_sel_hi:[1,0]\n"
"v_pk_add_f32 v[198:199], v[198:199], 1.0 op_sel_hi:[1,0]\n"
"v_pk_fma_f32 v[192:193], v[192:193], v[192:193], 1.0 op_sel_hi:[1,1,0]\n"
"v_pk_fma_f32 v[194:195], v[194:195], v[194:195], 1.0 op_sel_hi:[1,1,0]\n"
"v_pk_fma_f32 v[196:197], v[196:197], v[196:197], 1.0 op_sel_hi:[1,1,0]\n"
"v_pk_fma_f32 v[198:199], v[198:199], v[198:199], 1.0 op_sel_hi:[1,1,0]\n"
"v_rcp_f32 v192, v192\n"
"v_rcp_f32 v193, v193\n"
"v_rcp_f32 v194, v194\n"
"v_rcp_f32 v195, v195\n"
"v_rcp_f32 v196, v196\n"
"v_rcp_f32 v197, v197\n"
"v_rcp_f32 v198, v198\n"
"v_rcp_f32 v199, v199\n"
"v_pk_fma_f32 v[192:193], v[192:193], v[80:81], v[82:83]\n"
"v_pk_fma_f32 v[194:195], v[194:195], v[80:81], v[82:83]\n"
"v_pk_fma_f32 v[196:197], v[196:197], v[80:81], v[82:83]\n"
"v_pk_fma_f32 v[198:199], v[198:199], v[80:81], v[82:83]\n"
"v_pk_mul_f32 v[192:193], v[192:193], v[0:1]\n"
"v_pk_mul_f32 v[194:195], v[194:195], v[2:3]\n"
"v_pk_mul_f32 v[196:197], v[196:197], v[8:9]\n"
"v_pk_mul_f32 v[198:199], v[198:199], v[10:11]\n"
"v_cvt_pk_bf16_f32 v0, v192, v193\n"
"v_cvt_pk_bf16_f32 v1, v194, v195\n"
"v_cvt_pk_bf16_f32 v2, v196, v197\n"
"v_cvt_pk_bf16_f32 v3, v198, v199\n"
"buffer_store_dwordx4 v[0:3], v232, s[40:43], s72 offen offset:0 sc1\n"
"v_exp_f32 v192, v4\n"
"v_exp_f32 v193, v5\n"
"v_exp_f32 v194, v6\n"
"v_exp_f32 v195, v7\n"
"v_exp_f32 v196, v12\n"
"v_exp_f32 v197, v13\n"
"v_exp_f32 v198, v14\n"
"v_exp_f32 v199, v15\n"
"v_pk_add_f32 v[192:193], v[192:193], 1.0 op_sel_hi:[1,0]\n"
"v_pk_add_f32 v[194:195], v[194:195], 1.0 op_sel_hi:[1,0]\n"
"v_pk_add_f32 v[196:197], v[196:197], 1.0 op_sel_hi:[1,0]\n"
"v_pk_add_f32 v[198:199], v[198:199], 1.0 op_sel_hi:[1,0]\n"
"v_pk_fma_f32 v[192:193], v[192:193], v[192:193], 1.0 op_sel_hi:[1,1,0]\n"
"v_pk_fma_f32 v[194:195], v[194:195], v[194:195], 1.0 op_sel_hi:[1,1,0]\n"
"v_pk_fma_f32 v[196:197], v[196:197], v[196:197], 1.0 op_sel_hi:[1,1,0]\n"
"v_pk_fma_f32 v[198:199], v[198:199], v[198:199], 1.0 op_sel_hi:[1,1,0]\n"
"v_rcp_f32 v192, v192\n"
"v_rcp_f32 v193, v193\n"
"v_rcp_f32 v194, v194\n"
"v_rcp_f32 v195, v195\n"
"v_rcp_f32 v196, v196\n"
"v_rcp_f32 v197, v197\n"
"v_rcp_f32 v198, v198\n"
"v_rcp_f32 v199, v199\n"
"v_pk_fma_f32 v[192:193], v[192:193], v[80:81], v[82:83]\n"
"v_pk_fma_f32 v[194:195], v[194:195], v[80:81], v[82:83]\n"
"v_pk_fma_f32 v[196:197], v[196:197], v[80:81], v[82:83]\n"
"v_pk_fma_f32 v[198:199], v[198:199], v[80:81], v[82:83]\n"
"v_pk_mul_f32 v[192:193], v[192:193], v[4:5]\n"
"v_pk_mul_f32 v[194:195], v[194:195], v[6:7]\n"
"v_pk_mul_f32 v[196:197], v[196:197], v[12:13]\n"
"v_pk_mul_f32 v[198:199], v[198:199], v[14:15]\n"
"v_cvt_pk_bf16_f32 v4, v192, v193\n"
"v_cvt_pk_bf16_f32 v5, v194, v195\n"
"v_cvt_pk_bf16_f32 v6, v196, v197\n"
"v_cvt_pk_bf16_f32 v7, v198, v199\n"
"buffer_store_dwordx4 v[4:7], v232, s[40:43], s73 offen offset:0 sc1\n"
"v_exp_f32 v192, v16\n"
"v_exp_f32 v193, v17\n"
"v_exp_f32 v194, v18\n"
"v_exp_f32 v195, v19\n"
"v_exp_f32 v196, v24\n"
"v_exp_f32 v197, v25\n"
"v_exp_f32 v198, v26\n"
"v_exp_f32 v199, v27\n"
"v_pk_add_f32 v[192:193], v[192:193], 1.0 op_sel_hi:[1,0]\n"
"v_pk_add_f32 v[194:195], v[194:195], 1.0 op_sel_hi:[1,0]\n"
"v_pk_add_f32 v[196:197], v[196:197], 1.0 op_sel_hi:[1,0]\n"
"v_pk_add_f32 v[198:199], v[198:199], 1.0 op_sel_hi:[1,0]\n"
"v_pk_fma_f32 v[192:193], v[192:193], v[192:193], 1.0 op_sel_hi:[1,1,0]\n"
"v_pk_fma_f32 v[194:195], v[194:195], v[194:195], 1.0 op_sel_hi:[1,1,0]\n"
"v_pk_fma_f32 v[196:197], v[196:197], v[196:197], 1.0 op_sel_hi:[1,1,0]\n"
"v_pk_fma_f32 v[198:199], v[198:199], v[198:199], 1.0 op_sel_hi:[1,1,0]\n"
"v_rcp_f32 v192, v192\n"
"v_rcp_f32 v193, v193\n"
"v_rcp_f32 v194, v194\n"
"v_rcp_f32 v195, v195\n"
"v_rcp_f32 v196, v196\n"
"v_rcp_f32 v197, v197\n"
"v_rcp_f32 v198, v198\n"
"v_rcp_f32 v199, v199\n"
"v_pk_fma_f32 v[192:193], v[192:193], v[80:81], v[82:83]\n"
"v_pk_fma_f32 v[194:195], v[194:195], v[80:81], v[82:83]\n"
"v_pk_fma_f32 v[196:197], v[196:197], v[80:81], v[82:83]\n"
"v_pk_fma_f32 v[198:199], v[198:199], v[80:81], v[82:83]\n"
"v_pk_mul_f32 v[192:193], v[192:193], v[16:17]\n"
"v_pk_mul_f32 v[194:195], v[194:195], v[18:19]\n"
"v_pk_mul_f32 v[196:197], v[196:197], v[24:25]\n"
"v_pk_mul_f32 v[198:199], v[198:199], v[26:27]\n"
"v_cvt_pk_bf16_f32 v16, v192, v193\n"
"v_cvt_pk_bf16_f32 v17, v194, v195\n"
"v_cvt_pk_bf16_f32 v18, v196, v197\n"
"v_cvt_pk_bf16_f32 v19, v198, v199\n"
"buffer_store_dwordx4 v[16:19], v232, s[40:43], s72 offen offset:1024 sc1\n"
"v_exp_f32 v192, v20\n"
"v_exp_f32 v193, v21\n"
"v_exp_f32 v194, v22\n"
"v_exp_f32 v195, v23\n"
"v_exp_f32 v196, v28\n"
"v_exp_f32 v197, v29\n"
"v_exp_f32 v198, v30\n"
"v_exp_f32 v199, v31\n"
"v_pk_add_f32 v[192:193], v[192:193], 1.0 op_sel_hi:[1,0]\n"
"v_pk_add_f32 v[194:195], v[194:195], 1.0 op_sel_hi:[1,0]\n"
"v_pk_add_f32 v[196:197], v[196:197], 1.0 op_sel_hi:[1,0]\n"
"v_pk_add_f32 v[198:199], v[198:199], 1.0 op_sel_hi:[1,0]\n"
"v_pk_fma_f32 v[192:193], v[192:193], v[192:193], 1.0 op_sel_hi:[1,1,0]\n"
"v_pk_fma_f32 v[194:195], v[194:195], v[194:195], 1.0 op_sel_hi:[1,1,0]\n"
"v_pk_fma_f32 v[196:197], v[196:197], v[196:197], 1.0 op_sel_hi:[1,1,0]\n"
"v_pk_fma_f32 v[198:199], v[198:199], v[198:199], 1.0 op_sel_hi:[1,1,0]\n"
"v_rcp_f32 v192, v192\n"
"v_rcp_f32 v193, v193\n"
"v_rcp_f32 v194, v194\n"
"v_rcp_f32 v195, v195\n"
"v_rcp_f32 v196, v196\n"
"v_rcp_f32 v197, v197\n"
"v_rcp_f32 v198, v198\n"
"v_rcp_f32 v199, v199\n"
"v_pk_fma_f32 v[192:193], v[192:193], v[80:81], v[82:83]\n"
"v_pk_fma_f32 v[194:195], v[194:195], v[80:81], v[82:83]\n"
"v_pk_fma_f32 v[196:197], v[196:197], v[80:81], v[82:83]\n"
"v_pk_fma_f32 v[198:199], v[198:199], v[80:81], v[82:83]\n"
"v_pk_mul_f32 v[192:193], v[192:193], v[20:21]\n"
"v_pk_mul_f32 v[194:195], v[194:195], v[22:23]\n"
"v_pk_mul_f32 v[196:197], v[196:197], v[28:29]\n"
"v_pk_mul_f32 v[198:199], v[198:199], v[30:31]\n"
"v_cvt_pk_bf16_f32 v20, v192, v193\n"
"v_cvt_pk_bf16_f32 v21, v194, v195\n"
"v_cvt_pk_bf16_f32 v22, v196, v197\n"
"v_cvt_pk_bf16_f32 v23, v198, v199\n"
"buffer_store_dwordx4 v[20:23], v232, s[40:43], s73 offen offset:1024 sc1\n"
"s_waitcnt lgkmcnt(14)\n"
"v_mfma_f32_16x16x32_bf16 v[32:35], v[160:163], a[0:3], v[104:107]\n"
"s_add_u32 m0, s58, 0\n"
"s_add_u32 s60, s78, 196608\n"
"v_mfma_f32_16x16x32_bf16 v[32:35], v[164:167], a[8:11], v[32:35]\n"
"buffer_load_dwordx4 v228, s[28:31], s60 offen offset:0 lds\n"
"s_xor_b32 s16, s11, 0\n"
"v_mfma_f32_16x16x32_bf16 v[36:39], v[160:163], a[4:7], v[104:107]\n"
"ds_read_b64_tr_b16 v[160:161], v64 offset:16384\n"
"s_mul_i32 s17, s62, 229376\n"
"v_mfma_f32_16x16x32_bf16 v[36:39], v[164:167], a[12:15], v[36:39]\n"
"ds_read_b64_tr_b16 v[162:163], v64 offset:18432\n"
"v_mfma_f32_16x16x32_bf16 v[40:43], v[168:171], a[0:3], v[108:111]\n"
"ds_read_b64_tr_b16 v[164:165], v64 offset:20480\n"
"s_mul_i32 s15, s16, 57344\n"
"v_mfma_f32_16x16x32_bf16 v[40:43], v[172:175], a[8:11], v[40:43]\n"
"ds_read_b64_tr_b16 v[166:167], v64 offset:22528\n"
"buffer_load_dwordx4 v229, s[28:31], s60 offen offset:1024 lds\n"
"v_mfma_f32_16x16x32_bf16 v[44:47], v[168:171], a[4:7], v[108:111]\n"
"ds_read_b64_tr_b16 v[168:169], v65 offset:16384\n"
"s_add_u32 s17, s17, s15\n"
"v_mfma_f32_16x16x32_bf16 v[44:47], v[172:175], a[12:15], v[44:47]\n"
"ds_read_b64_tr_b16 v[170:171], v65 offset:18432\n"
"s_waitcnt lgkmcnt(14)\n"
"v_mfma_f32_16x16x32_bf16 v[48:51], v[176:179], a[0:3], v[112:115]\n"
"ds_read_b64_tr_b16 v[172:173], v65 offset:20480\n"
"v_add_u32 v116, s17, v232\n"
"v_mfma_f32_16x16x32_bf16 v[48:51], v[180:183], a[8:11], v[48:51]\n"
"ds_read_b64_tr_b16 v[174:175], v65 offset:22528\n"
"v_add_u32 v120, s17, v78\n"
"v_mfma_f32_16x16x32_bf16 v[52:55], v[176:179], a[4:7], v[112:115]\n"
"ds_read_b64_tr_b16 v[176:177], v66 offset:16384\n"
"buffer_load_dwordx4 v230, s[28:31], s60 offen offset:2048 lds\n"
"v_mfma_f32_16x16x32_bf16 v[52:55], v[180:183], a[12:15], v[52:55]\n"
"ds_read_b64_tr_b16 v[178:179], v66 offset:18432\n"
"v_mfma_f32_16x16x32_bf16 v[32:35], v[184:187], a[16:19], v[32:35]\n"
"ds_read_b64_tr_b16 v[180:181], v66 offset:20480\n"
"s_xor_b32 s16, s11, 1\n"
"v_mfma_f32_16x16x32_bf16 v[32:35], v[188:191], a[24:27], v[32:35]\n"
"ds_read_b64_tr_b16 v[182:183], v66 offset:22528\n"
"s_mul_i32 s17, s62, 229376\n"
"v_mfma_f32_16x16x32_bf16 v[36:39], v[184:187], a[20:23], v[36:39]\n"
"ds_read_b64_tr_b16 v[184:185], v64 offset:24576\n"
"s_mul_i32 s15, s16, 57344\n"
"v_mfma_f32_16x16x32_bf16 v[36:39], v[188:191], a[28:31], v[36:39]\n"
"ds_read_b64_tr_b16 v[186:187], v64 offset:26624\n"
"s_waitcnt lgkmcnt(14)\n"
"v_mfma_f32_16x16x32_bf16 v[40:43], v[144:147], a[16:19], v[40:43]\n"
"ds_read_b64_tr_b16 v[188:189], v64 offset:28672\n"
"buffer_load_dwordx4 v231, s[28:31], s60 offen offset:3072 lds\n"
"v_mfma_f32_16x16x32_bf16 v[40:43], v[148:151], a[24:27], v[40:43]\n"
"ds_read_b64_tr_b16 v[190:191], v64 offset:30720\n"
"s_add_u32 s17, s17, s15\n"
"v_mfma_f32_16x16x32_bf16 v[44:47], v[144:147], a[20:23], v[44:47]\n"
"ds_read_b64_tr_b16 v[144:145], v65 offset:24576\n"
"v_add_u32 v117, s17, v232\n"
"v_mfma_f32_16x16x32_bf16 v[44:47], v[148:151], a[28:31], v[44:47]\n"
"ds_read_b64_tr_b16 v[146:147], v65 offset:26624\n"
"v_mfma_f32_16x16x32_bf16 v[48:51], v[152:155], a[16:19], v[48:51]\n"
"ds_read_b64_tr_b16 v[148:149], v65 offset:28672\n"
"s_add_u32 m0, s58, 4096\n"
"v_mfma_f32_16x16x32_bf16 v[48:51], v[156:159], a[24:27], v[48:51]\n"
"ds_read_b64_tr_b16 v[150:151], v65 offset:30720\n"
"s_add_u32 s60, s78, 229376\n"
"v_mfma_f32_16x16x32_bf16 v[52:55], v[152:155], a[20:23], v[52:55]\n"
"ds_read_b64_tr_b16 v[152:153], v66 offset:24576\n"
"buffer_load_dwordx4 v228, s[28:31], s60 offen offset:0 lds\n"
"v_mfma_f32_16x16x32_bf16 v[52:55], v[156:159], a[28:31], v[52:55]\n"
"ds_read_b64_tr_b16 v[154:155], v66 offset:26624\n"
"s_waitcnt lgkmcnt(14)\n"
"v_mfma_f32_16x16x32_bf16 v[32:35], v[160:163], a[32:35], v[32:35]\n"
"ds_read_b64_tr_b16 v[156:157], v66 offset:28672\n"
"v_add_u32 v121, s17, v78\n"
"v_mfma_f32_16x16x32_bf16 v[32:35], v[164:167], a[40:43], v[32:35]\n"
"ds_read_b64_tr_b16 v[158:159], v66 offset:30720\n"
"buffer_load_dwordx4 v229, s[28:31], s60 offen offset:1024 lds\n"
"v_mfma_f32_16x16x32_bf16 v[36:39], v[160:163], a[36:39], v[36:39]\n"
"ds_read_b64_tr_b16 v[160:161], v64 offset:57344\n"
"s_xor_b32 s16, s11, 2\n"
"v_mfma_f32_16x16x32_bf16 v[36:39], v[164:167], a[44:47], v[36:39]\n"
"ds_read_b64_tr_b16 v[162:163], v64 offset:59392\n"
"v_mfma_f32_16x16x32_bf16 v[40:43], v[168:171], a[32:35], v[40:43]\n"
"ds_read_b64_tr_b16 v[164:165], v64 offset:61440\n"
"s_mul_i32 s17, s62, 229376\n"
"v_mfma_f32_16x16x32_bf16 v[40:43], v[172:175], a[40:43], v[40:43]\n"
"ds_read_b64_tr_b16 v[166:167], v64 offset:63488\n"
"s_mul_i32 s15, s16, 57344\n"
"v_mfma_f32_16x16x32_bf16 v[44:47], v[168:171], a[36:39], v[44:47]\n"
"ds_read_b64_tr_b16 v[168:169], v65 offset:57344\n"
"buffer_load_dwordx4 v230, s[28:31], s60 offen offset:2048 lds\n"
"v_mfma_f32_16x16x32_bf16 v[44:47], v[172:175], a[44:47], v[44:47]\n"
"ds_read_b64_tr_b16 v[170:171], v65 offset:59392\n"
"s_waitcnt lgkmcnt(14)\n"
"v_mfma_f32_16x16x32_bf16 v[48:51], v[176:179], a[32:35], v[48:51]\n"
"ds_read_b64_tr_b16 v[172:173], v65 offset:61440\n"
"s_add_u32 s17, s17, s15\n"
"v_mfma_f32_16x16x32_bf16 v[48:51], v[180:183], a[40:43], v[48:51]\n"
"ds_read_b64_tr_b16 v[174:175], v65 offset:63488\n"
"v_add_u32 v118, s17, v232\n"
"v_mfma_f32_16x16x32_bf16 v[52:55], v[176:179], a[36:39], v[52:55]\n"
"ds_read_b64_tr_b16 v[176:177], v66 offset:57344\n"
"v_add_u32 v122, s17, v78\n"
"v_mfma_f32_16x16x32_bf16 v[52:55], v[180:183], a[44:47], v[52:55]\n"
"ds_read_b64_tr_b16 v[178:179], v66 offset:59392\n"
"v_mfma_f32_16x16x32_bf16 v[32:35], v[184:187], a[48:51], v[32:35]\n"
"ds_read_b64_tr_b16 v[180:181], v66 offset:61440\n"
"buffer_load_dwordx4 v231, s[28:31], s60 offen offset:3072 lds\n"
"v_mfma_f32_16x16x32_bf16 v[32:35], v[188:191], a[56:59], v[32:35]\n"
"ds_read_b64_tr_b16 v[182:183], v66 offset:63488\n"
"s_xor_b32 s16, s11, 3\n"
"v_mfma_f32_16x16x32_bf16 v[36:39], v[184:187], a[52:55], v[36:39]\n"
"s_waitcnt vmcnt(8)\n"
"buffer_store_dwordx2 v[220:221], v237, s[40:43], s34 offen sc1\n"
"ds_read_b64_tr_b16 v[184:185], v68 offset:0\n"
"v_mfma_f32_16x16x32_bf16 v[36:39], v[188:191], a[60:63], v[36:39]\n"
"ds_read_b64_tr_b16 v[186:187], v68 offset:2048\n"
"s_waitcnt lgkmcnt(14)\n"
"v_mfma_f32_16x16x32_bf16 v[40:43], v[144:147], a[48:51], v[40:43]\n"
"ds_read_b64_tr_b16 v[188:189], v68 offset:4096\n"
"s_add_u32 m0, s58, 8192\n"
"v_mfma_f32_16x16x32_bf16 v[40:43], v[148:151], a[56:59], v[40:43]\n"
"ds_read_b64_tr_b16 v[190:191], v68 offset:6144\n"
"s_add_u32 s60, s79, 0\n"
"v_mfma_f32_16x16x32_bf16 v[44:47], v[144:147], a[52:55], v[44:47]\n"
"ds_read_b64_tr_b16 v[144:145], v69 offset:0\n"
"buffer_load_dwordx4 v228, s[28:31], s60 offen offset:0 lds\n"
"v_mfma_f32_16x16x32_bf16 v[44:47], v[148:151], a[60:63], v[44:47]\n"
"ds_read_b64_tr_b16 v[146:147], v69 offset:2048\n"
"v_mfma_f32_16x16x32_bf16 v[48:51], v[152:155], a[48:51], v[48:51]\n"
"ds_read_b64_tr_b16 v[148:149], v69 offset:4096\n"
"s_mul_i32 s17, s62, 229376\n"
"v_mfma_f32_16x16x32_bf16 v[48:51], v[156:159], a[56:59], v[48:51]\n"
"ds_read_b64_tr_b16 v[150:151], v69 offset:6144\n"
"buffer_load_dwordx4 v229, s[28:31], s60 offen offset:1024 lds\n"
"v_mfma_f32_16x16x32_bf16 v[52:55], v[152:155], a[52:55], v[52:55]\n"
"ds_read_b64_tr_b16 v[152:153], v70 offset:0\n"
"s_mul_i32 s15, s16, 57344\n"
"v_mfma_f32_16x16x32_bf16 v[52:55], v[156:159], a[60:63], v[52:55]\n"
"ds_read_b64_tr_b16 v[154:155], v70 offset:2048\n"
"s_waitcnt lgkmcnt(14)\n"
"v_mfma_f32_16x16x32_bf16 v[32:35], v[160:163], a[64:67], v[32:35]\n"
"ds_read_b64_tr_b16 v[156:157], v70 offset:4096\n"
"s_add_u32 s17, s17, s15\n"
"v_mfma_f32_16x16x32_bf16 v[32:35], v[164:167], a[72:75], v[32:35]\n"
"ds_read_b64_tr_b16 v[158:159], v70 offset:6144\n"
"v_add_u32 v119, s17, v232\n"
"v_mfma_f32_16x16x32_bf16 v[36:39], v[160:163], a[68:71], v[36:39]\n"
"ds_read_b64_tr_b16 v[160:161], v72 offset:8192\n"
"buffer_load_dwordx4 v230, s[28:31], s60 offen offset:2048 lds\n"
"v_mfma_f32_16x16x32_bf16 v[36:39], v[164:167], a[76:79], v[36:39]\n"
"ds_read_b64_tr_b16 v[162:163], v72 offset:10240\n"
"v_mfma_f32_16x16x32_bf16 v[40:43], v[168:171], a[64:67], v[40:43]\n"
"ds_read_b64_tr_b16 v[164:165], v72 offset:12288\n"
"v_add_u32 v123, s17, v78\n"
"v_mfma_f32_16x16x32_bf16 v[40:43], v[172:175], a[72:75], v[40:43]\n"
"ds_read_b64_tr_b16 v[166:167], v72 offset:14336\n"
"v_bfe_u32 v234, v232, 4, 4\n"
"v_mfma_f32_16x16x32_bf16 v[44:47], v[168:171], a[68:71], v[44:47]\n"
"ds_read_b64_tr_b16 v[168:169], v73 offset:8192\n"
"v_lshl_add_u32 v124, v234, 10, v86\n"
"v_mfma_f32_16x16x32_bf16 v[44:47], v[172:175], a[76:79], v[44:47]\n"
"ds_read_b64_tr_b16 v[170:171], v73 offset:10240\n"
"s_waitcnt lgkmcnt(14)\n"
"v_mfma_f32_16x16x32_bf16 v[48:51], v[176:179], a[64:67], v[48:51]\n"
"ds_read_b64_tr_b16 v[172:173], v73 offset:12288\n"
"buffer_load_dwordx4 v231, s[28:31], s60 offen offset:3072 lds\n"
"v_mfma_f32_16x16x32_bf16 v[48:51], v[180:183], a[72:75], v[48:51]\n"
"ds_read_b64_tr_b16 v[174:175], v73 offset:14336\n"
"s_lshl_b32 s17, s62, 16\n"
"v_mfma_f32_16x16x32_bf16 v[52:55], v[176:179], a[68:71], v[52:55]\n"
"ds_read_b64_tr_b16 v[176:177], v74 offset:8192\n"
"s_lshl_b32 s15, s11, 14\n"
"v_mfma_f32_16x16x32_bf16 v[52:55], v[180:183], a[76:79], v[52:55]\n"
"ds_read_b64_tr_b16 v[178:179], v74 offset:10240\n"
"v_mfma_f32_16x16x32_bf16 v[32:35], v[184:187], a[80:83], v[32:35]\n"
"ds_read_b64_tr_b16 v[180:181], v74 offset:12288\n"
"s_add_u32 m0, s58, 12288\n"
"v_mfma_f32_16x16x32_bf16 v[32:35], v[188:191], a[88:91], v[32:35]\n"
"ds_read_b64_tr_b16 v[182:183], v74 offset:14336\n"
"s_add_u32 s60, s79, 49152\n"
"v_mfma_f32_16x16x32_bf16 v[36:39], v[184:187], a[84:87], v[36:39]\n"
"ds_read_b64_tr_b16 v[184:185], v72 offset:16384\n"
"buffer_load_dwordx4 v228, s[28:31], s60 offen offset:0 lds\n"
"v_mfma_f32_16x16x32_bf16 v[36:39], v[188:191], a[92:95], v[36:39]\n"
"ds_read_b64_tr_b16 v[186:187], v72 offset:18432\n"
"s_waitcnt lgkmcnt(14)\n"
"v_mfma_f32_16x16x32_bf16 v[40:43], v[144:147], a[80:83], v[40:43]\n"
"ds_read_b64_tr_b16 v[188:189], v72 offset:20480\n"
"s_add_u32 s17, s17, s15\n"
"v_mfma_f32_16x16x32_bf16 v[40:43], v[148:151], a[88:91], v[40:43]\n"
"ds_read_b64_tr_b16 v[190:191], v72 offset:22528\n"
"buffer_load_dwordx4 v229, s[28:31], s60 offen offset:1024 lds\n"
"v_mfma_f32_16x16x32_bf16 v[44:47], v[144:147], a[84:87], v[44:47]\n"
"ds_read_b64_tr_b16 v[144:145], v73 offset:16384\n"
"s_lshl_b32 s15, s63, 7\n"
"v_mfma_f32_16x16x32_bf16 v[44:47], v[148:151], a[92:95], v[44:47]\n"
"ds_read_b64_tr_b16 v[146:147], v73 offset:18432\n"
"v_mfma_f32_16x16x32_bf16 v[48:51], v[152:155], a[80:83], v[48:51]\n"
"ds_read_b64_tr_b16 v[148:149], v73 offset:20480\n"
"s_add_u32 s17, s17, s15\n"
"v_mfma_f32_16x16x32_bf16 v[48:51], v[156:159], a[88:91], v[48:51]\n"
"ds_read_b64_tr_b16 v[150:151], v73 offset:22528\n"
"v_add_u32 v124, s17, v124\n"
"v_mfma_f32_16x16x32_bf16 v[52:55], v[152:155], a[84:87], v[52:55]\n"
"ds_read_b64_tr_b16 v[152:153], v74 offset:16384\n"
"buffer_load_dwordx4 v230, s[28:31], s60 offen offset:2048 lds\n"
"v_mfma_f32_16x16x32_bf16 v[52:55], v[156:159], a[92:95], v[52:55]\n"
"ds_read_b64_tr_b16 v[154:155], v74 offset:18432\n"
"s_waitcnt lgkmcnt(14)\n"
"v_mfma_f32_16x16x32_bf16 v[32:35], v[160:163], a[96:99], v[32:35]\n"
"ds_read_b64_tr_b16 v[156:157], v74 offset:20480\n"
"v_and_b32 v234, 8, v86\n"
"v_mfma_f32_16x16x32_bf16 v[32:35], v[164:167], a[104:107], v[32:35]\n"
"ds_read_b64_tr_b16 v[158:159], v74 offset:22528\n"
"v_mad_u32_u24 v124, v234, 3, v124\n"
"v_mfma_f32_16x16x32_bf16 v[36:39], v[160:163], a[100:103], v[36:39]\n"
"v_lshlrev_b32 v125, 1, v86\n"
"s_lshl_b32 s59, s66, 16\n"
"v_mfma_f32_16x16x32_bf16 v[36:39], v[164:167], a[108:111], v[36:39]\n"
"buffer_load_dwordx4 v231, s[28:31], s60 offen offset:3072 lds\n"
"v_mfma_f32_16x16x32_bf16 v[40:43], v[168:171], a[96:99], v[40:43]\n"
"s_lshl_b32 s15, s63, 8\n"
"s_add_u32 s59, s59, s15\n"
"v_mfma_f32_16x16x32_bf16 v[40:43], v[172:175], a[104:107], v[40:43]\n"
"s_add_u32 s59, s59, 530432\n"
"s_lshl_b32 s74, s62, 16\n"
"v_mfma_f32_16x16x32_bf16 v[44:47], v[168:171], a[100:103], v[44:47]\n"
"s_lshl_b32 s15, s11, 14\n"
"s_add_u32 s74, s74, s15\n"
"v_mfma_f32_16x16x32_bf16 v[44:47], v[172:175], a[108:111], v[44:47]\n"
"s_lshl_b32 s15, s63, 11\n"
"s_waitcnt lgkmcnt(12)\n"
"v_mfma_f32_16x16x32_bf16 v[48:51], v[176:179], a[96:99], v[48:51]\n"
"s_add_u32 s74, s74, s15\n"
"s_add_u32 s74, s74, 56623104\n"
"v_mfma_f32_16x16x32_bf16 v[48:51], v[180:183], a[104:107], v[48:51]\n"
"s_mul_i32 s68, s11, 14336\n"
"s_add_u32 s68, s68, 58720256\n"
"v_mfma_f32_16x16x32_bf16 v[52:55], v[176:179], a[100:103], v[52:55]\n"
"s_add_u32 s69, s68, 3584\n"
"s_add_u32 s70, s68, 7168\n"
"v_mfma_f32_16x16x32_bf16 v[52:55], v[180:183], a[108:111], v[52:55]\n"
"s_add_u32 s71, s68, 10752\n"
"s_waitcnt lgkmcnt(8)\n"
"v_mfma_f32_16x16x32_bf16 v[32:35], v[184:187], a[112:115], v[32:35]\n"
"s_add_u32 s34, s34, 262144\n"
"v_mfma_f32_16x16x32_bf16 v[32:35], v[188:191], a[120:123], v[32:35]\n"
"v_mfma_f32_16x16x32_bf16 v[36:39], v[184:187], a[116:119], v[36:39]\n"
"v_mfma_f32_16x16x32_bf16 v[36:39], v[188:191], a[124:127], v[36:39]\n"
"s_waitcnt lgkmcnt(4)\n"
"v_mfma_f32_16x16x32_bf16 v[40:43], v[144:147], a[112:115], v[40:43]\n"
"v_mfma_f32_16x16x32_bf16 v[40:43], v[148:151], a[120:123], v[40:43]\n"
"v_mfma_f32_16x16x32_bf16 v[44:47], v[144:147], a[116:119], v[44:47]\n"
"v_mfma_f32_16x16x32_bf16 v[44:47], v[148:151], a[124:127], v[44:47]\n"
"s_waitcnt lgkmcnt(0)\n"
"v_mfma_f32_16x16x32_bf16 v[48:51], v[152:155], a[112:115], v[48:51]\n"
"v_mfma_f32_16x16x32_bf16 v[48:51], v[156:159], a[120:123], v[48:51]\n"
"v_mfma_f32_16x16x32_bf16 v[52:55], v[152:155], a[116:119], v[52:55]\n"
"v_mfma_f32_16x16x32_bf16 v[52:55], v[156:159], a[124:127], v[52:55]\n"
"s_barrier\n"
"s_add_u32 m0, s58, 16384\n"
"s_add_u32 s60, s79, 98304\n"
"buffer_load_dwordx4 v228, s[28:31], s60 offen offset:0 lds\n"
"buffer_load_dwordx4 v229, s[28:31], s60 offen offset:1024 lds\n"
"buffer_load_dwordx4 v230, s[28:31], s60 offen offset:2048 lds\n"
"buffer_load_dwordx4 v231, s[28:31], s60 offen offset:3072 lds\n"
"v_exp_f32 v192, v32\n"
"v_exp_f32 v193, v33\n"
"v_exp_f32 v194, v34\n"
"v_exp_f32 v195, v35\n"
"v_exp_f32 v196, v40\n"
"v_exp_f32 v197, v41\n"
"v_exp_f32 v198, v42\n"
"v_exp_f32 v199, v43\n"
"v_pk_add_f32 v[192:193], v[192:193], 1.0 op_sel_hi:[1,0]\n"
"v_pk_add_f32 v[194:195], v[194:195], 1.0 op_sel_hi:[1,0]\n"
"v_pk_add_f32 v[196:197], v[196:197], 1.0 op_sel_hi:[1,0]\n"
"v_pk_add_f32 v[198:199], v[198:199], 1.0 op_sel_hi:[1,0]\n"
"v_pk_fma_f32 v[192:193], v[192:193], v[192:193], 1.0 op_sel_hi:[1,1,0]\n"
"v_pk_fma_f32 v[194:195], v[194:195], v[194:195], 1.0 op_sel_hi:[1,1,0]\n"
"v_pk_fma_f32 v[196:197], v[196:197], v[196:197], 1.0 op_sel_hi:[1,1,0]\n"
"v_pk_fma_f32 v[198:199], v[198:199], v[198:199], 1.0 op_sel_hi:[1,1,0]\n"
"v_rcp_f32 v192, v192\n"
"v_rcp_f32 v193, v193\n"
"v_rcp_f32 v194, v194\n"
"v_rcp_f32 v195, v195\n"
"v_rcp_f32 v196, v196\n"
"v_rcp_f32 v197, v197\n"
"v_rcp_f32 v198, v198\n"
"v_rcp_f32 v199, v199\n"
"v_pk_fma_f32 v[192:193], v[192:193], v[80:81], v[82:83]\n"
"v_pk_fma_f32 v[194:195], v[194:195], v[80:81], v[82:83]\n"
"v_pk_fma_f32 v[196:197], v[196:197], v[80:81], v[82:83]\n"
"v_pk_fma_f32 v[198:199], v[198:199], v[80:81], v[82:83]\n"
"v_pk_mul_f32 v[192:193], v[192:193], v[32:33]\n"
"v_pk_mul_f32 v[194:195], v[194:195], v[34:35]\n"
"v_pk_mul_f32 v[196:197], v[196:197], v[40:41]\n"
"v_pk_mul_f32 v[198:199], v[198:199], v[42:43]\n"
"v_cvt_pk_bf16_f32 v32, v192, v193\n"
"v_cvt_pk_bf16_f32 v33, v194, v195\n"
"v_cvt_pk_bf16_f32 v34, v196, v197\n"
"v_cvt_pk_bf16_f32 v35, v198, v199\n"
"buffer_store_dwordx4 v[32:35], v232, s[40:43], s72 offen offset:2048 sc1\n"
"s_add_u32 m0, s58, 20480\n"
"s_add_u32 s60, s79, 147456\n"
"buffer_load_dwordx4 v228, s[28:31], s60 offen offset:0 lds\n"
"buffer_load_dwordx4 v229, s[28:31], s60 offen offset:1024 lds\n"
"buffer_load_dwordx4 v230, s[28:31], s60 offen offset:2048 lds\n"
"buffer_load_dwordx4 v231, s[28:31], s60 offen offset:3072 lds\n"
"buffer_load_dwordx2 v[222:223], v236, s[40:43], s35 offen sc1\n"
"v_exp_f32 v192, v36\n"
"v_exp_f32 v193, v37\n"
"v_exp_f32 v194, v38\n"
"v_exp_f32 v195, v39\n"
"v_exp_f32 v196, v44\n"
"v_exp_f32 v197, v45\n"
"v_exp_f32 v198, v46\n"
"v_exp_f32 v199, v47\n"
"v_pk_add_f32 v[192:193], v[192:193], 1.0 op_sel_hi:[1,0]\n"
"v_pk_add_f32 v[194:195], v[194:195], 1.0 op_sel_hi:[1,0]\n"
"v_pk_add_f32 v[196:197], v[196:197], 1.0 op_sel_hi:[1,0]\n"
"v_pk_add_f32 v[198:199], v[198:199], 1.0 op_sel_hi:[1,0]\n"
"v_pk_fma_f32 v[192:193], v[192:193], v[192:193], 1.0 op_sel_hi:[1,1,0]\n"
"v_pk_fma_f32 v[194:195], v[194:195], v[194:195], 1.0 op_sel_hi:[1,1,0]\n"
"v_pk_fma_f32 v[196:197], v[196:197], v[196:197], 1.0 op_sel_hi:[1,1,0]\n"
"v_pk_fma_f32 v[198:199], v[198:199], v[198:199], 1.0 op_sel_hi:[1,1,0]\n"
"v_rcp_f32 v192, v192\n"
"v_rcp_f32 v193, v193\n"
"v_rcp_f32 v194, v194\n"
"v_rcp_f32 v195, v195\n"
"v_rcp_f32 v196, v196\n"
"v_rcp_f32 v197, v197\n"
"v_rcp_f32 v198, v198\n"
"v_rcp_f32 v199, v199\n"
"v_pk_fma_f32 v[192:193], v[192:193], v[80:81], v[82:83]\n"
"v_pk_fma_f32 v[194:195], v[194:195], v[80:81], v[82:83]\n"
"v_pk_fma_f32 v[196:197], v[196:197], v[80:81], v[82:83]\n"
"v_pk_fma_f32 v[198:199], v[198:199], v[80:81], v[82:83]\n"
"v_pk_mul_f32 v[192:193], v[192:193], v[36:37]\n"
"v_pk_mul_f32 v[194:195], v[194:195], v[38:39]\n"
"v_pk_mul_f32 v[196:197], v[196:197], v[44:45]\n"
"v_pk_mul_f32 v[198:199], v[198:199], v[46:47]\n"
"v_cvt_pk_bf16_f32 v36, v192, v193\n"
"v_cvt_pk_bf16_f32 v37, v194, v195\n"
"v_cvt_pk_bf16_f32 v38, v196, v197\n"
"v_cvt_pk_bf16_f32 v39, v198, v199\n"
"buffer_store_dwordx4 v[36:39], v232, s[40:43], s73 offen offset:2048 sc1\n"
"v_exp_f32 v192, v48\n"
"v_exp_f32 v193, v49\n"
"v_exp_f32 v194, v50\n"
"v_exp_f32 v195, v51\n"
"v_pk_add_f32 v[192:193], v[192:193], 1.0 op_sel_hi:[1,0]\n"
"v_pk_add_f32 v[194:195], v[194:195], 1.0 op_sel_hi:[1,0]\n"
"v_pk_fma_f32 v[192:193], v[192:193], v[192:193], 1.0 op_sel_hi:[1,1,0]\n"
"v_pk_fma_f32 v[194:195], v[194:195], v[194:195], 1.0 op_sel_hi:[1,1,0]\n"
"v_rcp_f32 v192, v192\n"
"v_rcp_f32 v193, v193\n"
"v_rcp_f32 v194, v194\n"
"v_rcp_f32 v195, v195\n"
"v_pk_fma_f32 v[192:193], v[192:193], v[80:81], v[82:83]\n"
"v_pk_fma_f32 v[194:195], v[194:195], v[80:81], v[82:83]\n"
"v_pk_mul_f32 v[192:193], v[192:193], v[48:49]\n"
"v_pk_mul_f32 v[194:195], v[194:195], v[50:51]\n"
"v_cvt_pk_bf16_f32 v48, v192, v193\n"
"v_cvt_pk_bf16_f32 v49, v194, v195\n"
"buffer_store_dwordx2 v[48:49], v78, s[40:43], s72 offen offset:3072 sc1\n"
"v_exp_f32 v192, v52\n"
"v_exp_f32 v193, v53\n"
"v_exp_f32 v194, v54\n"
"v_exp_f32 v195, v55\n"
"v_pk_add_f32 v[192:193], v[192:193], 1.0 op_sel_hi:[1,0]\n"
"v_pk_add_f32 v[194:195], v[194:195], 1.0 op_sel_hi:[1,0]\n"
"v_pk_fma_f32 v[192:193], v[192:193], v[192:193], 1.0 op_sel_hi:[1,1,0]\n"
"v_pk_fma_f32 v[194:195], v[194:195], v[194:195], 1.0 op_sel_hi:[1,1,0]\n"
"v_rcp_f32 v192, v192\n"
"v_rcp_f32 v193, v193\n"
"v_rcp_f32 v194, v194\n"
"v_rcp_f32 v195, v195\n"
"v_pk_fma_f32 v[192:193], v[192:193], v[80:81], v[82:83]\n"
"v_pk_fma_f32 v[194:195], v[194:195], v[80:81], v[82:83]\n"
"v_pk_mul_f32 v[192:193], v[192:193], v[52:53]\n"
"v_pk_mul_f32 v[194:195], v[194:195], v[54:55]\n"
"v_cvt_pk_bf16_f32 v52, v192, v193\n"
"v_cvt_pk_bf16_f32 v53, v194, v195\n"
"buffer_store_dwordx2 v[52:53], v78, s[40:43], s73 offen offset:3072 sc1\n"
"s_waitcnt vmcnt(3)\n"
"v_cmp_ne_u64 vcc, s[6:7], v[222:223]\n"
"s_nop 0\n"
"s_cbranch_vccnz Lpa_%=\n"
"Lpa_ok_%=:\n"
"buffer_load_dwordx4 a[0:3], v116, s[40:43], s68 offen offset:0 sc1\n"
"buffer_load_dwordx4 a[4:7], v116, s[40:43], s68 offen offset:1024 sc1\n"
"buffer_load_dwordx4 a[8:11], v117, s[40:43], s68 offen offset:0 sc1\n"
"buffer_load_dwordx4 a[12:15], v117, s[40:43], s68 offen offset:1024 sc1\n"
"buffer_load_dwordx4 a[16:19], v118, s[40:43], s68 offen offset:0 sc1\n"
"buffer_load_dwordx4 a[20:23], v118, s[40:43], s68 offen offset:1024 sc1\n"
"s_waitcnt vmcnt(6)\n"
"buffer_store_dwordx2 v[220:221], v237, s[40:43], s34 offen sc1\n"
"buffer_load_dwordx4 a[24:27], v119, s[40:43], s68 offen offset:0 sc1\n"
"buffer_load_dwordx4 a[28:31], v119, s[40:43], s68 offen offset:1024 sc1\n"
"buffer_load_dwordx4 a[32:35], v116, s[40:43], s69 offen offset:0 sc1\n"
"buffer_load_dwordx4 a[36:39], v116, s[40:43], s69 offen offset:1024 sc1\n"
"buffer_load_dwordx4 a[40:43], v117, s[40:43], s69 offen offset:0 sc1\n"
"buffer_load_dwordx4 a[44:47], v117, s[40:43], s69 offen offset:1024 sc1\n"
"buffer_load_dwordx4 a[48:51], v118, s[40:43], s69 offen offset:0 sc1\n"
"s_waitcnt vmcnt(10)\n"
"v_mfma_f32_16x16x32_bf16 v[0:3], a[128:131], a[0:3], 0\n"
"buffer_load_dwordx4 a[52:55], v118, s[40:43], s69 offen offset:1024 sc1\n"
"ds_read_b64_tr_b16 v[160:161], v224 offset:32768\n"
"v_mfma_f32_16x16x32_bf16 v[0:3], a[132:135], a[4:7], v[0:3]\n"
"ds_read_b64_tr_b16 v[162:163], v224 offset:34816\n"
"s_add_u32 s35, s35, 262144\n"
"v_mfma_f32_16x16x32_bf16 v[4:7], a[128:131], a[8:11], 0\n"
"ds_read_b64_tr_b16 v[164:165], v224 offset:36864\n"
"v_add_u32 v233, s58, v232\n"
"v_mfma_f32_16x16x32_bf16 v[4:7], a[132:135], a[12:15], v[4:7]\n"
"ds_read_b64_tr_b16 v[166:167], v224 offset:38912\n"
"s_waitcnt vmcnt(6)\n"
"v_mfma_f32_16x16x32_bf16 v[8:11], a[128:131], a[16:19], 0\n"
"buffer_load_dwordx4 a[56:59], v119, s[40:43], s69 offen offset:0 sc1\n"
"ds_read_b64_tr_b16 v[168:169], v225 offset:32768\n"
"v_mfma_f32_16x16x32_bf16 v[8:11], a[132:135], a[20:23], v[8:11]\n"
"ds_read_b64_tr_b16 v[170:171], v225 offset:34816\n"
"s_xor_b32 s16, s11, 1\n"
"v_mfma_f32_16x16x32_bf16 v[12:15], a[128:131], a[24:27], 0\n"
"ds_read_b64_tr_b16 v[172:173], v225 offset:36864\n"
"s_mul_i32 s16, s16, 40960\n"
"v_mfma_f32_16x16x32_bf16 v[12:15], a[132:135], a[28:31], v[12:15]\n"
"ds_read_b64_tr_b16 v[174:175], v225 offset:38912\n"
"v_add_u32 v65, s16, v232\n"
"v_mfma_f32_16x16x32_bf16 v[16:19], a[136:139], a[0:3], 0\n"
"buffer_load_dwordx4 a[60:63], v119, s[40:43], s69 offen offset:1024 sc1\n"
"ds_read_b64_tr_b16 v[176:177], v226 offset:32768\n"
"v_mfma_f32_16x16x32_bf16 v[16:19], a[140:143], a[4:7], v[16:19]\n"
"ds_read_b64_tr_b16 v[178:179], v226 offset:34816\n"
"s_xor_b32 s16, s11, 2\n"
"v_mfma_f32_16x16x32_bf16 v[20:23], a[136:139], a[8:11], 0\n"
"ds_read_b64_tr_b16 v[180:181], v226 offset:36864\n"
"s_mul_i32 s16, s16, 40960\n"
"v_mfma_f32_16x16x32_bf16 v[20:23], a[140:143], a[12:15], v[20:23]\n"
"ds_read_b64_tr_b16 v[182:183], v226 offset:38912\n"
"v_add_u32 v66, s16, v232\n"
"v_mfma_f32_16x16x32_bf16 v[24:27], a[136:139], a[16:19], 0\n"
"buffer_load_dwordx4 a[64:67], v116, s[40:43], s70 offen offset:0 sc1\n"
"ds_read_b64_tr_b16 v[184:185], v227 offset:32768\n"
"v_mfma_f32_16x16x32_bf16 v[24:27], a[140:143], a[20:23], v[24:27]\n"
"ds_read_b64_tr_b16 v[186:187], v227 offset:34816\n"
"s_xor_b32 s16, s11, 3\n"
"v_mfma_f32_16x16x32_bf16 v[28:31], a[136:139], a[24:27], 0\n"
"ds_read_b64_tr_b16 v[188:189], v227 offset:36864\n"
"s_mul_i32 s16, s16, 40960\n"
"v_mfma_f32_16x16x32_bf16 v[28:31], a[140:143], a[28:31], v[28:31]\n"
"ds_read_b64_tr_b16 v[190:191], v227 offset:38912\n"
"v_add_u32 v67, s16, v232\n"
"v_mfma_f32_16x16x32_bf16 v[32:35], a[144:147], a[0:3], 0\n"
"buffer_load_dwordx4 a[68:71], v116, s[40:43], s70 offen offset:1024 sc1\n"
"v_mfma_f32_16x16x32_bf16 v[32:35], a[148:151], a[4:7], v[32:35]\n"
"v_mfma_f32_16x16x32_bf16 v[36:39], a[144:147], a[8:11], 0\n"
"v_mfma_f32_16x16x32_bf16 v[36:39], a[148:151], a[12:15], v[36:39]\n"
"v_mfma_f32_16x16x32_bf16 v[40:43], a[144:147], a[16:19], 0\n"
"buffer_load_dwordx4 a[72:75], v117, s[40:43], s70 offen offset:0 sc1\n"
"v_mfma_f32_16x16x32_bf16 v[40:43], a[148:151], a[20:23], v[40:43]\n"
"v_mfma_f32_16x16x32_bf16 v[44:47], a[144:147], a[24:27], 0\n"
"v_mfma_f32_16x16x32_bf16 v[44:47], a[148:151], a[28:31], v[44:47]\n"
"v_mfma_f32_16x16x32_bf16 v[48:51], a[152:155], a[0:3], 0\n"
"buffer_load_dwordx4 a[76:79], v117, s[40:43], s70 offen offset:1024 sc1\n"
"v_mfma_f32_16x16x32_bf16 v[48:51], a[156:159], a[4:7], v[48:51]\n"
"v_mfma_f32_16x16x32_bf16 v[52:55], a[152:155], a[8:11], 0\n"
"v_mfma_f32_16x16x32_bf16 v[52:55], a[156:159], a[12:15], v[52:55]\n"
"v_mfma_f32_16x16x32_bf16 v[56:59], a[152:155], a[16:19], 0\n"
"buffer_load_dwordx4 a[80:83], v118, s[40:43], s70 offen offset:0 sc1\n"
"v_mfma_f32_16x16x32_bf16 v[56:59], a[156:159], a[20:23], v[56:59]\n"
"v_mfma_f32_16x16x32_bf16 v[60:63], a[152:155], a[24:27], 0\n"
"v_mfma_f32_16x16x32_bf16 v[60:63], a[156:159], a[28:31], v[60:63]\n"
"s_waitcnt vmcnt(9)\n"
"v_mfma_f32_16x16x32_bf16 v[0:3], a[160:163], a[32:35], v[0:3]\n"
"buffer_load_dwordx4 a[84:87], v118, s[40:43], s70 offen offset:1024 sc1\n"
"v_mfma_f32_16x16x32_bf16 v[0:3], a[164:167], a[36:39], v[0:3]\n"
"v_mfma_f32_16x16x32_bf16 v[4:7], a[160:163], a[40:43], v[4:7]\n"
"v_mfma_f32_16x16x32_bf16 v[4:7], a[164:167], a[44:47], v[4:7]\n"
"s_waitcnt vmcnt(6)\n"
"v_mfma_f32_16x16x32_bf16 v[8:11], a[160:163], a[48:51], v[8:11]\n"
"buffer_load_dwordx4 a[88:91], v119, s[40:43], s70 offen offset:0 sc1\n"
"v_mfma_f32_16x16x32_bf16 v[8:11], a[164:167], a[52:55], v[8:11]\n"
"v_mfma_f32_16x16x32_bf16 v[12:15], a[160:163], a[56:59], v[12:15]\n"
"v_mfma_f32_16x16x32_bf16 v[12:15], a[164:167], a[60:63], v[12:15]\n"
"v_mfma_f32_16x16x32_bf16 v[16:19], a[168:171], a[32:35], v[16:19]\n"
"buffer_load_dwordx4 a[92:95], v119, s[40:43], s70 offen offset:1024 sc1\n"
"v_mfma_f32_16x16x32_bf16 v[16:19], a[172:175], a[36:39], v[16:19]\n"
"v_mfma_f32_16x16x32_bf16 v[20:23], a[168:171], a[40:43], v[20:23]\n"
"v_mfma_f32_16x16x32_bf16 v[20:23], a[172:175], a[44:47], v[20:23]\n"
"v_mfma_f32_16x16x32_bf16 v[24:27], a[168:171], a[48:51], v[24:27]\n"
"buffer_load_dwordx4 a[96:99], v116, s[40:43], s71 offen offset:0 sc1\n"
"v_mfma_f32_16x16x32_bf16 v[24:27], a[172:175], a[52:55], v[24:27]\n"
"v_mfma_f32_16x16x32_bf16 v[28:31], a[168:171], a[56:59], v[28:31]\n"
"v_mfma_f32_16x16x32_bf16 v[28:31], a[172:175], a[60:63], v[28:31]\n"
"v_mfma_f32_16x16x32_bf16 v[32:35], a[176:179], a[32:35], v[32:35]\n"
"buffer_load_dwordx4 a[100:103], v116, s[40:43], s71 offen offset:1024 sc1\n"
"v_mfma_f32_16x16x32_bf16 v[32:35], a[180:183], a[36:39], v[32:35]\n"
"v_mfma_f32_16x16x32_bf16 v[36:39], a[176:179], a[40:43], v[36:39]\n"
"v_mfma_f32_16x16x32_bf16 v[36:39], a[180:183], a[44:47], v[36:39]\n"
"v_mfma_f32_16x16x32_bf16 v[40:43], a[176:179], a[48:51], v[40:43]\n"
"buffer_load_dwordx4 a[104:107], v117, s[40:43], s71 offen offset:0 sc1\n"
"v_mfma_f32_16x16x32_bf16 v[40:43], a[180:183], a[52:55], v[40:43]\n"
"v_mfma_f32_16x16x32_bf16 v[44:47], a[176:179], a[56:59], v[44:47]\n"
"v_mfma_f32_16x16x32_bf16 v[44:47], a[180:183], a[60:63], v[44:47]\n"
"v_mfma_f32_16x16x32_bf16 v[48:51], a[184:187], a[32:35], v[48:51]\n"
"buffer_load_dwordx4 a[108:111], v117, s[40:43], s71 offen offset:1024 sc1\n"
"v_mfma_f32_16x16x32_bf16 v[48:51], a[188:191], a[36:39], v[48:51]\n"
"v_mfma_f32_16x16x32_bf16 v[52:55], a[184:187], a[40:43], v[52:55]\n"
"v_mfma_f32_16x16x32_bf16 v[52:55], a[188:191], a[44:47], v[52:55]\n"
"v_mfma_f32_16x16x32_bf16 v[56:59], a[184:187], a[48:51], v[56:59]\n"
"buffer_load_dwordx4 a[112:115], v118, s[40:43], s71 offen offset:0 sc1\n"
"v_mfma_f32_16x16x32_bf16 v[56:59], a[188:191], a[52:55], v[56:59]\n"
"v_mfma_f32_16x16x32_bf16 v[60:63], a[184:187], a[56:59], v[60:63]\n"
"v_mfma_f32_16x16x32_bf16 v[60:63], a[188:191], a[60:63], v[60:63]\n"
"s_waitcnt vmcnt(9) lgkmcnt(12)\n"
"v_mfma_f32_16x16x32_bf16 v[0:3], v[160:163], a[64:67], v[0:3]\n"
"buffer_load_dwordx4 a[116:119], v118, s[40:43], s71 offen offset:1024 sc1\n"
"v_mfma_f32_16x16x32_bf16 v[0:3], v[164:167], a[68:71], v[0:3]\n"
"v_mfma_f32_16x16x32_bf16 v[4:7], v[160:163], a[72:75], v[4:7]\n"
"v_mfma_f32_16x16x32_bf16 v[4:7], v[164:167], a[76:79], v[4:7]\n"
"s_waitcnt vmcnt(6)\n"
"v_mfma_f32_16x16x32_bf16 v[8:11], v[160:163], a[80:83], v[8:11]\n"
"buffer_load_dwordx4 a[120:123], v119, s[40:43], s71 offen offset:0 sc1\n"
"v_mfma_f32_16x16x32_bf16 v[8:11], v[164:167], a[84:87], v[8:11]\n"
"v_mfma_f32_16x16x32_bf16 v[12:15], v[160:163], a[88:91], v[12:15]\n"
"ds_read_b64_tr_b16 v[160:161], v224 offset:0\n"
"v_mfma_f32_16x16x32_bf16 v[12:15], v[164:167], a[92:95], v[12:15]\n"
"ds_read_b64_tr_b16 v[162:163], v224 offset:2048\n"
"s_waitcnt lgkmcnt(10)\n"
"v_mfma_f32_16x16x32_bf16 v[16:19], v[168:171], a[64:67], v[16:19]\n"
"buffer_load_dwordx4 a[124:127], v119, s[40:43], s71 offen offset:1024 sc1\n"
"ds_read_b64_tr_b16 v[164:165], v224 offset:4096\n"
"v_mfma_f32_16x16x32_bf16 v[16:19], v[172:175], a[68:71], v[16:19]\n"
"ds_read_b64_tr_b16 v[166:167], v224 offset:6144\n"
"v_mfma_f32_16x16x32_bf16 v[20:23], v[168:171], a[72:75], v[20:23]\n"
"v_mfma_f32_16x16x32_bf16 v[20:23], v[172:175], a[76:79], v[20:23]\n"
"v_mfma_f32_16x16x32_bf16 v[24:27], v[168:171], a[80:83], v[24:27]\n"
"v_mfma_f32_16x16x32_bf16 v[24:27], v[172:175], a[84:87], v[24:27]\n"
"v_mfma_f32_16x16x32_bf16 v[28:31], v[168:171], a[88:91], v[28:31]\n"
"ds_read_b64_tr_b16 v[168:169], v225 offset:0\n"
"v_mfma_f32_16x16x32_bf16 v[28:31], v[172:175], a[92:95], v[28:31]\n"
"ds_read_b64_tr_b16 v[170:171], v225 offset:2048\n"
"s_waitcnt lgkmcnt(10)\n"
"v_mfma_f32_16x16x32_bf16 v[32:35], v[176:179], a[64:67], v[32:35]\n"
"buffer_load_dwordx2 v[222:223], v236, s[40:43], s35 offen sc1\n"
"ds_read_b64_tr_b16 v[172:173], v225 offset:4096\n"
"v_mfma_f32_16x16x32_bf16 v[32:35], v[180:183], a[68:71], v[32:35]\n"
"ds_read_b64_tr_b16 v[174:175], v225 offset:6144\n"
"s_add_u32 m0, s58, 24576\n"
"v_mfma_f32_16x16x32_bf16 v[36:39], v[176:179], a[72:75], v[36:39]\n"
"s_add_u32 s60, s79, 32768\n"
"buffer_load_dwordx4 v228, s[28:31], s60 offen offset:0 lds\n"
"v_mfma_f32_16x16x32_bf16 v[36:39], v[180:183], a[76:79], v[36:39]\n"
"v_mfma_f32_16x16x32_bf16 v[40:43], v[176:179], a[80:83], v[40:43]\n"
"buffer_load_dwordx4 v229, s[28:31], s60 offen offset:1024 lds\n"
"v_mfma_f32_16x16x32_bf16 v[40:43], v[180:183], a[84:87], v[40:43]\n"
"v_mfma_f32_16x16x32_bf16 v[44:47], v[176:179], a[88:91], v[44:47]\n"
"ds_read_b64_tr_b16 v[176:177], v226 offset:0\n"
"v_mfma_f32_16x16x32_bf16 v[44:47], v[180:183], a[92:95], v[44:47]\n"
"ds_read_b64_tr_b16 v[178:179], v226 offset:2048\n"
"s_waitcnt lgkmcnt(10)\n"
"v_mfma_f32_16x16x32_bf16 v[48:51], v[184:187], a[64:67], v[48:51]\n"
"ds_read_b64_tr_b16 v[180:181], v226 offset:4096\n"
"s_add_u32 s60, s79, 65536\n"
"v_mfma_f32_16x16x32_bf16 v[48:51], v[188:191], a[68:71], v[48:51]\n"
"ds_read_b64_tr_b16 v[182:183], v226 offset:6144\n"
"buffer_load_dwordx4 v230, s[28:31], s60 offen offset:2048 lds\n"
"v_mfma_f32_16x16x32_bf16 v[52:55], v[184:187], a[72:75], v[52:55]\n"
"buffer_load_dwordx4 v231, s[28:31], s60 offen offset:3072 lds\n"
"v_mfma_f32_16x16x32_bf16 v[52:55], v[188:191], a[76:79], v[52:55]\n"
"v_mfma_f32_16x16x32_bf16 v[56:59], v[184:187], a[80:83], v[56:59]\n"
"v_mfma_f32_16x16x32_bf16 v[56:59], v[188:191], a[84:87], v[56:59]\n"
"s_add_u32 m0, s58, 28672\n"
"s_add_u32 s60, s79, 131072\n"
"v_mfma_f32_16x16x32_bf16 v[60:63], v[184:187], a[88:91], v[60:63]\n"
"ds_read_b64_tr_b16 v[184:185], v227 offset:0\n"
"buffer_load_dwordx4 v228, s[28:31], s60 offen offset:0 lds\n"
"v_mfma_f32_16x16x32_bf16 v[60:63], v[188:191], a[92:95], v[60:63]\n"
"ds_read_b64_tr_b16 v[186:187], v227 offset:2048\n"
"s_waitcnt vmcnt(10) lgkmcnt(10)\n"
"v_mfma_f32_16x16x32_bf16 v[0:3], v[160:163], a[96:99], v[0:3]\n"
"buffer_load_dwordx4 v[200:203], v232, s[40:43], s74 offen offset:0 sc1\n"
"ds_read_b64_tr_b16 v[188:189], v227 offset:4096\n"
"v_mfma_f32_16x16x32_bf16 v[0:3], v[164:167], a[100:103], v[0:3]\n"
"ds_read_b64_tr_b16 v[190:191], v227 offset:6144\n"
"buffer_load_dwordx4 v229, s[28:31], s60 offen offset:1024 lds\n"
"v_mfma_f32_16x16x32_bf16 v[4:7], v[160:163], a[104:107], v[4:7]\n"
"buffer_load_dwordx4 v[204:207], v232, s[40:43], s74 offen offset:1024 sc1\n"
"v_mfma_f32_16x16x32_bf16 v[4:7], v[164:167], a[108:111], v[4:7]\n"
"s_add_u32 s60, s79, 163840\n"
"s_waitcnt vmcnt(9)\n"
"v_mfma_f32_16x16x32_bf16 v[8:11], v[160:163], a[112:115], v[8:11]\n"
"buffer_load_dwordx4 v[128:131], v125, s[40:43], s59 offen offset:0 sc1\n"
"buffer_load_dwordx4 v230, s[28:31], s60 offen offset:2048 lds\n"
"v_mfma_f32_16x16x32_bf16 v[8:11], v[164:167], a[116:119], v[8:11]\n"
"v_mfma_f32_16x16x32_bf16 v[12:15], v[160:163], a[120:123], v[12:15]\n"
"buffer_load_dwordx4 v[132:135], v125, s[40:43], s59 offen offset:64 sc1\n"
"ds_read_b64_tr_b16 v[160:161], v224 offset:8192\n"
"v_mfma_f32_16x16x32_bf16 v[12:15], v[164:167], a[124:127], v[12:15]\n"
"ds_read_b64_tr_b16 v[162:163], v224 offset:10240\n"
"s_waitcnt lgkmcnt(10)\n"
"v_mfma_f32_16x16x32_bf16 v[16:19], v[168:171], a[96:99], v[16:19]\n"
"buffer_load_dwordx4 v[136:139], v125, s[40:43], s59 offen offset:128 sc1\n"
"ds_read_b64_tr_b16 v[164:165], v224 offset:12288\n"
"v_mfma_f32_16x16x32_bf16 v[16:19], v[172:175], a[100:103], v[16:19]\n"
"ds_read_b64_tr_b16 v[166:167], v224 offset:14336\n"
"buffer_load_dwordx4 v231, s[28:31], s60 offen offset:3072 lds\n"
"v_mfma_f32_16x16x32_bf16 v[20:23], v[168:171], a[104:107], v[20:23]\n"
"buffer_load_dwordx4 v[140:143], v125, s[40:43], s59 offen offset:192 sc1\n"
"v_mfma_f32_16x16x32_bf16 v[20:23], v[172:175], a[108:111], v[20:23]\n"
"v_mfma_f32_16x16x32_bf16 v[24:27], v[168:171], a[112:115], v[24:27]\n"
"v_mfma_f32_16x16x32_bf16 v[24:27], v[172:175], a[116:119], v[24:27]\n"
"s_waitcnt vmcnt(14)\n"
"v_cmp_ne_u64 vcc, s[6:7], v[222:223]\n"
"v_mfma_f32_16x16x32_bf16 v[28:31], v[168:171], a[120:123], v[28:31]\n"
"s_cbranch_vccnz Lpb_%=\n"
"Lpb_ok_%=:\n"
"buffer_load_dwordx4 a[192:195], v116, s[40:43], s68 offen offset:2048 sc1\n"
"v_mfma_f32_16x16x32_bf16 v[28:31], v[172:175], a[124:127], v[28:31]\n"
"buffer_load_dwordx4 a[196:199], v116, s[40:43], s69 offen offset:2048 sc1\n"
"s_waitcnt lgkmcnt(8)\n"
"v_mfma_f32_16x16x32_bf16 v[32:35], v[176:179], a[96:99], v[32:35]\n"
"buffer_load_dwordx4 a[200:203], v117, s[40:43], s68 offen offset:2048 sc1\n"
"ds_read_b64_tr_b16 v[168:169], v225 offset:8192\n"
"v_mfma_f32_16x16x32_bf16 v[32:35], v[180:183], a[100:103], v[32:35]\n"
"buffer_load_dwordx4 a[204:207], v117, s[40:43], s69 offen offset:2048 sc1\n"
"ds_read_b64_tr_b16 v[170:171], v225 offset:10240\n"
"v_mfma_f32_16x16x32_bf16 v[36:39], v[176:179], a[104:107], v[36:39]\n"
"buffer_load_dwordx4 a[208:211], v118, s[40:43], s68 offen offset:2048 sc1\n"
"ds_read_b64_tr_b16 v[172:173], v225 offset:12288\n"
"v_mfma_f32_16x16x32_bf16 v[36:39], v[180:183], a[108:111], v[36:39]\n"
"buffer_load_dwordx4 a[212:215], v118, s[40:43], s69 offen offset:2048 sc1\n"
"ds_read_b64_tr_b16 v[174:175], v225 offset:14336\n"
"v_mfma_f32_16x16x32_bf16 v[40:43], v[176:179], a[112:115], v[40:43]\n"
"buffer_load_dwordx4 a[216:219], v119, s[40:43], s68 offen offset:2048 sc1\n"
"v_mfma_f32_16x16x32_bf16 v[40:43], v[180:183], a[116:119], v[40:43]\n"
"buffer_load_dwordx4 a[220:223], v119, s[40:43], s69 offen offset:2048 sc1\n"
"v_mfma_f32_16x16x32_bf16 v[44:47], v[176:179], a[120:123], v[44:47]\n"
"buffer_load_dwordx4 a[224:227], v116, s[40:43], s70 offen offset:2048 sc1\n"
"ds_read_b64_tr_b16 v[176:177], v226 offset:8192\n"
"v_mfma_f32_16x16x32_bf16 v[44:47], v[180:183], a[124:127], v[44:47]\n"
"buffer_load_dwordx4 a[228:231], v116, s[40:43], s71 offen offset:2048 sc1\n"
"s_waitcnt lgkmcnt(9)\n"
"v_mfma_f32_16x16x32_bf16 v[48:51], v[184:187], a[96:99], v[48:51]\n"
"buffer_load_dwordx4 a[232:235], v117, s[40:43], s70 offen offset:2048 sc1\n"
"ds_read_b64_tr_b16 v[178:179], v226 offset:10240\n"
"v_mfma_f32_16x16x32_bf16 v[48:51], v[188:191], a[100:103], v[48:51]\n"
"buffer_load_dwordx4 a[236:239], v117, s[40:43], s71 offen offset:2048 sc1\n"
"ds_read_b64_tr_b16 v[180:181], v226 offset:12288\n"
"v_mfma_f32_16x16x32_bf16 v[52:55], v[184:187], a[104:107], v[52:55]\n"
"ds_read_b64_tr_b16 v[182:183], v226 offset:14336\n"
"v_mfma_f32_16x16x32_bf16 v[52:55], v[188:191], a[108:111], v[52:55]\n"
"v_mfma_f32_16x16x32_bf16 v[56:59], v[184:187], a[112:115], v[56:59]\n"
"buffer_load_dwordx4 a[240:243], v118, s[40:43], s70 offen offset:2048 sc1\n"
"v_mfma_f32_16x16x32_bf16 v[56:59], v[188:191], a[116:119], v[56:59]\n"
"v_mfma_f32_16x16x32_bf16 v[60:63], v[184:187], a[120:123], v[60:63]\n"
"ds_read_b64_tr_b16 v[184:185], v227 offset:8192\n"
"v_mfma_f32_16x16x32_bf16 v[60:63], v[188:191], a[124:127], v[60:63]\n"
"ds_read_b64_tr_b16 v[186:187], v227 offset:10240\n"
"s_waitcnt vmcnt(9) lgkmcnt(10)\n"
"v_mfma_f32_16x16x32_bf16 v[0:3], v[160:163], a[192:195], v[0:3]\n"
"buffer_load_dwordx4 a[244:247], v118, s[40:43], s71 offen offset:2048 sc1\n"
"ds_read_b64_tr_b16 v[188:189], v227 offset:12288\n"
"v_mfma_f32_16x16x32_bf16 v[0:3], v[164:167], a[196:199], v[0:3]\n"
"ds_read_b64_tr_b16 v[190:191], v227 offset:14336\n"
"v_mfma_f32_16x16x32_bf16 v[4:7], v[160:163], a[200:203], v[4:7]\n"
"v_mfma_f32_16x16x32_bf16 v[4:7], v[164:167], a[204:207], v[4:7]\n"
"s_waitcnt vmcnt(6)\n"
"v_mfma_f32_16x16x32_bf16 v[8:11], v[160:163], a[208:211], v[8:11]\n"
"buffer_load_dwordx4 a[248:251], v119, s[40:43], s70 offen offset:2048 sc1\n"
"v_mfma_f32_16x16x32_bf16 v[8:11], v[164:167], a[212:215], v[8:11]\n"
"v_mfma_f32_16x16x32_bf16 v[12:15], v[160:163], a[216:219], v[12:15]\n"
"ds_read_b64_tr_b16 v[160:161], v224 offset:16384\n"
"v_mfma_f32_16x16x32_bf16 v[12:15], v[164:167], a[220:223], v[12:15]\n"
"ds_read_b64_tr_b16 v[162:163], v224 offset:18432\n"
"s_waitcnt lgkmcnt(10)\n"
"v_mfma_f32_16x16x32_bf16 v[16:19], v[168:171], a[192:195], v[16:19]\n"
"buffer_load_dwordx4 a[252:255], v119, s[40:43], s71 offen offset:2048 sc1\n"
"ds_read_b64_tr_b16 v[164:165], v224 offset:20480\n"
"v_mfma_f32_16x16x32_bf16 v[16:19], v[172:175], a[196:199], v[16:19]\n"
"ds_read_b64_tr_b16 v[166:167], v224 offset:22528\n"
"v_mfma_f32_16x16x32_bf16 v[20:23], v[168:171], a[200:203], v[20:23]\n"
"v_mfma_f32_16x16x32_bf16 v[20:23], v[172:175], a[204:207], v[20:23]\n"
"v_mfma_f32_16x16x32_bf16 v[24:27], v[168:171], a[208:211], v[24:27]\n"
"buffer_load_dwordx2 a[0:1], v120, s[40:43], s68 offen offset:3072 sc1\n"
"buffer_load_dwordx2 a[2:3], v120, s[40:43], s69 offen offset:3072 sc1\n"
"v_mfma_f32_16x16x32_bf16 v[24:27], v[172:175], a[212:215], v[24:27]\n"
"v_mfma_f32_16x16x32_bf16 v[28:31], v[168:171], a[216:219], v[28:31]\n"
"ds_read_b64_tr_b16 v[168:169], v225 offset:16384\n"
"v_mfma_f32_16x16x32_bf16 v[28:31], v[172:175], a[220:223], v[28:31]\n"
"ds_read_b64_tr_b16 v[170:171], v225 offset:18432\n"
"s_waitcnt lgkmcnt(10)\n"
"v_mfma_f32_16x16x32_bf16 v[32:35], v[176:179], a[192:195], v[32:35]\n"
"buffer_load_dwordx2 a[4:5], v120, s[40:43], s70 offen offset:3072 sc1\n"
"buffer_load_dwordx2 a[6:7], v120, s[40:43], s71 offen offset:3072 sc1\n"
"v_mfma_f32_16x16x32_bf16 v[32:35], v[180:183], a[196:199], v[32:35]\n"
"ds_read_b64_tr_b16 v[172:173], v225 offset:20480\n"
"v_mfma_f32_16x16x32_bf16 v[36:39], v[176:179], a[200:203], v[36:39]\n"
"ds_read_b64_tr_b16 v[174:175], v225 offset:22528\n"
"v_mfma_f32_16x16x32_bf16 v[36:39], v[180:183], a[204:207], v[36:39]\n"
"v_mfma_f32_16x16x32_bf16 v[40:43], v[176:179], a[208:211], v[40:43]\n"
"buffer_load_dwordx2 a[8:9], v121, s[40:43], s68 offen offset:3072 sc1\n"
"buffer_load_dwordx2 a[10:11], v121, s[40:43], s69 offen offset:3072 sc1\n"
"v_mfma_f32_16x16x32_bf16 v[40:43], v[180:183], a[212:215], v[40:43]\n"
"v_mfma_f32_16x16x32_bf16 v[44:47], v[176:179], a[216:219], v[44:47]\n"
"ds_read_b64_tr_b16 v[176:177], v226 offset:16384\n"
"v_mfma_f32_16x16x32_bf16 v[44:47], v[180:183], a[220:223], v[44:47]\n"
"ds_read_b64_tr_b16 v[178:179], v226 offset:18432\n"
"s_waitcnt lgkmcnt(10)\n"
"v_mfma_f32_16x16x32_bf16 v[48:51], v[184:187], a[192:195], v[48:51]\n"
"buffer_load_dwordx2 a[12:13], v121, s[40:43], s70 offen offset:3072 sc1\n"
"buffer_load_dwordx2 a[14:15], v121, s[40:43], s71 offen offset:3072 sc1\n"
"v_mfma_f32_16x16x32_bf16 v[48:51], v[188:191], a[196:199], v[48:51]\n"
"ds_read_b64_tr_b16 v[180:181], v226 offset:20480\n"
"v_mfma_f32_16x16x32_bf16 v[52:55], v[184:187], a[200:203], v[52:55]\n"
"ds_read_b64_tr_b16 v[182:183], v226 offset:22528\n"
"v_mfma_f32_16x16x32_bf16 v[52:55], v[188:191], a[204:207], v[52:55]\n"
"v_mfma_f32_16x16x32_bf16 v[56:59], v[184:187], a[208:211], v[56:59]\n"
"buffer_load_dwordx2 a[16:17], v122, s[40:43], s68 offen offset:3072 sc1\n"
"buffer_load_dwordx2 a[18:19], v122, s[40:43], s69 offen offset:3072 sc1\n"
"v_mfma_f32_16x16x32_bf16 v[56:59], v[188:191], a[212:215], v[56:59]\n"
"v_lshlrev_b32 v240, 16, v200\n"
"v_and_b32 v241, 0xffff0000, v200\n"
"v_mfma_f32_16x16x32_bf16 v[60:63], v[184:187], a[216:219], v[60:63]\n"
"ds_read_b64_tr_b16 v[184:185], v227 offset:16384\n"
"v_lshlrev_b32 v242, 16, v201\n"
"v_mfma_f32_16x16x32_bf16 v[60:63], v[188:191], a[220:223], v[60:63]\n"
"ds_read_b64_tr_b16 v[186:187], v227 offset:18432\n"
"s_waitcnt vmcnt(14) lgkmcnt(10)\n"
"v_mfma_f32_16x16x32_bf16 v[0:3], v[160:163], a[224:227], v[0:3]\n"
"buffer_load_dwordx2 a[20:21], v122, s[40:43], s70 offen offset:3072 sc1\n"
"buffer_load_dwordx2 a[22:23], v122, s[40:43], s71 offen offset:3072 sc1\n"
"v_mfma_f32_16x16x32_bf16 v[0:3], v[164:167], a[228:231], v[0:3]\n"
"ds_read_b64_tr_b16 v[188:189], v227 offset:20480\n"
"v_and_b32 v243, 0xffff0000, v201\n"
"v_mfma_f32_16x16x32_bf16 v[4:7], v[160:163], a[232:235], v[4:7]\n"
"ds_read_b64_tr_b16 v[190:191], v227 offset:22528\n"
"v_lshlrev_b32 v244, 16, v202\n"
"v_mfma_f32_16x16x32_bf16 v[4:7], v[164:167], a[236:239], v[4:7]\n"
"v_and_b32 v245, 0xffff0000, v202\n"
"s_waitcnt vmcnt(12)\n"
"v_mfma_f32_16x16x32_bf16 v[8:11], v[160:163], a[240:243], v[8:11]\n"
"buffer_load_dwordx2 a[24:25], v123, s[40:43], s68 offen offset:3072 sc1\n"
"buffer_load_dwordx2 a[26:27], v123, s[40:43], s69 offen offset:3072 sc1\n"
"v_mfma_f32_16x16x32_bf16 v[8:11], v[164:167], a[244:247], v[8:11]\n"
"v_lshlrev_b32 v246, 16, v203\n"
"v_and_b32 v247, 0xffff0000, v203\n"
"v_mfma_f32_16x16x32_bf16 v[12:15], v[160:163], a[248:251], v[12:15]\n"
"ds_read_b64_tr_b16 v[160:161], v224 offset:24576\n"
"v_lshlrev_b32 v248, 16, v204\n"
"v_mfma_f32_16x16x32_bf16 v[12:15], v[164:167], a[252:255], v[12:15]\n"
"ds_read_b64_tr_b16 v[162:163], v224 offset:26624\n"
"s_waitcnt lgkmcnt(10)\n"
"v_mfma_f32_16x16x32_bf16 v[16:19], v[168:171], a[224:227], v[16:19]\n"
"buffer_load_dwordx2 a[28:29], v123, s[40:43], s70 offen offset:3072 sc1\n"
"buffer_load_dwordx2 a[30:31], v123, s[40:43], s71 offen offset:3072 sc1\n"
"v_mfma_f32_16x16x32_bf16 v[16:19], v[172:175], a[228:231], v[16:19]\n"
"ds_read_b64_tr_b16 v[164:165], v224 offset:28672\n"
"v_and_b32 v249, 0xffff0000, v204\n"
"v_mfma_f32_16x16x32_bf16 v[20:23], v[168:171], a[232:235], v[20:23]\n"
"ds_read_b64_tr_b16 v[166:167], v224 offset:30720\n"
"v_lshlrev_b32 v250, 16, v205\n"
"v_mfma_f32_16x16x32_bf16 v[20:23], v[172:175], a[236:239], v[20:23]\n"
"v_and_b32 v251, 0xffff0000, v205\n"
"v_lshlrev_b32 v252, 16, v206\n"
"v_mfma_f32_16x16x32_bf16 v[24:27], v[168:171], a[240:243], v[24:27]\n"
"v_and_b32 v253, 0xffff0000, v206\n"
"v_lshlrev_b32 v254, 16, v207\n"
"v_mfma_f32_16x16x32_bf16 v[24:27], v[172:175], a[244:247], v[24:27]\n"
"v_and_b32 v255, 0xffff0000, v207\n"
"v_mfma_f32_16x16x32_bf16 v[28:31], v[168:171], a[248:251], v[28:31]\n"
"ds_read_b64_tr_b16 v[168:169], v225 offset:24576\n"
"v_mfma_f32_16x16x32_bf16 v[28:31], v[172:175], a[252:255], v[28:31]\n"
"ds_read_b64_tr_b16 v[170:171], v225 offset:26624\n"
"s_waitcnt lgkmcnt(10)\n"
"v_mfma_f32_16x16x32_bf16 v[32:35], v[176:179], a[224:227], v[32:35]\n"
"ds_read_b64_tr_b16 v[172:173], v225 offset:28672\n"
"v_mfma_f32_16x16x32_bf16 v[32:35], v[180:183], a[228:231], v[32:35]\n"
"ds_read_b64_tr_b16 v[174:175], v225 offset:30720\n"
"v_mfma_f32_16x16x32_bf16 v[36:39], v[176:179], a[232:235], v[36:39]\n"
"v_mfma_f32_16x16x32_bf16 v[36:39], v[180:183], a[236:239], v[36:39]\n"
"v_mfma_f32_16x16x32_bf16 v[40:43], v[176:179], a[240:243], v[40:43]\n"
"v_mfma_f32_16x16x32_bf16 v[40:43], v[180:183], a[244:247], v[40:43]\n"
"v_mfma_f32_16x16x32_bf16 v[44:47], v[176:179], a[248:251], v[44:47]\n"
"ds_read_b64_tr_b16 v[176:177], v226 offset:24576\n"
"v_mfma_f32_16x16x32_bf16 v[44:47], v[180:183], a[252:255], v[44:47]\n"
"ds_read_b64_tr_b16 v[178:179], v226 offset:26624\n"
"s_waitcnt lgkmcnt(10)\n"
"v_mfma_f32_16x16x32_bf16 v[48:51], v[184:187], a[224:227], v[48:51]\n"
"ds_read_b64_tr_b16 v[180:181], v226 offset:28672\n"
"v_mfma_f32_16x16x32_bf16 v[48:51], v[188:191], a[228:231], v[48:51]\n"
"ds_read_b64_tr_b16 v[182:183], v226 offset:30720\n"
"v_mfma_f32_16x16x32_bf16 v[52:55], v[184:187], a[232:235], v[52:55]\n"
"v_mfma_f32_16x16x32_bf16 v[52:55], v[188:191], a[236:239], v[52:55]\n"
"v_mfma_f32_16x16x32_bf16 v[56:59], v[184:187], a[240:243], v[56:59]\n"
"v_mfma_f32_16x16x32_bf16 v[56:59], v[188:191], a[244:247], v[56:59]\n"
"v_mfma_f32_16x16x32_bf16 v[60:63], v[184:187], a[248:251], v[60:63]\n"
"ds_read_b64_tr_b16 v[184:185], v227 offset:24576\n"
"v_mfma_f32_16x16x32_bf16 v[60:63], v[188:191], a[252:255], v[60:63]\n"
"ds_read_b64_tr_b16 v[186:187], v227 offset:26624\n"
"s_waitcnt vmcnt(8) lgkmcnt(10)\n"
"v_mfma_f32_16x16x32_bf16 v[0:3], v[160:163], a[0:3], v[0:3]\n"
"ds_read_b64_tr_b16 v[188:189], v227 offset:28672\n"
"v_mfma_f32_16x16x32_bf16 v[0:3], v[164:167], a[4:7], v[0:3]\n"
"ds_read_b64_tr_b16 v[190:191], v227 offset:30720\n"
"v_mfma_f32_16x16x32_bf16 v[4:7], v[160:163], a[8:11], v[4:7]\n"
"v_mfma_f32_16x16x32_bf16 v[4:7], v[164:167], a[12:15], v[4:7]\n"
"s_waitcnt vmcnt(0)\n"
"v_mfma_f32_16x16x32_bf16 v[8:11], v[160:163], a[16:19], v[8:11]\n"
"v_mfma_f32_16x16x32_bf16 v[8:11], v[164:167], a[20:23], v[8:11]\n"
"v_mfma_f32_16x16x32_bf16 v[12:15], v[160:163], a[24:27], v[12:15]\n"
"v_mfma_f32_16x16x32_bf16 v[12:15], v[164:167], a[28:31], v[12:15]\n"
"s_waitcnt lgkmcnt(8)\n"
"v_mfma_f32_16x16x32_bf16 v[16:19], v[168:171], a[0:3], v[16:19]\n"
"v_mfma_f32_16x16x32_bf16 v[16:19], v[172:175], a[4:7], v[16:19]\n"
"v_mfma_f32_16x16x32_bf16 v[20:23], v[168:171], a[8:11], v[20:23]\n"
"v_mfma_f32_16x16x32_bf16 v[20:23], v[172:175], a[12:15], v[20:23]\n"
"ds_write_b128 v233, v[4:7] offset:0\n"
"v_mfma_f32_16x16x32_bf16 v[24:27], v[168:171], a[16:19], v[24:27]\n"
"ds_write_b128 v233, v[8:11] offset:4096\n"
"v_mfma_f32_16x16x32_bf16 v[24:27], v[172:175], a[20:23], v[24:27]\n"
"ds_write_b128 v233, v[12:15] offset:8192\n"
"v_mfma_f32_16x16x32_bf16 v[28:31], v[168:171], a[24:27], v[28:31]\n"
"v_mfma_f32_16x16x32_bf16 v[28:31], v[172:175], a[28:31], v[28:31]\n"
"s_waitcnt lgkmcnt(7)\n"
"v_mfma_f32_16x16x32_bf16 v[32:35], v[176:179], a[0:3], v[32:35]\n"
"ds_write_b128 v233, v[20:23] offset:1024\n"
"v_mfma_f32_16x16x32_bf16 v[32:35], v[180:183], a[4:7], v[32:35]\n"
"v_mfma_f32_16x16x32_bf16 v[36:39], v[176:179], a[8:11], v[36:39]\n"
"v_mfma_f32_16x16x32_bf16 v[36:39], v[180:183], a[12:15], v[36:39]\n"
"ds_write_b128 v233, v[24:27] offset:5120\n"
"v_mfma_f32_16x16x32_bf16 v[40:43], v[176:179], a[16:19], v[40:43]\n"
"v_mfma_f32_16x16x32_bf16 v[40:43], v[180:183], a[20:23], v[40:43]\n"
"ds_write_b128 v233, v[28:31] offset:9216\n"
"v_mfma_f32_16x16x32_bf16 v[44:47], v[176:179], a[24:27], v[44:47]\n"
"v_mfma_f32_16x16x32_bf16 v[44:47], v[180:183], a[28:31], v[44:47]\n"
"s_waitcnt lgkmcnt(6)\n"
"v_mfma_f32_16x16x32_bf16 v[48:51], v[184:187], a[0:3], v[48:51]\n"
"v_mfma_f32_16x16x32_bf16 v[48:51], v[188:191], a[4:7], v[48:51]\n"
"ds_write_b128 v233, v[36:39] offset:2048\n"
"v_mfma_f32_16x16x32_bf16 v[52:55], v[184:187], a[8:11], v[52:55]\n"
"v_mfma_f32_16x16x32_bf16 v[52:55], v[188:191], a[12:15], v[52:55]\n"
"ds_write_b128 v233, v[40:43] offset:6144\n"
"v_mfma_f32_16x16x32_bf16 v[56:59], v[184:187], a[16:19], v[56:59]\n"
"v_mfma_f32_16x16x32_bf16 v[56:59], v[188:191], a[20:23], v[56:59]\n"
"ds_write_b128 v233, v[44:47] offset:10240\n"
"v_mfma_f32_16x16x32_bf16 v[60:63], v[184:187], a[24:27], v[60:63]\n"
"v_mfma_f32_16x16x32_bf16 v[60:63], v[188:191], a[28:31], v[60:63]\n"
"s_nop 1\n"
"ds_write_b128 v233, v[52:55] offset:3072\n"
"s_nop 1\n"
"ds_write_b128 v233, v[56:59] offset:7168\n"
"s_nop 1\n"
"ds_write_b128 v233, v[60:63] offset:11264\n"
"s_waitcnt lgkmcnt(0)\n"
"s_barrier\n"
"ds_read_b128 v[4:7], v65 offset:0\n"
"ds_read_b128 v[8:11], v66 offset:4096\n"
"ds_read_b128 v[12:15], v67 offset:8192\n"
"ds_read_b128 v[20:23], v65 offset:1024\n"
"ds_read_b128 v[24:27], v66 offset:5120\n"
"ds_read_b128 v[28:31], v67 offset:9216\n"
"ds_read_b128 v[36:39], v65 offset:2048\n"
"ds_read_b128 v[40:43], v66 offset:6144\n"
"ds_read_b128 v[44:47], v67 offset:10240\n"
"ds_read_b128 v[52:55], v65 offset:3072\n"
"ds_read_b128 v[56:59], v66 offset:7168\n"
"ds_read_b128 v[60:63], v67 offset:11264\n"
"s_waitcnt lgkmcnt(11)\n"
"v_pk_add_f32 v[0:1], v[0:1], v[4:5]\n"
"v_pk_add_f32 v[2:3], v[2:3], v[6:7]\n"
"s_waitcnt lgkmcnt(9)\n"
"v_pk_add_f32 v[8:9], v[8:9], v[12:13]\n"
"v_pk_add_f32 v[10:11], v[10:11], v[14:15]\n"
"v_pk_add_f32 v[0:1], v[0:1], v[8:9]\n"
"v_pk_add_f32 v[2:3], v[2:3], v[10:11]\n"
"v_pk_fma_f32 v[0:1], v[128:129], v[0:1], v[240:241]\n"
"v_pk_fma_f32 v[2:3], v[130:131], v[2:3], v[242:243]\n"
"v_cvt_pk_bf16_f32 v4, v0, v1\n"
"v_cvt_pk_bf16_f32 v5, v2, v3\n"
"s_waitcnt lgkmcnt(8)\n"
"v_pk_add_f32 v[16:17], v[16:17], v[20:21]\n"
"v_pk_add_f32 v[18:19], v[18:19], v[22:23]\n"
"s_waitcnt lgkmcnt(6)\n"
"v_pk_add_f32 v[24:25], v[24:25], v[28:29]\n"
"v_pk_add_f32 v[26:27], v[26:27], v[30:31]\n"
"v_pk_add_f32 v[16:17], v[16:17], v[24:25]\n"
"v_pk_add_f32 v[18:19], v[18:19], v[26:27]\n"
"v_pk_fma_f32 v[16:17], v[132:133], v[16:17], v[244:245]\n"
"v_pk_fma_f32 v[18:19], v[134:135], v[18:19], v[246:247]\n"
"v_cvt_pk_bf16_f32 v6, v16, v17\n"
"v_cvt_pk_bf16_f32 v7, v18, v19\n"
"s_nop 0\n"
"v_permlane16_swap_b32 v4, v6\n"
"v_permlane16_swap_b32 v5, v7\n"
"buffer_store_dwordx4 v[4:7], v124, s[36:39], 0 offen offset:0\n"
"s_waitcnt lgkmcnt(5)\n"
"v_pk_add_f32 v[32:33], v[32:33], v[36:37]\n"
"v_pk_add_f32 v[34:35], v[34:35], v[38:39]\n"
"s_waitcnt lgkmcnt(3)\n"
"v_pk_add_f32 v[40:41], v[40:41], v[44:45]\n"
"v_pk_add_f32 v[42:43], v[42:43], v[46:47]\n"
"v_pk_add_f32 v[32:33], v[32:33], v[40:41]\n"
"v_pk_add_f32 v[34:35], v[34:35], v[42:43]\n"
"v_pk_fma_f32 v[32:33], v[136:137], v[32:33], v[248:249]\n"
"v_pk_fma_f32 v[34:35], v[138:139], v[34:35], v[250:251]\n"
"v_cvt_pk_bf16_f32 v36, v32, v33\n"
"v_cvt_pk_bf16_f32 v37, v34, v35\n"
"s_waitcnt lgkmcnt(2)\n"
"v_pk_add_f32 v[48:49], v[48:49], v[52:53]\n"
"v_pk_add_f32 v[50:51], v[50:51], v[54:55]\n"
"s_waitcnt lgkmcnt(0)\n"
"v_pk_add_f32 v[56:57], v[56:57], v[60:61]\n"
"v_pk_add_f32 v[58:59], v[58:59], v[62:63]\n"
"v_pk_add_f32 v[48:49], v[48:49], v[56:57]\n"
"v_pk_add_f32 v[50:51], v[50:51], v[58:59]\n"
"v_pk_fma_f32 v[48:49], v[140:141], v[48:49], v[252:253]\n"
"v_pk_fma_f32 v[50:51], v[142:143], v[50:51], v[254:255]\n"
"v_cvt_pk_bf16_f32 v38, v48, v49\n"
"v_cvt_pk_bf16_f32 v39, v50, v51\n"
"s_nop 0\n"
"v_permlane16_swap_b32 v36, v38\n"
"v_permlane16_swap_b32 v37, v39\n"
"buffer_store_dwordx4 v[36:39], v124, s[36:39], 0 offen offset:64\n"
"v_mov_b32 v250, s6\n"
"v_mov_b32 v251, s7\n"
"s_or_b32 s16, s10, s11\n"
"s_min_u32 s16, s16, 1\n"
"s_lshl_b32 s16, s16, 27\n"
"buffer_store_dwordx2 v[250:251], v237, s[40:43], s16 offen sc1\n"
"s_endpgm\n"
"Lpm_%=:\n"
"buffer_load_dwordx2 v[250:251], v134, s[40:43], s49 offen sc1\n"
"s_add_u32 s19, s19, 16\n"
"s_waitcnt vmcnt(0)\n"
"v_cmp_ne_u64 vcc, s[6:7], v[250:251]\n"
"s_nop 0\n"
"s_cbranch_vccz Lpm_ok_%=\n"
"s_cmp_lt_u32 s19, 0x400000\n"
"s_cbranch_scc1 Lpm_%=\n"
"s_branch Lpm_ok_%=\n"
"Lph_%=:\n"
"buffer_load_dwordx2 v[250:251], v239, s[40:43], s51 offen sc1\n"
"s_add_u32 s19, s19, 16\n"
"s_waitcnt vmcnt(0)\n"
"v_cmp_ne_u64 vcc, s[6:7], v[250:251]\n"
"s_nop 0\n"
"s_cbranch_vccz Lph_ok_%=\n"
"s_cmp_lt_u32 s19, 0x400000\n"
"s_cbranch_scc1 Lph_%=\n"
"s_branch Lph_ok_%=\n"
"Lpk_%=:\n"
"buffer_load_dwordx2 v[250:251], v238, s[40:43], s51 offen sc1\n"
"s_add_u32 s19, s19, 16\n"
"s_waitcnt vmcnt(0)\n"
"v_cmp_ne_u64 vcc, s[6:7], v[250:251]\n"
"s_nop 0\n"
"s_cbranch_vccz Lpk_ok_%=\n"
"s_cmp_lt_u32 s19, 0x400000\n"
"s_cbranch_scc1 Lpk_%=\n"
"s_branch Lpk_ok_%=\n"
"Lpc_%=:\n"
"v_mov_b64 v[222:223], v[220:221]\n"
"s_mov_b64 exec, 0xf\n"
"v_mov_b64 v[222:223], 0\n"
"buffer_atomic_add_x2 v[222:223], v236, s[40:43], s35 offen sc0 sc1\n"
"s_mov_b64 exec, -1\n"
"s_add_u32 s19, s19, 16\n"
"s_waitcnt vmcnt(0)\n"
"v_cmp_ne_u64 vcc, s[6:7], v[222:223]\n"
"s_nop 0\n"
"s_cbranch_vccz Lpc_ok_%=\n"
"s_cmp_lt_u32 s19, 0x400000\n"
"s_cbranch_scc1 Lpc_%=\n"
"s_branch Lpc_ok_%=\n"
"Lpd_%=:\n"
"v_mov_b64 v[222:223], v[220:221]\n"
"s_mov_b64 exec, 0xf\n"
"v_mov_b64 v[222:223], 0\n"
"buffer_atomic_add_x2 v[222:223], v236, s[40:43], s35 offen sc0 sc1\n"
"s_mov_b64 exec, -1\n"
"s_add_u32 s19, s19, 16\n"
"s_waitcnt vmcnt(0)\n"
"v_cmp_ne_u64 vcc, s[6:7], v[222:223]\n"
"s_nop 0\n"
"s_cbranch_vccz Lpd_ok_%=\n"
"s_cmp_lt_u32 s19, 0x400000\n"
"s_cbranch_scc1 Lpd_%=\n"
"s_branch Lpd_ok_%=\n"
"Lpp_%=:\n"
"v_mov_b64 v[222:223], v[220:221]\n"
"s_mov_b64 exec, 0xf\n"
"v_mov_b64 v[222:223], 0\n"
"buffer_atomic_add_x2 v[222:223], v236, s[40:43], s35 offen sc0 sc1\n"
"s_mov_b64 exec, -1\n"
"s_add_u32 s19, s19, 16\n"
"s_waitcnt vmcnt(0)\n"
"v_cmp_ne_u64 vcc, s[6:7], v[222:223]\n"
"s_nop 0\n"
"s_cbranch_vccz Lpp_ok_%=\n"
"s_cmp_lt_u32 s19, 0x400000\n"
"s_cbranch_scc1 Lpp_%=\n"
"s_branch Lpp_ok_%=\n"
"Lpy_%=:\n"
"v_mov_b64 v[222:223], v[220:221]\n"
"s_mov_b64 exec, 0xffff\n"
"v_mov_b64 v[222:223], 0\n"
"buffer_atomic_add_x2 v[222:223], v236, s[40:43], s35 offen sc0 sc1\n"
"s_mov_b64 exec, -1\n"
"s_add_u32 s19, s19, 16\n"
"s_waitcnt vmcnt(0)\n"
"v_cmp_ne_u64 vcc, s[6:7], v[222:223]\n"
"s_nop 0\n"
"s_cbranch_vccz Lpy_ok_%=\n"
"s_cmp_lt_u32 s19, 0x400000\n"
"s_cbranch_scc1 Lpy_%=\n"
"s_branch Lpy_ok_%=\n"
"Lpa_%=:\n"
"v_mov_b64 v[222:223], v[220:221]\n"
"s_mov_b64 exec, 0x3333\n"
"v_mov_b64 v[222:223], 0\n"
"buffer_atomic_add_x2 v[222:223], v236, s[40:43], s35 offen sc0 sc1\n"
"s_mov_b64 exec, -1\n"
"s_add_u32 s19, s19, 16\n"
"s_waitcnt vmcnt(0)\n"
"v_cmp_ne_u64 vcc, s[6:7], v[222:223]\n"
"s_nop 0\n"
"s_cbranch_vccz Lpa_ok_%=\n"
"s_cmp_lt_u32 s19, 0x400000\n"
"s_cbranch_scc1 Lpa_%=\n"
"s_branch Lpa_ok_%=\n"
"Lpb_%=:\n"
"v_mov_b64 v[222:223], v[220:221]\n"
"s_mov_b64 exec, 0x3333\n"
"v_mov_b64 v[222:223], 0\n"
"buffer_atomic_add_x2 v[222:223], v236, s[40:43], s35 offen sc0 sc1\n"
"s_mov_b64 exec, -1\n"
"s_add_u32 s19, s19, 16\n"
"s_waitcnt vmcnt(0)\n"
"v_cmp_ne_u64 vcc, s[6:7], v[222:223]\n"
"s_nop 0\n"
"s_cbranch_vccz Lpb_ok_%=\n"
"s_cmp_lt_u32 s19, 0x400000\n"
"s_cbranch_scc1 Lpb_%=\n"
"s_branch Lpb_ok_%=\n"
"Lslow_%=:\n"
"s_waitcnt vmcnt(0) lgkmcnt(0)\n"
"s_and_b32 s16, s11, 2\n"
"s_mul_i32 s16, s16, s70\n"
"v_bfe_u32 v200, v232, 4, 4\n"
"v_lshrrev_b32 v201, 8, v232\n"
"v_lshlrev_b32 v201, 2, v201\n"
"v_sub_u32 v200, v200, v201\n"
"v_mov_b32 v204, 0xff800000\n"
"s_xor_b32 s17, s16, 0\n"
"s_lshl_b32 s18, s68, 6\n"
"s_lshl_b32 s15, s17, 4\n"
"s_add_u32 s18, s18, s15\n"
"v_add_u32 v218, s18, v200\n"
"v_mov_b32 v216, v204\n"
"v_mov_b32 v217, 0\n"
"s_xor_b32 s17, s16, 1\n"
"s_lshl_b32 s18, s68, 6\n"
"s_lshl_b32 s15, s17, 4\n"
"s_add_u32 s18, s18, s15\n"
"v_add_u32 v222, s18, v200\n"
"v_mov_b32 v220, v204\n"
"v_mov_b32 v221, 0\n"
"s_xor_b32 s17, s16, 2\n"
"s_lshl_b32 s18, s68, 6\n"
"s_lshl_b32 s15, s17, 4\n"
"s_add_u32 s18, s18, s15\n"
"v_add_u32 v226, s18, v200\n"
"v_mov_b32 v224, v204\n"
"v_mov_b32 v225, 0\n"
"s_xor_b32 s17, s16, 3\n"
"s_lshl_b32 s18, s68, 6\n"
"s_lshl_b32 s15, s17, 4\n"
"s_add_u32 s18, s18, s15\n"
"v_add_u32 v230, s18, v200\n"
"v_mov_b32 v228, v204\n"
"v_mov_b32 v229, 0\n"
"v_mov_b32 v64, 0\n"
"v_mov_b32 v65, 0\n"
"v_mov_b32 v66, 0\n"
"v_mov_b32 v67, 0\n"
"v_mov_b32 v68, 0\n"
"v_mov_b32 v69, 0\n"
"v_mov_b32 v70, 0\n"
"v_mov_b32 v71, 0\n"
"v_mov_b32 v72, 0\n"
"v_mov_b32 v73, 0\n"
"v_mov_b32 v74, 0\n"
"v_mov_b32 v75, 0\n"
"v_mov_b32 v76, 0\n"
"v_mov_b32 v77, 0\n"
"v_mov_b32 v78, 0\n"
"v_mov_b32 v79, 0\n"
"v_mov_b32 v80, 0\n"
"v_mov_b32 v81, 0\n"
"v_mov_b32 v82, 0\n"
"v_mov_b32 v83, 0\n"
"v_mov_b32 v84, 0\n"
"v_mov_b32 v85, 0\n"
"v_mov_b32 v86, 0\n"
"v_mov_b32 v87, 0\n"
"v_mov_b32 v88, 0\n"
"v_mov_b32 v89, 0\n"
"v_mov_b32 v90, 0\n"
"v_mov_b32 v91, 0\n"
"v_mov_b32 v92, 0\n"
"v_mov_b32 v93, 0\n"
"v_mov_b32 v94, 0\n"
"v_mov_b32 v95, 0\n"
"v_mov_b32 v96, 0\n"
"v_mov_b32 v97, 0\n"
"v_mov_b32 v98, 0\n"
"v_mov_b32 v99, 0\n"
"v_mov_b32 v100, 0\n"
"v_mov_b32 v101, 0\n"
"v_mov_b32 v102, 0\n"
"v_mov_b32 v103, 0\n"
"v_mov_b32 v104, 0\n"
"v_mov_b32 v105, 0\n"
"v_mov_b32 v106, 0\n"
"v_mov_b32 v107, 0\n"
"v_mov_b32 v108, 0\n"
"v_mov_b32 v109, 0\n"
"v_mov_b32 v110, 0\n"
"v_mov_b32 v111, 0\n"
"v_mov_b32 v112, 0\n"
"v_mov_b32 v113, 0\n"
"v_mov_b32 v114, 0\n"
"v_mov_b32 v115, 0\n"
"v_mov_b32 v116, 0\n"
"v_mov_b32 v117, 0\n"
"v_mov_b32 v118, 0\n"
"v_mov_b32 v119, 0\n"
"v_mov_b32 v120, 0\n"
"v_mov_b32 v121, 0\n"
"v_mov_b32 v122, 0\n"
"v_mov_b32 v123, 0\n"
"v_mov_b32 v124, 0\n"
"v_mov_b32 v125, 0\n"
"v_mov_b32 v126, 0\n"
"v_mov_b32 v127, 0\n"
"v_mov_b32 v128, 0\n"
"v_mov_b32 v129, 0\n"
"v_mov_b32 v130, 0\n"
"v_mov_b32 v131, 0\n"
"v_mov_b32 v132, 0\n"
"v_mov_b32 v133, 0\n"
"v_mov_b32 v134, 0\n"
"v_mov_b32 v135, 0\n"
"v_mov_b32 v136, 0\n"
"v_mov_b32 v137, 0\n"
"v_mov_b32 v138, 0\n"
"v_mov_b32 v139, 0\n"
"v_mov_b32 v140, 0\n"
"v_mov_b32 v141, 0\n"
"v_mov_b32 v142, 0\n"
"v_mov_b32 v143, 0\n"
"v_mov_b32 v144, 0\n"
"v_mov_b32 v145, 0\n"
"v_mov_b32 v146, 0\n"
"v_mov_b32 v147, 0\n"
"v_mov_b32 v148, 0\n"
"v_mov_b32 v149, 0\n"
"v_mov_b32 v150, 0\n"
"v_mov_b32 v151, 0\n"
"v_mov_b32 v152, 0\n"
"v_mov_b32 v153, 0\n"
"v_mov_b32 v154, 0\n"
"v_mov_b32 v155, 0\n"
"v_mov_b32 v156, 0\n"
"v_mov_b32 v157, 0\n"
"v_mov_b32 v158, 0\n"
"v_mov_b32 v159, 0\n"
"v_mov_b32 v160, 0\n"
"v_mov_b32 v161, 0\n"
"v_mov_b32 v162, 0\n"
"v_mov_b32 v163, 0\n"
"v_mov_b32 v164, 0\n"
"v_mov_b32 v165, 0\n"
"v_mov_b32 v166, 0\n"
"v_mov_b32 v167, 0\n"
"v_mov_b32 v168, 0\n"
"v_mov_b32 v169, 0\n"
"v_mov_b32 v170, 0\n"
"v_mov_b32 v171, 0\n"
"v_mov_b32 v172, 0\n"
"v_mov_b32 v173, 0\n"
"v_mov_b32 v174, 0\n"
"v_mov_b32 v175, 0\n"
"v_mov_b32 v176, 0\n"
"v_mov_b32 v177, 0\n"
"v_mov_b32 v178, 0\n"
"v_mov_b32 v179, 0\n"
"v_mov_b32 v180, 0\n"
"v_mov_b32 v181, 0\n"
"v_mov_b32 v182, 0\n"
"v_mov_b32 v183, 0\n"
"v_mov_b32 v184, 0\n"
"v_mov_b32 v185, 0\n"
"v_mov_b32 v186, 0\n"
"v_mov_b32 v187, 0\n"
"v_mov_b32 v188, 0\n"
"v_mov_b32 v189, 0\n"
"v_mov_b32 v190, 0\n"
"v_mov_b32 v191, 0\n"
"s_mov_b32 s16, 0\n"
"s_mov_b32 s17, 8192\n"
"s_lshl_b32 s18, s68, 15\n"
"s_add_u32 s18, s18, 32768\n"
"Lsl_%=:\n"
"buffer_load_dwordx4 a[0:3], v232, s[44:47], s16 offen offset:0\n"
"buffer_load_dwordx4 a[4:7], v232, s[44:47], s16 offen offset:1024\n"
"buffer_load_dwordx4 a[8:11], v232, s[44:47], s16 offen offset:2048\n"
"buffer_load_dwordx4 a[12:15], v232, s[44:47], s16 offen offset:3072\n"
"buffer_load_dwordx4 a[16:19], v233, s[44:47], s16 offen offset:0\n"
"buffer_load_dwordx4 a[20:23], v233, s[44:47], s16 offen offset:1024\n"
"buffer_load_dwordx4 a[24:27], v233, s[44:47], s16 offen offset:2048\n"
"buffer_load_dwordx4 a[28:31], v233, s[44:47], s16 offen offset:3072\n"
"buffer_load_dwordx4 a[32:35], v232, s[44:47], s17 offen offset:0\n"
"buffer_load_dwordx4 a[36:39], v232, s[44:47], s17 offen offset:1024\n"
"buffer_load_dwordx4 a[40:43], v232, s[44:47], s17 offen offset:2048\n"
"buffer_load_dwordx4 a[44:47], v232, s[44:47], s17 offen offset:3072\n"
"buffer_load_dwordx4 a[48:51], v233, s[44:47], s17 offen offset:0\n"
"buffer_load_dwordx4 a[52:55], v233, s[44:47], s17 offen offset:1024\n"
"buffer_load_dwordx4 a[56:59], v233, s[44:47], s17 offen offset:2048\n"
"buffer_load_dwordx4 a[60:63], v233, s[44:47], s17 offen offset:3072\n"
"s_waitcnt vmcnt(12)\n"
"v_mfma_f32_16x16x32_bf16 v[192:195], a[0:3], v[0:3], 0\n"
"v_mfma_f32_16x16x32_bf16 v[192:195], a[4:7], v[4:7], v[192:195]\n"
"v_mfma_f32_16x16x32_bf16 v[192:195], a[8:11], v[8:11], v[192:195]\n"
"v_mfma_f32_16x16x32_bf16 v[192:195], a[12:15], v[12:15], v[192:195]\n"
"s_waitcnt vmcnt(8)\n"
"v_mfma_f32_16x16x32_bf16 v[196:199], a[16:19], v[0:3], 0\n"
"v_mfma_f32_16x16x32_bf16 v[196:199], a[20:23], v[4:7], v[196:199]\n"
"v_mfma_f32_16x16x32_bf16 v[196:199], a[24:27], v[8:11], v[196:199]\n"
"v_mfma_f32_16x16x32_bf16 v[196:199], a[28:31], v[12:15], v[196:199]\n"
"v_cmp_gt_i32 vcc, 0, v218\n"
"s_nop 1\n"
"v_cndmask_b32 v192, v192, v204, vcc\n"
"v_cmp_gt_i32 vcc, 1, v218\n"
"s_nop 1\n"
"v_cndmask_b32 v193, v193, v204, vcc\n"
"v_cmp_gt_i32 vcc, 2, v218\n"
"s_nop 1\n"
"v_cndmask_b32 v194, v194, v204, vcc\n"
"v_cmp_gt_i32 vcc, 3, v218\n"
"s_nop 1\n"
"v_cndmask_b32 v195, v195, v204, vcc\n"
"v_cmp_gt_i32 vcc, 16, v218\n"
"s_nop 1\n"
"v_cndmask_b32 v196, v196, v204, vcc\n"
"v_cmp_gt_i32 vcc, 17, v218\n"
"s_nop 1\n"
"v_cndmask_b32 v197, v197, v204, vcc\n"
"v_cmp_gt_i32 vcc, 18, v218\n"
"s_nop 1\n"
"v_cndmask_b32 v198, v198, v204, vcc\n"
"v_cmp_gt_i32 vcc, 19, v218\n"
"s_nop 1\n"
"v_cndmask_b32 v199, v199, v204, vcc\n"
"v_max3_f32 v200, v192, v193, v216\n"
"v_max3_f32 v200, v200, v194, v195\n"
"v_max3_f32 v200, v200, v196, v197\n"
"v_max3_f32 v200, v200, v198, v199\n"
"v_mov_b32 v201, v200\n"
"s_nop 1\n"
"v_permlane16_swap_b32 v200, v201\n"
"v_max_f32 v200, v200, v201\n"
"v_mov_b32 v201, v200\n"
"s_nop 1\n"
"v_permlane32_swap_b32 v200, v201\n"
"v_max_f32 v200, v200, v201\n"
"v_sub_f32 v202, v216, v200\n"
"v_exp_f32 v202, v202\n"
"v_mov_b32 v216, v200\n"
"v_subrev_u32 v218, 32, v218\n"
"v_sub_f32 v192, v192, v200\n"
"v_sub_f32 v193, v193, v200\n"
"v_sub_f32 v194, v194, v200\n"
"v_sub_f32 v195, v195, v200\n"
"v_sub_f32 v196, v196, v200\n"
"v_sub_f32 v197, v197, v200\n"
"v_sub_f32 v198, v198, v200\n"
"v_sub_f32 v199, v199, v200\n"
"v_exp_f32 v192, v192\n"
"v_exp_f32 v193, v193\n"
"v_exp_f32 v194, v194\n"
"v_exp_f32 v195, v195\n"
"v_exp_f32 v196, v196\n"
"v_exp_f32 v197, v197\n"
"v_exp_f32 v198, v198\n"
"v_exp_f32 v199, v199\n"
"v_mul_f32 v217, v217, v202\n"
"v_pk_mul_f32 v[64:65], v[64:65], v[202:203] op_sel_hi:[1,0]\n"
"v_pk_mul_f32 v[66:67], v[66:67], v[202:203] op_sel_hi:[1,0]\n"
"v_pk_mul_f32 v[68:69], v[68:69], v[202:203] op_sel_hi:[1,0]\n"
"v_pk_mul_f32 v[70:71], v[70:71], v[202:203] op_sel_hi:[1,0]\n"
"v_pk_mul_f32 v[72:73], v[72:73], v[202:203] op_sel_hi:[1,0]\n"
"v_pk_mul_f32 v[74:75], v[74:75], v[202:203] op_sel_hi:[1,0]\n"
"v_pk_mul_f32 v[76:77], v[76:77], v[202:203] op_sel_hi:[1,0]\n"
"v_pk_mul_f32 v[78:79], v[78:79], v[202:203] op_sel_hi:[1,0]\n"
"v_pk_mul_f32 v[80:81], v[80:81], v[202:203] op_sel_hi:[1,0]\n"
"v_pk_mul_f32 v[82:83], v[82:83], v[202:203] op_sel_hi:[1,0]\n"
"v_pk_mul_f32 v[84:85], v[84:85], v[202:203] op_sel_hi:[1,0]\n"
"v_pk_mul_f32 v[86:87], v[86:87], v[202:203] op_sel_hi:[1,0]\n"
"v_pk_mul_f32 v[88:89], v[88:89], v[202:203] op_sel_hi:[1,0]\n"
"v_pk_mul_f32 v[90:91], v[90:91], v[202:203] op_sel_hi:[1,0]\n"
"v_pk_mul_f32 v[92:93], v[92:93], v[202:203] op_sel_hi:[1,0]\n"
"v_pk_mul_f32 v[94:95], v[94:95], v[202:203] op_sel_hi:[1,0]\n"
"v_add_f32 v217, v217, v192\n"
"v_add_f32 v217, v217, v193\n"
"v_add_f32 v217, v217, v194\n"
"v_add_f32 v217, v217, v195\n"
"v_add_f32 v217, v217, v196\n"
"v_add_f32 v217, v217, v197\n"
"v_add_f32 v217, v217, v198\n"
"v_add_f32 v217, v217, v199\n"
"v_cvt_pk_bf16_f32 v208, v192, v193\n"
"v_cvt_pk_bf16_f32 v209, v194, v195\n"
"v_cvt_pk_bf16_f32 v210, v196, v197\n"
"v_cvt_pk_bf16_f32 v211, v198, v199\n"
"s_waitcnt vmcnt(7)\n"
"s_nop 0\n"
"v_mfma_f32_16x16x32_bf16 v[64:67], a[32:35], v[208:211], v[64:67]\n"
"s_waitcnt vmcnt(6)\n"
"v_mfma_f32_16x16x32_bf16 v[68:71], a[36:39], v[208:211], v[68:71]\n"
"s_waitcnt vmcnt(5)\n"
"v_mfma_f32_16x16x32_bf16 v[72:75], a[40:43], v[208:211], v[72:75]\n"
"s_waitcnt vmcnt(4)\n"
"v_mfma_f32_16x16x32_bf16 v[76:79], a[44:47], v[208:211], v[76:79]\n"
"s_waitcnt vmcnt(3)\n"
"v_mfma_f32_16x16x32_bf16 v[80:83], a[48:51], v[208:211], v[80:83]\n"
"s_waitcnt vmcnt(2)\n"
"v_mfma_f32_16x16x32_bf16 v[84:87], a[52:55], v[208:211], v[84:87]\n"
"s_waitcnt vmcnt(1)\n"
"v_mfma_f32_16x16x32_bf16 v[88:91], a[56:59], v[208:211], v[88:91]\n"
"s_waitcnt vmcnt(0)\n"
"v_mfma_f32_16x16x32_bf16 v[92:95], a[60:63], v[208:211], v[92:95]\n"
"v_mfma_f32_16x16x32_bf16 v[192:195], a[0:3], v[16:19], 0\n"
"v_mfma_f32_16x16x32_bf16 v[192:195], a[4:7], v[20:23], v[192:195]\n"
"v_mfma_f32_16x16x32_bf16 v[192:195], a[8:11], v[24:27], v[192:195]\n"
"v_mfma_f32_16x16x32_bf16 v[192:195], a[12:15], v[28:31], v[192:195]\n"
"v_mfma_f32_16x16x32_bf16 v[196:199], a[16:19], v[16:19], 0\n"
"v_mfma_f32_16x16x32_bf16 v[196:199], a[20:23], v[20:23], v[196:199]\n"
"v_mfma_f32_16x16x32_bf16 v[196:199], a[24:27], v[24:27], v[196:199]\n"
"v_mfma_f32_16x16x32_bf16 v[196:199], a[28:31], v[28:31], v[196:199]\n"
"v_cmp_gt_i32 vcc, 0, v222\n"
"s_nop 2\n"
"v_cndmask_b32 v192, v192, v204, vcc\n"
"v_cmp_gt_i32 vcc, 1, v222\n"
"s_nop 1\n"
"v_cndmask_b32 v193, v193, v204, vcc\n"
"v_cmp_gt_i32 vcc, 2, v222\n"
"s_nop 1\n"
"v_cndmask_b32 v194, v194, v204, vcc\n"
"v_cmp_gt_i32 vcc, 3, v222\n"
"s_nop 1\n"
"v_cndmask_b32 v195, v195, v204, vcc\n"
"v_cmp_gt_i32 vcc, 16, v222\n"
"s_nop 1\n"
"v_cndmask_b32 v196, v196, v204, vcc\n"
"v_cmp_gt_i32 vcc, 17, v222\n"
"s_nop 1\n"
"v_cndmask_b32 v197, v197, v204, vcc\n"
"v_cmp_gt_i32 vcc, 18, v222\n"
"s_nop 1\n"
"v_cndmask_b32 v198, v198, v204, vcc\n"
"v_cmp_gt_i32 vcc, 19, v222\n"
"s_nop 1\n"
"v_cndmask_b32 v199, v199, v204, vcc\n"
"v_max3_f32 v200, v192, v193, v220\n"
"v_max3_f32 v200, v200, v194, v195\n"
"v_max3_f32 v200, v200, v196, v197\n"
"v_max3_f32 v200, v200, v198, v199\n"
"v_mov_b32 v201, v200\n"
"s_nop 1\n"
"v_permlane16_swap_b32 v200, v201\n"
"v_max_f32 v200, v200, v201\n"
"v_mov_b32 v201, v200\n"
"s_nop 1\n"
"v_permlane32_swap_b32 v200, v201\n"
"v_max_f32 v200, v200, v201\n"
"v_sub_f32 v202, v220, v200\n"
"v_exp_f32 v202, v202\n"
"v_mov_b32 v220, v200\n"
"v_subrev_u32 v222, 32, v222\n"
"v_sub_f32 v192, v192, v200\n"
"v_sub_f32 v193, v193, v200\n"
"v_sub_f32 v194, v194, v200\n"
"v_sub_f32 v195, v195, v200\n"
"v_sub_f32 v196, v196, v200\n"
"v_sub_f32 v197, v197, v200\n"
"v_sub_f32 v198, v198, v200\n"
"v_sub_f32 v199, v199, v200\n"
"v_exp_f32 v192, v192\n"
"v_exp_f32 v193, v193\n"
"v_exp_f32 v194, v194\n"
"v_exp_f32 v195, v195\n"
"v_exp_f32 v196, v196\n"
"v_exp_f32 v197, v197\n"
"v_exp_f32 v198, v198\n"
"v_exp_f32 v199, v199\n"
"v_mul_f32 v221, v221, v202\n"
"v_pk_mul_f32 v[96:97], v[96:97], v[202:203] op_sel_hi:[1,0]\n"
"v_pk_mul_f32 v[98:99], v[98:99], v[202:203] op_sel_hi:[1,0]\n"
"v_pk_mul_f32 v[100:101], v[100:101], v[202:203] op_sel_hi:[1,0]\n"
"v_pk_mul_f32 v[102:103], v[102:103], v[202:203] op_sel_hi:[1,0]\n"
"v_pk_mul_f32 v[104:105], v[104:105], v[202:203] op_sel_hi:[1,0]\n"
"v_pk_mul_f32 v[106:107], v[106:107], v[202:203] op_sel_hi:[1,0]\n"
"v_pk_mul_f32 v[108:109], v[108:109], v[202:203] op_sel_hi:[1,0]\n"
"v_pk_mul_f32 v[110:111], v[110:111], v[202:203] op_sel_hi:[1,0]\n"
"v_pk_mul_f32 v[112:113], v[112:113], v[202:203] op_sel_hi:[1,0]\n"
"v_pk_mul_f32 v[114:115], v[114:115], v[202:203] op_sel_hi:[1,0]\n"
"v_pk_mul_f32 v[116:117], v[116:117], v[202:203] op_sel_hi:[1,0]\n"
"v_pk_mul_f32 v[118:119], v[118:119], v[202:203] op_sel_hi:[1,0]\n"
"v_pk_mul_f32 v[120:121], v[120:121], v[202:203] op_sel_hi:[1,0]\n"
"v_pk_mul_f32 v[122:123], v[122:123], v[202:203] op_sel_hi:[1,0]\n"
"v_pk_mul_f32 v[124:125], v[124:125], v[202:203] op_sel_hi:[1,0]\n"
"v_pk_mul_f32 v[126:127], v[126:127], v[202:203] op_sel_hi:[1,0]\n"
"v_add_f32 v221, v221, v192\n"
"v_add_f32 v221, v221, v193\n"
"v_add_f32 v221, v221, v194\n"
"v_add_f32 v221, v221, v195\n"
"v_add_f32 v221, v221, v196\n"
"v_add_f32 v221, v221, v197\n"
"v_add_f32 v221, v221, v198\n"
"v_add_f32 v221, v221, v199\n"
"v_cvt_pk_bf16_f32 v208, v192, v193\n"
"v_cvt_pk_bf16_f32 v209, v194, v195\n"
"v_cvt_pk_bf16_f32 v210, v196, v197\n"
"v_cvt_pk_bf16_f32 v211, v198, v199\n"
"s_nop 1\n"
"v_mfma_f32_16x16x32_bf16 v[96:99], a[32:35], v[208:211], v[96:99]\n"
"v_mfma_f32_16x16x32_bf16 v[100:103], a[36:39], v[208:211], v[100:103]\n"
"v_mfma_f32_16x16x32_bf16 v[104:107], a[40:43], v[208:211], v[104:107]\n"
"v_mfma_f32_16x16x32_bf16 v[108:111], a[44:47], v[208:211], v[108:111]\n"
"v_mfma_f32_16x16x32_bf16 v[112:115], a[48:51], v[208:211], v[112:115]\n"
"v_mfma_f32_16x16x32_bf16 v[116:119], a[52:55], v[208:211], v[116:119]\n"
"v_mfma_f32_16x16x32_bf16 v[120:123], a[56:59], v[208:211], v[120:123]\n"
"v_mfma_f32_16x16x32_bf16 v[124:127], a[60:63], v[208:211], v[124:127]\n"
"v_mfma_f32_16x16x32_bf16 v[192:195], a[0:3], v[32:35], 0\n"
"v_mfma_f32_16x16x32_bf16 v[192:195], a[4:7], v[36:39], v[192:195]\n"
"v_mfma_f32_16x16x32_bf16 v[192:195], a[8:11], v[40:43], v[192:195]\n"
"v_mfma_f32_16x16x32_bf16 v[192:195], a[12:15], v[44:47], v[192:195]\n"
"v_mfma_f32_16x16x32_bf16 v[196:199], a[16:19], v[32:35], 0\n"
"v_mfma_f32_16x16x32_bf16 v[196:199], a[20:23], v[36:39], v[196:199]\n"
"v_mfma_f32_16x16x32_bf16 v[196:199], a[24:27], v[40:43], v[196:199]\n"
"v_mfma_f32_16x16x32_bf16 v[196:199], a[28:31], v[44:47], v[196:199]\n"
"v_cmp_gt_i32 vcc, 0, v226\n"
"s_nop 2\n"
"v_cndmask_b32 v192, v192, v204, vcc\n"
"v_cmp_gt_i32 vcc, 1, v226\n"
"s_nop 1\n"
"v_cndmask_b32 v193, v193, v204, vcc\n"
"v_cmp_gt_i32 vcc, 2, v226\n"
"s_nop 1\n"
"v_cndmask_b32 v194, v194, v204, vcc\n"
"v_cmp_gt_i32 vcc, 3, v226\n"
"s_nop 1\n"
"v_cndmask_b32 v195, v195, v204, vcc\n"
"v_cmp_gt_i32 vcc, 16, v226\n"
"s_nop 1\n"
"v_cndmask_b32 v196, v196, v204, vcc\n"
"v_cmp_gt_i32 vcc, 17, v226\n"
"s_nop 1\n"
"v_cndmask_b32 v197, v197, v204, vcc\n"
"v_cmp_gt_i32 vcc, 18, v226\n"
"s_nop 1\n"
"v_cndmask_b32 v198, v198, v204, vcc\n"
"v_cmp_gt_i32 vcc, 19, v226\n"
"s_nop 1\n"
"v_cndmask_b32 v199, v199, v204, vcc\n"
"v_max3_f32 v200, v192, v193, v224\n"
"v_max3_f32 v200, v200, v194, v195\n"
"v_max3_f32 v200, v200, v196, v197\n"
"v_max3_f32 v200, v200, v198, v199\n"
"v_mov_b32 v201, v200\n"
"s_nop 1\n"
"v_permlane16_swap_b32 v200, v201\n"
"v_max_f32 v200, v200, v201\n"
"v_mov_b32 v201, v200\n"
"s_nop 1\n"
"v_permlane32_swap_b32 v200, v201\n"
"v_max_f32 v200, v200, v201\n"
"v_sub_f32 v202, v224, v200\n"
"v_exp_f32 v202, v202\n"
"v_mov_b32 v224, v200\n"
"v_subrev_u32 v226, 32, v226\n"
"v_sub_f32 v192, v192, v200\n"
"v_sub_f32 v193, v193, v200\n"
"v_sub_f32 v194, v194, v200\n"
"v_sub_f32 v195, v195, v200\n"
"v_sub_f32 v196, v196, v200\n"
"v_sub_f32 v197, v197, v200\n"
"v_sub_f32 v198, v198, v200\n"
"v_sub_f32 v199, v199, v200\n"
"v_exp_f32 v192, v192\n"
"v_exp_f32 v193, v193\n"
"v_exp_f32 v194, v194\n"
"v_exp_f32 v195, v195\n"
"v_exp_f32 v196, v196\n"
"v_exp_f32 v197, v197\n"
"v_exp_f32 v198, v198\n"
"v_exp_f32 v199, v199\n"
"v_mul_f32 v225, v225, v202\n"
"v_pk_mul_f32 v[128:129], v[128:129], v[202:203] op_sel_hi:[1,0]\n"
"v_pk_mul_f32 v[130:131], v[130:131], v[202:203] op_sel_hi:[1,0]\n"
"v_pk_mul_f32 v[132:133], v[132:133], v[202:203] op_sel_hi:[1,0]\n"
"v_pk_mul_f32 v[134:135], v[134:135], v[202:203] op_sel_hi:[1,0]\n"
"v_pk_mul_f32 v[136:137], v[136:137], v[202:203] op_sel_hi:[1,0]\n"
"v_pk_mul_f32 v[138:139], v[138:139], v[202:203] op_sel_hi:[1,0]\n"
"v_pk_mul_f32 v[140:141], v[140:141], v[202:203] op_sel_hi:[1,0]\n"
"v_pk_mul_f32 v[142:143], v[142:143], v[202:203] op_sel_hi:[1,0]\n"
"v_pk_mul_f32 v[144:145], v[144:145], v[202:203] op_sel_hi:[1,0]\n"
"v_pk_mul_f32 v[146:147], v[146:147], v[202:203] op_sel_hi:[1,0]\n"
"v_pk_mul_f32 v[148:149], v[148:149], v[202:203] op_sel_hi:[1,0]\n"
"v_pk_mul_f32 v[150:151], v[150:151], v[202:203] op_sel_hi:[1,0]\n"
"v_pk_mul_f32 v[152:153], v[152:153], v[202:203] op_sel_hi:[1,0]\n"
"v_pk_mul_f32 v[154:155], v[154:155], v[202:203] op_sel_hi:[1,0]\n"
"v_pk_mul_f32 v[156:157], v[156:157], v[202:203] op_sel_hi:[1,0]\n"
"v_pk_mul_f32 v[158:159], v[158:159], v[202:203] op_sel_hi:[1,0]\n"
"v_add_f32 v225, v225, v192\n"
"v_add_f32 v225, v225, v193\n"
"v_add_f32 v225, v225, v194\n"
"v_add_f32 v225, v225, v195\n"
"v_add_f32 v225, v225, v196\n"
"v_add_f32 v225, v225, v197\n"
"v_add_f32 v225, v225, v198\n"
"v_add_f32 v225, v225, v199\n"
"v_cvt_pk_bf16_f32 v208, v192, v193\n"
"v_cvt_pk_bf16_f32 v209, v194, v195\n"
"v_cvt_pk_bf16_f32 v210, v196, v197\n"
"v_cvt_pk_bf16_f32 v211, v198, v199\n"
"s_nop 1\n"
"v_mfma_f32_16x16x32_bf16 v[128:131], a[32:35], v[208:211], v[128:131]\n"
"v_mfma_f32_16x16x32_bf16 v[132:135], a[36:39], v[208:211], v[132:135]\n"
"v_mfma_f32_16x16x32_bf16 v[136:139], a[40:43], v[208:211], v[136:139]\n"
"v_mfma_f32_16x16x32_bf16 v[140:143], a[44:47], v[208:211], v[140:143]\n"
"v_mfma_f32_16x16x32_bf16 v[144:147], a[48:51], v[208:211], v[144:147]\n"
"v_mfma_f32_16x16x32_bf16 v[148:151], a[52:55], v[208:211], v[148:151]\n"
"v_mfma_f32_16x16x32_bf16 v[152:155], a[56:59], v[208:211], v[152:155]\n"
"v_mfma_f32_16x16x32_bf16 v[156:159], a[60:63], v[208:211], v[156:159]\n"
"v_mfma_f32_16x16x32_bf16 v[192:195], a[0:3], v[48:51], 0\n"
"v_mfma_f32_16x16x32_bf16 v[192:195], a[4:7], v[52:55], v[192:195]\n"
"v_mfma_f32_16x16x32_bf16 v[192:195], a[8:11], v[56:59], v[192:195]\n"
"v_mfma_f32_16x16x32_bf16 v[192:195], a[12:15], v[60:63], v[192:195]\n"
"v_mfma_f32_16x16x32_bf16 v[196:199], a[16:19], v[48:51], 0\n"
"v_mfma_f32_16x16x32_bf16 v[196:199], a[20:23], v[52:55], v[196:199]\n"
"v_mfma_f32_16x16x32_bf16 v[196:199], a[24:27], v[56:59], v[196:199]\n"
"v_mfma_f32_16x16x32_bf16 v[196:199], a[28:31], v[60:63], v[196:199]\n"
"v_cmp_gt_i32 vcc, 0, v230\n"
"s_nop 2\n"
"v_cndmask_b32 v192, v192, v204, vcc\n"
"v_cmp_gt_i32 vcc, 1, v230\n"
"s_nop 1\n"
"v_cndmask_b32 v193, v193, v204, vcc\n"
"v_cmp_gt_i32 vcc, 2, v230\n"
"s_nop 1\n"
"v_cndmask_b32 v194, v194, v204, vcc\n"
"v_cmp_gt_i32 vcc, 3, v230\n"
"s_nop 1\n"
"v_cndmask_b32 v195, v195, v204, vcc\n"
"v_cmp_gt_i32 vcc, 16, v230\n"
"s_nop 1\n"
"v_cndmask_b32 v196, v196, v204, vcc\n"
"v_cmp_gt_i32 vcc, 17, v230\n"
"s_nop 1\n"
"v_cndmask_b32 v197, v197, v204, vcc\n"
"v_cmp_gt_i32 vcc, 18, v230\n"
"s_nop 1\n"
"v_cndmask_b32 v198, v198, v204, vcc\n"
"v_cmp_gt_i32 vcc, 19, v230\n"
"s_nop 1\n"
"v_cndmask_b32 v199, v199, v204, vcc\n"
"v_max3_f32 v200, v192, v193, v228\n"
"v_max3_f32 v200, v200, v194, v195\n"
"v_max3_f32 v200, v200, v196, v197\n"
"v_max3_f32 v200, v200, v198, v199\n"
"v_mov_b32 v201, v200\n"
"s_nop 1\n"
"v_permlane16_swap_b32 v200, v201\n"
"v_max_f32 v200, v200, v201\n"
"v_mov_b32 v201, v200\n"
"s_nop 1\n"
"v_permlane32_swap_b32 v200, v201\n"
"v_max_f32 v200, v200, v201\n"
"v_sub_f32 v202, v228, v200\n"
"v_exp_f32 v202, v202\n"
"v_mov_b32 v228, v200\n"
"v_subrev_u32 v230, 32, v230\n"
"v_sub_f32 v192, v192, v200\n"
"v_sub_f32 v193, v193, v200\n"
"v_sub_f32 v194, v194, v200\n"
"v_sub_f32 v195, v195, v200\n"
"v_sub_f32 v196, v196, v200\n"
"v_sub_f32 v197, v197, v200\n"
"v_sub_f32 v198, v198, v200\n"
"v_sub_f32 v199, v199, v200\n"
"v_exp_f32 v192, v192\n"
"v_exp_f32 v193, v193\n"
"v_exp_f32 v194, v194\n"
"v_exp_f32 v195, v195\n"
"v_exp_f32 v196, v196\n"
"v_exp_f32 v197, v197\n"
"v_exp_f32 v198, v198\n"
"v_exp_f32 v199, v199\n"
"v_mul_f32 v229, v229, v202\n"
"v_pk_mul_f32 v[160:161], v[160:161], v[202:203] op_sel_hi:[1,0]\n"
"v_pk_mul_f32 v[162:163], v[162:163], v[202:203] op_sel_hi:[1,0]\n"
"v_pk_mul_f32 v[164:165], v[164:165], v[202:203] op_sel_hi:[1,0]\n"
"v_pk_mul_f32 v[166:167], v[166:167], v[202:203] op_sel_hi:[1,0]\n"
"v_pk_mul_f32 v[168:169], v[168:169], v[202:203] op_sel_hi:[1,0]\n"
"v_pk_mul_f32 v[170:171], v[170:171], v[202:203] op_sel_hi:[1,0]\n"
"v_pk_mul_f32 v[172:173], v[172:173], v[202:203] op_sel_hi:[1,0]\n"
"v_pk_mul_f32 v[174:175], v[174:175], v[202:203] op_sel_hi:[1,0]\n"
"v_pk_mul_f32 v[176:177], v[176:177], v[202:203] op_sel_hi:[1,0]\n"
"v_pk_mul_f32 v[178:179], v[178:179], v[202:203] op_sel_hi:[1,0]\n"
"v_pk_mul_f32 v[180:181], v[180:181], v[202:203] op_sel_hi:[1,0]\n"
"v_pk_mul_f32 v[182:183], v[182:183], v[202:203] op_sel_hi:[1,0]\n"
"v_pk_mul_f32 v[184:185], v[184:185], v[202:203] op_sel_hi:[1,0]\n"
"v_pk_mul_f32 v[186:187], v[186:187], v[202:203] op_sel_hi:[1,0]\n"
"v_pk_mul_f32 v[188:189], v[188:189], v[202:203] op_sel_hi:[1,0]\n"
"v_pk_mul_f32 v[190:191], v[190:191], v[202:203] op_sel_hi:[1,0]\n"
"v_add_f32 v229, v229, v192\n"
"v_add_f32 v229, v229, v193\n"
"v_add_f32 v229, v229, v194\n"
"v_add_f32 v229, v229, v195\n"
"v_add_f32 v229, v229, v196\n"
"v_add_f32 v229, v229, v197\n"
"v_add_f32 v229, v229, v198\n"
"v_add_f32 v229, v229, v199\n"
"v_cvt_pk_bf16_f32 v208, v192, v193\n"
"v_cvt_pk_bf16_f32 v209, v194, v195\n"
"v_cvt_pk_bf16_f32 v210, v196, v197\n"
"v_cvt_pk_bf16_f32 v211, v198, v199\n"
"s_nop 1\n"
"v_mfma_f32_16x16x32_bf16 v[160:163], a[32:35], v[208:211], v[160:163]\n"
"v_mfma_f32_16x16x32_bf16 v[164:167], a[36:39], v[208:211], v[164:167]\n"
"v_mfma_f32_16x16x32_bf16 v[168:171], a[40:43], v[208:211], v[168:171]\n"
"v_mfma_f32_16x16x32_bf16 v[172:175], a[44:47], v[208:211], v[172:175]\n"
"v_mfma_f32_16x16x32_bf16 v[176:179], a[48:51], v[208:211], v[176:179]\n"
"v_mfma_f32_16x16x32_bf16 v[180:183], a[52:55], v[208:211], v[180:183]\n"
"v_mfma_f32_16x16x32_bf16 v[184:187], a[56:59], v[208:211], v[184:187]\n"
"v_mfma_f32_16x16x32_bf16 v[188:191], a[60:63], v[208:211], v[188:191]\n"
"s_add_u32 s16, s16, 16384\n"
"s_add_u32 s17, s17, 16384\n"
"s_cmp_lt_u32 s16, s18\n"
"s_cbranch_scc1 Lsl_%=\n"
"v_mov_b32 v201, v217\n"
"s_nop 1\n"
"v_permlane16_swap_b32 v217, v201\n"
"v_add_f32 v217, v217, v201\n"
"v_mov_b32 v201, v217\n"
"s_nop 1\n"
"v_permlane32_swap_b32 v217, v201\n"
"v_add_f32 v217, v217, v201\n"
"v_mov_b32 v244, v217\n"
"v_mov_b32 v201, v221\n"
"s_nop 1\n"
"v_permlane16_swap_b32 v221, v201\n"
"v_add_f32 v221, v221, v201\n"
"v_mov_b32 v201, v221\n"
"s_nop 1\n"
"v_permlane32_swap_b32 v221, v201\n"
"v_add_f32 v221, v221, v201\n"
"v_mov_b32 v246, v221\n"
"v_mov_b32 v201, v225\n"
"s_nop 1\n"
"v_permlane16_swap_b32 v225, v201\n"
"v_add_f32 v225, v225, v201\n"
"v_mov_b32 v201, v225\n"
"s_nop 1\n"
"v_permlane32_swap_b32 v225, v201\n"
"v_add_f32 v225, v225, v201\n"
"v_mov_b32 v248, v225\n"
"v_mov_b32 v201, v229\n"
"s_nop 1\n"
"v_permlane16_swap_b32 v229, v201\n"
"v_add_f32 v229, v229, v201\n"
"v_mov_b32 v201, v229\n"
"s_nop 1\n"
"v_permlane32_swap_b32 v229, v201\n"
"v_add_f32 v229, v229, v201\n"
"v_mov_b32 v250, v229\n"
"s_add_u32 s17, s71, 8192\n"
"buffer_load_dwordx4 a[0:3], v232, s[44:47], s71 offen offset:0\n"
"buffer_load_dwordx4 a[4:7], v232, s[44:47], s71 offen offset:1024\n"
"buffer_load_dwordx4 a[8:11], v232, s[44:47], s71 offen offset:2048\n"
"buffer_load_dwordx4 a[12:15], v232, s[44:47], s71 offen offset:3072\n"
"buffer_load_dwordx4 a[16:19], v233, s[44:47], s71 offen offset:0\n"
"buffer_load_dwordx4 a[20:23], v233, s[44:47], s71 offen offset:1024\n"
"buffer_load_dwordx4 a[24:27], v233, s[44:47], s71 offen offset:2048\n"
"buffer_load_dwordx4 a[28:31], v233, s[44:47], s71 offen offset:3072\n"
"buffer_load_dwordx4 a[32:35], v232, s[44:47], s17 offen offset:0\n"
"buffer_load_dwordx4 a[36:39], v232, s[44:47], s17 offen offset:1024\n"
"buffer_load_dwordx4 a[40:43], v232, s[44:47], s17 offen offset:2048\n"
"buffer_load_dwordx4 a[44:47], v232, s[44:47], s17 offen offset:3072\n"
"buffer_load_dwordx4 a[48:51], v233, s[44:47], s17 offen offset:0\n"
"buffer_load_dwordx4 a[52:55], v233, s[44:47], s17 offen offset:1024\n"
"buffer_load_dwordx4 a[56:59], v233, s[44:47], s17 offen offset:2048\n"
"buffer_load_dwordx4 a[60:63], v233, s[44:47], s17 offen offset:3072\n"
"s_waitcnt vmcnt(0)\n"
"s_cmp_eq_u32 s70, 0\n"
"s_cbranch_scc1 Lmid_%=\n"
"v_mov_b64 v[64:65], 0\n"
"v_mov_b64 v[66:67], 0\n"
"v_mov_b64 v[68:69], 0\n"
"v_mov_b64 v[70:71], 0\n"
"v_mov_b64 v[72:73], 0\n"
"v_mov_b64 v[74:75], 0\n"
"v_mov_b64 v[76:77], 0\n"
"v_mov_b64 v[78:79], 0\n"
"v_mov_b64 v[80:81], 0\n"
"v_mov_b64 v[82:83], 0\n"
"v_mov_b64 v[84:85], 0\n"
"v_mov_b64 v[86:87], 0\n"
"v_mov_b64 v[88:89], 0\n"
"v_mov_b64 v[90:91], 0\n"
"v_mov_b64 v[92:93], 0\n"
"v_mov_b64 v[94:95], 0\n"
"v_mov_b64 v[96:97], 0\n"
"v_mov_b64 v[98:99], 0\n"
"v_mov_b64 v[100:101], 0\n"
"v_mov_b64 v[102:103], 0\n"
"v_mov_b64 v[104:105], 0\n"
"v_mov_b64 v[106:107], 0\n"
"v_mov_b64 v[108:109], 0\n"
"v_mov_b64 v[110:111], 0\n"
"v_mov_b64 v[112:113], 0\n"
"v_mov_b64 v[114:115], 0\n"
"v_mov_b64 v[116:117], 0\n"
"v_mov_b64 v[118:119], 0\n"
"v_mov_b64 v[120:121], 0\n"
"v_mov_b64 v[122:123], 0\n"
"v_mov_b64 v[124:125], 0\n"
"v_mov_b64 v[126:127], 0\n"
"s_branch Lfin_%=\n"
  :: "s"(__builtin_amdgcn_kernarg_segment_ptr()), "s"(blockIdx.x), "s"(wid), "s"((unsigned)(size_t)lds) : "v0","v1","v2","v3","v4","v5","v6","v7","v8","v9","v10","v11","v12","v13","v14","v15","v16","v17","v18","v19","v20","v21","v22","v23","v24","v25","v26","v27","v28","v29","v30","v31","v32","v33","v34","v35","v36","v37","v38","v39","v40","v41","v42","v43","v44","v45","v46","v47","v48","v49","v50","v51","v52","v53","v54","v55","v56","v57","v58","v59","v60","v61","v62","v63","v64","v65","v66","v67","v68","v69","v70","v71","v72","v73","v74","v75","v76","v77","v78","v79","v80","v81","v82","v83","v84","v85","v86","v87","v88","v89","v90","v91","v92","v93","v94","v95","v96","v97","v98","v99","v100","v101","v102","v103","v104","v105","v106","v107","v108","v109","v110","v111","v112","v113","v114","v115","v116","v117","v118","v119","v120","v121","v122","v123","v124","v125","v126","v127","v128","v129","v130","v131","v132","v133","v134","v135","v136","v137","v138","v139","v140","v141","v142","v143","v144","v145","v146","v147","v148","v149","v150","v151","v152","v153","v154","v155","v156","v157","v158","v159","v160","v161","v162","v163","v164","v165","v166","v167","v168","v169","v170","v171","v172","v173","v174","v175","v176","v177","v178","v179","v180","v181","v182","v183","v184","v185","v186","v187","v188","v189","v190","v191","v192","v193","v194","v195","v196","v197","v198","v199","v200","v201","v202","v203","v204","v205","v206","v207","v208","v209","v210","v211","v212","v213","v214","v215","v216","v217","v218","v219","v220","v221","v222","v223","v224","v225","v226","v227","v228","v229","v230","v231","v232","v233","v234","v235","v236","v237","v238","v239","v240","v241","v242","v243","v244","v245","v246","v247","v248","v249","v250","v251","v252","v253","v254","v255","a0","a1","a2","a3","a4","a5","a6","a7","a8","a9","a10","a11","a12","a13","a14","a15","a16","a17","a18","a19","a20","a21","a22","a23","a24","a25","a26","a27","a28","a29","a30","a31","a32","a33","a34","a35","a36","a37","a38","a39","a40","a41","a42","a43","a44","a45","a46","a47","a48","a49","a50","a51","a52","a53","a54","a55","a56","a57","a58","a59","a60","a61","a62","a63","a64","a65","a66","a67","a68","a69","a70","a71","a72","a73","a74","a75","a76","a77","a78","a79","a80","a81","a82","a83","a84","a85","a86","a87","a88","a89","a90","a91","a92","a93","a94","a95","a96","a97","a98","a99","a100","a101","a102","a103","a104","a105","a106","a107","a108","a109","a110","a111","a112","a113","a114","a115","a116","a117","a118","a119","a120","a121","a122","a123","a124","a125","a126","a127","a128","a129","a130","a131","a132","a133","a134","a135","a136","a137","a138","a139","a140","a141","a142","a143","a144","a145","a146","a147","a148","a149","a150","a151","a152","a153","a154","a155","a156","a157","a158","a159","a160","a161","a162","a163","a164","a165","a166","a167","a168","a169","a170","a171","a172","a173","a174","a175","a176","a177","a178","a179","a180","a181","a182","a183","a184","a185","a186","a187","a188","a189","a190","a191","a192","a193","a194","a195","a196","a197","a198","a199","a200","a201","a202","a203","a204","a205","a206","a207","a208","a209","a210","a211","a212","a213","a214","a215","a216","a217","a218","a219","a220","a221","a222","a223","a224","a225","a226","a227","a228","a229","a230","a231","a232","a233","a234","a235","a236","a237","a238","a239","a240","a241","a242","a243","a244","a245","a246","a247","a248","a249","a250","a251","a252","a253","a254","a255","s6","s7","s8","s9","s10","s11","s12","s13","s14","s15","s16","s17","s18","s19","s20","s21","s22","s23","s24","s25","s26","s27","s28","s29","s30","s31","s34","s35","s36","s37","s38","s39","s40","s41","s42","s43","s44","s45","s46","s47","s48","s49","s50","s51","s52","s53","s54","s55","s56","s57","s58","s59","s60","s61","s62","s63","s64","s65","s66","s67","s68","s69","s70","s71","s72","s73","s74","s75","s76","s77","s78","s79","s80","s81","s82","s83","s84","s85","s86","s87","s88","s89","s90","s91","s92","s93","s94","s95","s96","s97","s98","s99","vcc","scc","memory");
}

extern "C" void kernel_launch(void* const* d_in, const int* in_sizes, int n_in, void* d_out, int out_size, void* d_ws, size_t ws_size, hipStream_t stream){
  k_block<<<256,256,0,stream>>>(d_in[0],d_in[1],d_in[2],d_in[3],d_in[4],d_in[5],d_in[7],d_in[8],d_in[9],d_in[10],d_in[11],d_out,d_ws);
}
```
